```python
import torch

def _softmax(x, axis=-1):
    return torch.softmax(x, dim=axis)
def _gelu(x):
    return torch.nn.functional.gelu(x, approximate="tanh")
def _silu(x):
    return torch.nn.functional.silu(x)
def _sigmoid(x):
    return torch.sigmoid(x)
def _layernorm(x, axis=-1, eps=1e-5):
    m = x.mean(dim=axis, keepdim=True)
    v = x.var(dim=axis, keepdim=True, unbiased=False)
    return (x - m) / torch.sqrt(v + eps)
def _scatter_add(tgt, idx, src, axis=0):
    return tgt.clone().index_add_(axis, idx, src)
def _segment_sum(src, seg, n):
    out = torch.zeros((n,) + src.shape[1:], dtype=src.dtype)
    return out.index_add_(0, seg, src)
def _pad(x, pw):
    flat = tuple(v for pair in reversed(pw) for v in pair)
    return torch.nn.functional.pad(x, flat)

kernel_name = 'standalone__mha_softmax__near_ridge'

def setup_inputs():

    _w = (lambda a: (lambda t: t.to(torch.bfloat16) if t.is_floating_point() else t)(torch.as_tensor(a)))
    B, H, Sq, Sk, D = 8, 32, 1600, 512, 128
    return {
        "q": _w((lambda *s: torch.randn(*s, dtype=torch.bfloat16) * 0.8)(B, H, Sq, D)),
        "k": _w((lambda *s: torch.randn(*s, dtype=torch.bfloat16) * 0.8)(B, H, Sk, D)),
        "v": _w((lambda *s: torch.randn(*s, dtype=torch.bfloat16) * 0.05)(B, H, Sk, D)),
    }

def reference(q, k, v):
    scale = q.shape[-1] ** -0.5
    scores = torch.einsum("bhqd,bhkd->bhqk", q, k) * scale
    p = _softmax(scores, axis=-1)
    return torch.einsum("bhqk,bhkd->bhqd", p, v)
```

```cpp
#include <hip/hip_runtime.h>
#include <hip/hip_bf16.h>

typedef float    f32x4  __attribute__((ext_vector_type(4)));
typedef __bf16   bf16x8 __attribute__((ext_vector_type(8)));
typedef unsigned u32x4  __attribute__((ext_vector_type(4)));
typedef float    f32x2  __attribute__((ext_vector_type(2)));
typedef __bf16   bf16x2 __attribute__((ext_vector_type(2)));

constexpr int SQ = 1600, SK = 512, HD = 128;
constexpr unsigned QHEAD = SQ * HD * 2, KHEAD = SK * HD * 2;

__device__ __forceinline__ unsigned pack2(float a, float b) {
  f32x2 v = {a, b};
  return __builtin_bit_cast(unsigned, __builtin_convertvector(v, bf16x2));
}

__device__ __forceinline__ void exact_rows(const char* __restrict__ q, const char* __restrict__ k, const char* __restrict__ v, char* __restrict__ o, unsigned lane) {
  const unsigned g = lane >> 4, c = lane & 15;
  const float C = 0.12751743082f;
  bf16x8 qf[4];
#pragma unroll
  for (int ks = 0; ks < 4; ++ks) qf[ks] = *(const bf16x8*)(q + c * 256 + 64 * ks + 16 * g);
  float m = -__builtin_inff();
#pragma unroll 1
  for (int kb = 0; kb < SK / 16; ++kb) {
    f32x4 s = {0.f, 0.f, 0.f, 0.f};
#pragma unroll
    for (int ks = 0; ks < 4; ++ks) s = __builtin_amdgcn_mfma_f32_16x16x32_bf16(*(const bf16x8*)(k + (16 * kb + c) * 256 + 64 * ks + 16 * g), qf[ks], s, 0, 0, 0);
    m = fmaxf(fmaxf(m, fmaxf(s[0], s[1])), fmaxf(s[2], s[3]));
  }
  m = fmaxf(m, __shfl_xor(m, 16)); m = fmaxf(m, __shfl_xor(m, 32));
  f32x4 acc[8];
#pragma unroll
  for (int db = 0; db < 8; ++db) acc[db] = f32x4{0.f, 0.f, 0.f, 0.f};
  float l = 0.f;
#pragma unroll 1
  for (int t = 0; t < SK / 32; ++t) {
    f32x4 s[2];
#pragma unroll
    for (int hb = 0; hb < 2; ++hb) {
      s[hb] = f32x4{0.f, 0.f, 0.f, 0.f};
#pragma unroll
      for (int ks = 0; ks < 4; ++ks) s[hb] = __builtin_amdgcn_mfma_f32_16x16x32_bf16(*(const bf16x8*)(k + (32 * t + 16 * hb + c) * 256 + 64 * ks + 16 * g), qf[ks], s[hb], 0, 0, 0);
#pragma unroll
      for (int r = 0; r < 4; ++r) { s[hb][r] = __builtin_amdgcn_exp2f((s[hb][r] - m) * C); l += s[hb][r]; }
    }
    const u32x4 pw = {pack2(s[0][0], s[0][1]), pack2(s[0][2], s[0][3]), pack2(s[1][0], s[1][1]), pack2(s[1][2], s[1][3])};
    const bf16x8 p = __builtin_bit_cast(bf16x8, pw);
#pragma unroll
    for (int db = 0; db < 8; ++db) {
      const unsigned d = 32 * (db >> 1) + 8 * (c >> 2) + 4 * (db & 1) + (c & 3);
      bf16x8 vf;
#pragma unroll
      for (int j = 0; j < 8; ++j) vf[j] = *(const __bf16*)(v + (32 * t + 16 * (j >> 2) + 4 * g + (j & 3)) * 256 + 2 * d);
      acc[db] = __builtin_amdgcn_mfma_f32_16x16x32_bf16(vf, p, acc[db], 0, 0, 0);
    }
  }
  l += __shfl_xor(l, 16); l += __shfl_xor(l, 32);
  const float r = 1.0f / l;
#pragma unroll
  for (int mm = 0; mm < 4; ++mm) {
    const u32x4 w = {pack2(acc[2 * mm][0] * r, acc[2 * mm][1] * r), pack2(acc[2 * mm][2] * r, acc[2 * mm][3] * r),
                     pack2(acc[2 * mm + 1][0] * r, acc[2 * mm + 1][1] * r), pack2(acc[2 * mm + 1][2] * r, acc[2 * mm + 1][3] * r)};
    *(u32x4*)(o + c * 256 + 64 * mm + 16 * g) = w;
  }
}

__device__ __forceinline__ unsigned unit_head(unsigned wg, unsigned r) { return (wg & 7u) * 32u + (32u * r + (wg >> 3)) / 5u; }
__device__ __forceinline__ unsigned unit_qoff(unsigned wg, unsigned r, unsigned w) {
  return unit_head(wg, r) * QHEAD + (((32u * r + (wg >> 3)) % 5u) * 320u + 80u * w) * 256u;
}

__global__ __launch_bounds__(256, 1) void attn_fwd(const char* __restrict__ Q, const char* __restrict__ K, const char* __restrict__ V, char* __restrict__ O) {
  extern __shared__ __attribute__((aligned(16))) char lds[];
  const unsigned wg = blockIdx.x;
  const unsigned w = __builtin_amdgcn_readfirstlane(threadIdx.x >> 6);
  const unsigned ldsb = (unsigned)(size_t)lds;
  unsigned flags;
  asm volatile(
    "s_load_dwordx8 s[12:19], %[ka], 0x0\n"
    "s_getpc_b64 s[84:85]\n"
    "LP0%=:\n"
    "v_mbcnt_lo_u32_b32 v8, -1, 0\n"
    "s_and_b32 s85, s85, 0xffff\n"
    "v_mbcnt_hi_u32_b32 v8, -1, v8\n"
    "s_mov_b32 s86, LEND%=-LP0%=\n"
    "v_lshrrev_b32 v9, 4, v8\n"
    "s_mov_b32 s87, 0x20000\n"
    "v_lshlrev_b32 v8, 7, v8\n"
    "s_mul_i32 s83, %[w], 2048\n"
    "v_lshl_add_u32 v8, v9, 28, v8\n"
    "buffer_load_dword v235, v8, s[84:87], s83 offen\n"
    "s_lshr_b32 s90, %[wg], 3\n"
    "s_and_b32 s91, %[wg], 7\n"
    "s_lshl_b32 s91, s91, 5\n"
    "s_mul_i32 s63, %[w], 20480\n"
    "s_add_u32 s88, s90, 0\n"
    "s_mul_hi_u32 s89, s88, 0x33333334\n"
    "s_mul_i32 s25, s89, 5\n"
    "s_sub_u32 s88, s88, s25\n"
    "s_add_u32 s89, s89, s91\n"
    "s_mul_i32 s88, s88, 81920\n"
    "s_mul_i32 s25, s89, 409600\n"
    "s_add_u32 s25, s25, s88\n"
    "s_add_u32 s25, s25, s63\n"
    "s_lshl_b32 s20, s89, 17\n"
    "v_mbcnt_lo_u32_b32 v0, -1, 0\n"
    "v_mbcnt_hi_u32_b32 v0, -1, v0\n"
    "v_lshlrev_b32 v236, 7, v0\n"
    "v_lshrrev_b32 v1, 4, v0\n"
    "s_mov_b32 s38, 0x2000000\n"
    "v_and_b32 v2, 15, v0\n"
    "s_mov_b32 s39, 0x20000\n"
    "v_lshlrev_b32 v3, 2, v1\n"
    "s_lshl_b32 s59, %[w], 12\n"
    "v_xor_b32 v3, v3, v2\n"
    "s_mul_i32 s57, %[w], 4160\n"
    "v_lshlrev_b32 v3, 4, v3\n"
    "s_add_u32 s57, s57, %[lds]\n"
    "v_lshl_add_u32 v229, v1, 8, v3\n"
    "s_add_u32 s52, s20, s59\n"
    "s_mov_b32 s42, 0x2000000\n"
    "s_mov_b32 s43, 0x20000\n"
    "s_lshl_b32 s60, %[w], 11\n"
    "s_add_u32 s61, s60, 8192\n"
    "s_mul_i32 s58, %[w], 2064\n"
    "s_add_u32 s58, s58, %[lds]\n"
    "s_add_u32 s58, s58, 33280\n"
    "s_add_u32 s53, s20, s60\n"
    "s_add_u32 s54, s20, s61\n"
    "s_mov_b32 s97, s63\n"
    "s_mul_i32 s63, %[w], 23552\n"
    "s_add_u32 s63, s63, %[lds]\n"
    "s_add_u32 s63, s63, 66304\n"
    "s_add_u32 s46, s63, 0x6400000\n"
    "s_mov_b32 s47, 0x20000\n"
    "s_mov_b32 s55, s25\n"
    "v_xor_b32 v3, v2, v1\n"
    "v_lshlrev_b32 v3, 4, v3\n"
    "v_lshl_add_u32 v3, v1, 8, v3\n"
    "v_add_u32 v224, s63, v3\n"
    "v_xor_b32 v4, 64, v3\n"
    "v_add_u32 v225, s63, v4\n"
    "v_xor_b32 v4, 128, v3\n"
    "v_add_u32 v226, s63, v4\n"
    "v_xor_b32 v4, 192, v3\n"
    "v_add_u32 v227, s63, v4\n"
    "s_add_u32 s88, s90, 32\n"
    "s_mul_hi_u32 s89, s88, 0x33333334\n"
    "s_mul_i32 s26, s89, 5\n"
    "s_sub_u32 s88, s88, s26\n"
    "s_add_u32 s89, s89, s91\n"
    "s_mul_i32 s88, s88, 81920\n"
    "s_mul_i32 s26, s89, 409600\n"
    "s_add_u32 s26, s26, s88\n"
    "s_add_u32 s26, s26, s97\n"
    "s_lshl_b32 s21, s89, 17\n"
    "s_add_u32 s88, s90, 64\n"
    "s_mul_hi_u32 s89, s88, 0x33333334\n"
    "s_mul_i32 s27, s89, 5\n"
    "s_sub_u32 s88, s88, s27\n"
    "s_add_u32 s89, s89, s91\n"
    "s_mul_i32 s88, s88, 81920\n"
    "s_mul_i32 s27, s89, 409600\n"
    "s_add_u32 s27, s27, s88\n"
    "s_add_u32 s27, s27, s97\n"
    "s_lshl_b32 s22, s89, 17\n"
    "s_add_u32 s88, s90, 96\n"
    "s_mul_hi_u32 s89, s88, 0x33333334\n"
    "s_mul_i32 s28, s89, 5\n"
    "s_sub_u32 s88, s88, s28\n"
    "s_add_u32 s89, s89, s91\n"
    "s_mul_i32 s88, s88, 81920\n"
    "s_mul_i32 s28, s89, 409600\n"
    "s_add_u32 s28, s28, s88\n"
    "s_add_u32 s28, s28, s97\n"
    "s_lshl_b32 s23, s89, 17\n"
    "s_add_u32 s88, s90, 128\n"
    "s_mul_hi_u32 s89, s88, 0x33333334\n"
    "s_mul_i32 s29, s89, 5\n"
    "s_sub_u32 s88, s88, s29\n"
    "s_add_u32 s89, s89, s91\n"
    "s_mul_i32 s88, s88, 81920\n"
    "s_mul_i32 s29, s89, 409600\n"
    "s_add_u32 s29, s29, s88\n"
    "s_add_u32 s29, s29, s97\n"
    "s_lshl_b32 s24, s89, 17\n"
    "v_lshrrev_b32 v3, 2, v2\n"
    "v_lshlrev_b32 v4, 10, v3\n"
    "v_lshrrev_b32 v3, 3, v2\n"
    "v_lshl_add_u32 v4, v3, 5, v4\n"
    "v_and_b32 v3, 3, v0\n"
    "v_lshl_add_u32 v4, v3, 8, v4\n"
    "v_lshl_add_u32 v4, v1, 4, v4\n"
    "v_add_u32 v4, %[lds], v4\n"
    "v_lshl_add_u32 v212, v3, 6, v4\n"
    "v_xor_b32 v5, 1, v3\n"
    "v_lshl_add_u32 v213, v5, 6, v4\n"
    "v_xor_b32 v5, 2, v3\n"
    "v_lshl_add_u32 v214, v5, 6, v4\n"
    "v_xor_b32 v5, 3, v3\n"
    "v_lshl_add_u32 v215, v5, 6, v4\n"
    "v_mul_u32_u24 v4, 1032, v1\n"
    "v_lshl_add_u32 v4, v3, 4, v4\n"
    "v_bfe_u32 v5, v0, 2, 2\n"
    "v_lshl_add_u32 v4, v5, 8, v4\n"
    "v_add_u32 v4, %[lds], v4\n"
    "v_lshl_add_u32 v216, v5, 6, v4\n"
    "v_xor_b32 v6, 1, v5\n"
    "v_lshl_add_u32 v217, v6, 6, v4\n"
    "v_xor_b32 v6, 2, v5\n"
    "v_lshl_add_u32 v218, v6, 6, v4\n"
    "v_xor_b32 v6, 3, v5\n"
    "v_lshl_add_u32 v219, v6, 6, v4\n"
    "v_xor_b32 v4, v2, v1\n"
    "v_lshlrev_b32 v4, 4, v4\n"
    "v_lshl_add_u32 v4, v2, 8, v4\n"
    "v_add_u32 v220, s63, v4\n"
    "v_xor_b32 v5, 64, v4\n"
    "v_add_u32 v221, s63, v5\n"
    "v_xor_b32 v5, 128, v4\n"
    "v_add_u32 v222, s63, v5\n"
    "v_xor_b32 v5, 192, v4\n"
    "v_add_u32 v223, s63, v5\n"
    "v_lshl_add_u32 v228, v0, 4, s63\n"
    "v_cmp_gt_u32 vcc, 2, v3\n"
    "v_mov_b32 v5, 0x3f803f80\n"
    "s_mov_b32 s76, 0x3e0293ee\n"
    "v_cndmask_b32 v5, 0, v5, vcc\n"
    "v_accvgpr_write_b32 a240, v5\n"
    "v_accvgpr_write_b32 a241, v5\n"
    "v_accvgpr_write_b32 a242, v5\n"
    "v_accvgpr_write_b32 a243, v5\n"
    "v_and_b32 v5, 1, v0\n"
    "v_mov_b32 v6, 0xaaaaaaaa\n"
    "v_mul_lo_u32 v5, v5, v6\n"
    "v_add_u32 v237, 0x44444444, v5\n"
    "v_mov_b32 v192, 0\n"
    "v_mov_b32 v193, 0\n"
    "v_mov_b32 v194, 0\n"
    "v_mov_b32 v195, 0\n"
    "v_mov_b32 v196, 0\n"
    "v_mov_b32 v197, 0\n"
    "v_mov_b32 v198, 0\n"
    "v_mov_b32 v199, 0\n"
    "v_mov_b32 v200, 0\n"
    "v_mov_b32 v201, 0\n"
    "v_mov_b32 v202, 0\n"
    "v_mov_b32 v203, 0\n"
    "v_mov_b32 v204, 0\n"
    "v_mov_b32 v205, 0\n"
    "v_mov_b32 v206, 0\n"
    "v_mov_b32 v207, 0\n"
    "v_mov_b32 v208, 0\n"
    "v_mov_b32 v209, 0\n"
    "v_mov_b32 v210, 0\n"
    "v_mov_b32 v211, 0\n"
    "s_mov_b32 s50, 0\n"
    "s_mov_b32 s51, 0x20000\n"
    "s_add_u32 s81, s63, 0x6400000\n"
    "s_mov_b32 s56, s25\n"
    "s_mov_b32 s64, s25\n"
    "s_cmp_lt_u32 %[w], 2\n"
    "s_and_b32 s97, %[w], 1\n"
    "s_lshl_b32 s97, s97, 13\n"
    "s_add_u32 s96, s20, s97\n"
    "s_add_u32 s96, s96, 32768\n"
    "s_mov_b32 s75, 0x31800000\n"
    "s_mov_b32 s77, 0x1c000000\n"
    "s_mov_b32 s78, 0\n"
    "s_mov_b32 s96, 0\n"
    "s_mov_b32 s79, 5\n"
    "s_mov_b32 s80, 0xffff0000\n"
    "s_mov_b32 s71, s25\n"
    "s_mov_b32 s72, s26\n"
    "s_mov_b32 s73, s27\n"
    "s_mov_b32 s74, s28\n"
    "s_mov_b32 s82, s29\n"
    "s_mov_b32 s65, s21\n"
    "s_mov_b32 s66, s22\n"
    "s_mov_b32 s67, s23\n"
    "s_mov_b32 s68, s24\n"
    "s_mov_b32 s69, s24\n"
    "s_waitcnt lgkmcnt(0)\n"
    "s_mov_b64 s[36:37], s[14:15]\n"
    "s_and_b32 s37, s37, 0xffff\n"
    "s_add_u32 m0, s57, 0\n"
    "s_nop 0\n"
    "buffer_load_dwordx4 v229, s[36:39], s52 offen lds\n"
    "buffer_load_dwordx4 v229, s[36:39], s52 offen offset:1024 lds\n"
    "s_add_u32 m0, s57, 32\n"
    "s_nop 0\n"
    "buffer_load_dwordx4 v229, s[36:39], s52 offen offset:2048 lds\n"
    "buffer_load_dwordx4 v229, s[36:39], s52 offen offset:3072 lds\n"
    "s_mov_b64 s[40:41], s[16:17]\n"
    "s_and_b32 s41, s41, 0xffff\n"
    "s_add_u32 m0, s58, 0\n"
    "s_nop 0\n"
    "buffer_load_dwordx4 v229, s[40:43], s53 offen lds\n"
    "s_add_u32 m0, s58, 8\n"
    "s_nop 0\n"
    "buffer_load_dwordx4 v229, s[40:43], s53 offen offset:1024 lds\n"
    "s_add_u32 m0, s58, 16512\n"
    "s_nop 0\n"
    "buffer_load_dwordx4 v229, s[40:43], s54 offen lds\n"
    "s_add_u32 m0, s58, 16520\n"
    "s_nop 0\n"
    "buffer_load_dwordx4 v229, s[40:43], s54 offen offset:1024 lds\n"
    "s_mov_b64 s[44:45], s[12:13]\n"
    "s_and_b32 s45, s45, 0xffff\n"
    "s_sub_u32 s44, s44, s63\n"
    "s_subb_u32 s45, s45, 0\n"
    "s_add_u32 m0, s63, 0\n"
    "s_nop 0\n"
    "buffer_load_dwordx4 v224, s[44:47], s55 offen lds\n"
    "buffer_load_dwordx4 v225, s[44:47], s55 offen offset:1024 lds\n"
    "buffer_load_dwordx4 v226, s[44:47], s55 offen offset:2048 lds\n"
    "buffer_load_dwordx4 v227, s[44:47], s55 offen offset:3072 lds\n"
    "s_add_u32 s55, s55, 4096\n"
    "s_mov_b64 s[48:49], s[18:19]\n"
    "s_and_b32 s49, s49, 0xffff\n"
    "s_sub_u32 s48, s48, s63\n"
    "s_subb_u32 s49, s49, 0\n"
    "s_waitcnt vmcnt(4)\n"
    "s_barrier\n"
    "ds_read_b128 a[80:83], v212 offset:0\n"
    "s_add_u32 m0, s63, 4096\n"
    "s_nop 0\n"
    "buffer_load_dwordx4 v224, s[44:47], s55 offen lds\n"
    "ds_read_b128 a[84:87], v213 offset:0\n"
    "ds_read_b128 a[88:91], v214 offset:0\n"
    "ds_read_b128 a[92:95], v215 offset:0\n"
    "ds_read_b128 a[108:111], v215 offset:4160\n"
    "ds_read_b128 a[104:107], v214 offset:4160\n"
    "buffer_load_dwordx4 v225, s[44:47], s55 offen offset:1024 lds\n"
    "ds_read_b128 a[100:103], v213 offset:4160\n"
    "ds_read_b128 a[96:99], v212 offset:4160\n"
    "ds_read_b128 a[112:115], v212 offset:8320\n"
    "ds_read_b128 a[116:119], v213 offset:8320\n"
    "ds_read_b128 a[120:123], v214 offset:8320\n"
    "buffer_load_dwordx4 v226, s[44:47], s55 offen offset:2048 lds\n"
    "ds_read_b128 a[124:127], v215 offset:8320\n"
    "ds_read_b128 a[140:143], v215 offset:12480\n"
    "ds_read_b128 a[136:139], v214 offset:12480\n"
    "ds_read_b128 a[132:135], v213 offset:12480\n"
    "ds_read_b128 a[128:131], v212 offset:12480\n"
    "buffer_load_dwordx4 v227, s[44:47], s55 offen offset:3072 lds\n"
    "ds_read_b64_tr_b16 a[144:145], v216 offset:33280\n"
    "ds_read_b64_tr_b16 a[146:147], v216 offset:37408\n"
    "ds_read_b64_tr_b16 a[148:149], v216 offset:33288\n"
    "ds_read_b64_tr_b16 a[150:151], v216 offset:37416\n"
    "ds_read_b64_tr_b16 a[152:153], v217 offset:33280\n"
    "s_add_u32 s55, s55, 4096\n"
    "s_add_u32 s53, s53, 16384\n"
    "s_add_u32 m0, s58, 8256\n"
    "s_nop 0\n"
    "buffer_load_dwordx4 v229, s[40:43], s53 offen lds\n"
    "ds_read_b64_tr_b16 a[154:155], v217 offset:37408\n"
    "ds_read_b64_tr_b16 a[156:157], v217 offset:33288\n"
    "ds_read_b64_tr_b16 a[158:159], v217 offset:37416\n"
    "ds_read_b64_tr_b16 a[160:161], v218 offset:33280\n"
    "ds_read_b64_tr_b16 a[162:163], v218 offset:37408\n"
    "s_add_u32 m0, s58, 8264\n"
    "s_nop 0\n"
    "buffer_load_dwordx4 v229, s[40:43], s53 offen offset:1024 lds\n"
    "ds_read_b64_tr_b16 a[164:165], v218 offset:33288\n"
    "ds_read_b64_tr_b16 a[166:167], v218 offset:37416\n"
    "ds_read_b64_tr_b16 a[168:169], v219 offset:33280\n"
    "ds_read_b64_tr_b16 a[170:171], v219 offset:37408\n"
    "ds_read_b64_tr_b16 a[172:173], v219 offset:33288\n"
    "s_add_u32 m0, s63, 8192\n"
    "s_nop 0\n"
    "buffer_load_dwordx4 v224, s[44:47], s55 offen lds\n"
    "ds_read_b64_tr_b16 a[174:175], v219 offset:37416\n"
    "ds_read_b64_tr_b16 a[208:209], v216 offset:49792\n"
    "ds_read_b64_tr_b16 a[210:211], v216 offset:53920\n"
    "ds_read_b64_tr_b16 a[212:213], v216 offset:49800\n"
    "ds_read_b64_tr_b16 a[214:215], v216 offset:53928\n"
    "buffer_load_dwordx4 v225, s[44:47], s55 offen offset:1024 lds\n"
    "s_waitcnt vmcnt(8)\n"
    "ds_read_b128 v[128:131], v220 offset:0\n"
    "ds_read_b128 v[132:135], v221 offset:0\n"
    "ds_read_b128 v[136:139], v222 offset:0\n"
    "ds_read_b128 v[140:143], v223 offset:0\n"
    "s_waitcnt lgkmcnt(3)\n"
    "v_lshlrev_b32 v244, 16, v128\n"
    "buffer_load_dwordx4 v226, s[44:47], s55 offen offset:2048 lds\n"
    "v_and_b32 v245, s80, v128\n"
    "v_lshlrev_b32 v246, 16, v129\n"
    "v_and_b32 v247, s80, v129\n"
    "v_lshlrev_b32 v248, 16, v130\n"
    "v_and_b32 v249, s80, v130\n"
    "v_lshlrev_b32 v250, 16, v131\n"
    "v_and_b32 v251, s80, v131\n"
    "v_pk_mul_f32 v[244:245], v[244:245], s[76:77] op_sel_hi:[1,0]\n"
    "v_pk_mul_f32 v[246:247], v[246:247], s[76:77] op_sel_hi:[1,0]\n"
    "v_pk_mul_f32 v[248:249], v[248:249], s[76:77] op_sel_hi:[1,0]\n"
    "v_pk_mul_f32 v[250:251], v[250:251], s[76:77] op_sel_hi:[1,0]\n"
    "v_cvt_pk_bf16_f32 v244, v244, v245\n"
    "v_cvt_pk_bf16_f32 v246, v246, v247\n"
    "v_cvt_pk_bf16_f32 v248, v248, v249\n"
    "v_cvt_pk_bf16_f32 v250, v250, v251\n"
    "v_accvgpr_write_b32 a0, v244\n"
    "buffer_load_dwordx4 v227, s[44:47], s55 offen offset:3072 lds\n"
    "v_accvgpr_write_b32 a1, v246\n"
    "v_accvgpr_write_b32 a2, v248\n"
    "v_accvgpr_write_b32 a3, v250\n"
    "s_waitcnt lgkmcnt(2)\n"
    "v_lshlrev_b32 v244, 16, v132\n"
    "v_and_b32 v245, s80, v132\n"
    "v_lshlrev_b32 v246, 16, v133\n"
    "v_and_b32 v247, s80, v133\n"
    "v_lshlrev_b32 v248, 16, v134\n"
    "v_and_b32 v249, s80, v134\n"
    "v_lshlrev_b32 v250, 16, v135\n"
    "v_and_b32 v251, s80, v135\n"
    "v_pk_mul_f32 v[244:245], v[244:245], s[76:77] op_sel_hi:[1,0]\n"
    "v_pk_mul_f32 v[246:247], v[246:247], s[76:77] op_sel_hi:[1,0]\n"
    "v_pk_mul_f32 v[248:249], v[248:249], s[76:77] op_sel_hi:[1,0]\n"
    "v_pk_mul_f32 v[250:251], v[250:251], s[76:77] op_sel_hi:[1,0]\n"
    "v_cvt_pk_bf16_f32 v244, v244, v245\n"
    "s_add_u32 s55, s55, 4096\n"
    "s_add_u32 m0, s63, 12288\n"
    "s_nop 0\n"
    "buffer_load_dwordx4 v224, s[44:47], s55 offen lds\n"
    "v_cvt_pk_bf16_f32 v246, v246, v247\n"
    "v_cvt_pk_bf16_f32 v248, v248, v249\n"
    "v_cvt_pk_bf16_f32 v250, v250, v251\n"
    "v_accvgpr_write_b32 a4, v244\n"
    "v_accvgpr_write_b32 a5, v246\n"
    "v_accvgpr_write_b32 a6, v248\n"
    "v_accvgpr_write_b32 a7, v250\n"
    "s_waitcnt lgkmcnt(1)\n"
    "v_lshlrev_b32 v244, 16, v136\n"
    "v_and_b32 v245, s80, v136\n"
    "v_lshlrev_b32 v246, 16, v137\n"
    "v_and_b32 v247, s80, v137\n"
    "v_lshlrev_b32 v248, 16, v138\n"
    "v_and_b32 v249, s80, v138\n"
    "v_lshlrev_b32 v250, 16, v139\n"
    "v_and_b32 v251, s80, v139\n"
    "v_pk_mul_f32 v[244:245], v[244:245], s[76:77] op_sel_hi:[1,0]\n"
    "buffer_load_dwordx4 v225, s[44:47], s55 offen offset:1024 lds\n"
    "v_pk_mul_f32 v[246:247], v[246:247], s[76:77] op_sel_hi:[1,0]\n"
    "v_pk_mul_f32 v[248:249], v[248:249], s[76:77] op_sel_hi:[1,0]\n"
    "v_pk_mul_f32 v[250:251], v[250:251], s[76:77] op_sel_hi:[1,0]\n"
    "v_cvt_pk_bf16_f32 v244, v244, v245\n"
    "v_cvt_pk_bf16_f32 v246, v246, v247\n"
    "v_cvt_pk_bf16_f32 v248, v248, v249\n"
    "v_cvt_pk_bf16_f32 v250, v250, v251\n"
    "v_accvgpr_write_b32 a8, v244\n"
    "v_accvgpr_write_b32 a9, v246\n"
    "v_accvgpr_write_b32 a10, v248\n"
    "v_accvgpr_write_b32 a11, v250\n"
    "s_waitcnt lgkmcnt(0)\n"
    "v_lshlrev_b32 v244, 16, v140\n"
    "v_and_b32 v245, s80, v140\n"
    "v_lshlrev_b32 v246, 16, v141\n"
    "v_and_b32 v247, s80, v141\n"
    "v_lshlrev_b32 v248, 16, v142\n"
    "v_and_b32 v249, s80, v142\n"
    "v_lshlrev_b32 v250, 16, v143\n"
    "v_and_b32 v251, s80, v143\n"
    "v_pk_mul_f32 v[244:245], v[244:245], s[76:77] op_sel_hi:[1,0]\n"
    "v_pk_mul_f32 v[246:247], v[246:247], s[76:77] op_sel_hi:[1,0]\n"
    "v_pk_mul_f32 v[248:249], v[248:249], s[76:77] op_sel_hi:[1,0]\n"
    "v_pk_mul_f32 v[250:251], v[250:251], s[76:77] op_sel_hi:[1,0]\n"
    "v_cvt_pk_bf16_f32 v244, v244, v245\n"
    "v_cvt_pk_bf16_f32 v246, v246, v247\n"
    "v_cvt_pk_bf16_f32 v248, v248, v249\n"
    "v_cvt_pk_bf16_f32 v250, v250, v251\n"
    "v_accvgpr_write_b32 a12, v244\n"
    "v_accvgpr_write_b32 a13, v246\n"
    "v_accvgpr_write_b32 a14, v248\n"
    "v_accvgpr_write_b32 a15, v250\n"
    "s_nop 1\n"
    "v_mfma_f32_16x16x32_bf16 v[160:163], a[80:83], a[0:3], -4.0\n"
    "v_mfma_f32_16x16x32_bf16 v[160:163], a[84:87], a[4:7], v[160:163]\n"
    "v_mfma_f32_16x16x32_bf16 v[160:163], a[88:91], a[8:11], v[160:163]\n"
    "v_mfma_f32_16x16x32_bf16 v[160:163], a[92:95], a[12:15], v[160:163]\n"
    "v_mfma_f32_16x16x32_bf16 v[164:167], a[108:111], a[12:15], -4.0\n"
    "v_mfma_f32_16x16x32_bf16 v[164:167], a[104:107], a[8:11], v[164:167]\n"
    "v_mfma_f32_16x16x32_bf16 v[164:167], a[100:103], a[4:7], v[164:167]\n"
    "v_mfma_f32_16x16x32_bf16 v[164:167], a[96:99], a[0:3], v[164:167]\n"
    "v_mfma_f32_16x16x32_bf16 v[168:171], a[112:115], a[0:3], -4.0\n"
    "v_mfma_f32_16x16x32_bf16 v[168:171], a[116:119], a[4:7], v[168:171]\n"
    "v_mfma_f32_16x16x32_bf16 v[168:171], a[120:123], a[8:11], v[168:171]\n"
    "v_mfma_f32_16x16x32_bf16 v[168:171], a[124:127], a[12:15], v[168:171]\n"
    "v_mfma_f32_16x16x32_bf16 v[172:175], a[140:143], a[12:15], -4.0\n"
    "v_mfma_f32_16x16x32_bf16 v[172:175], a[136:139], a[8:11], v[172:175]\n"
    "v_mfma_f32_16x16x32_bf16 v[172:175], a[132:135], a[4:7], v[172:175]\n"
    "v_mfma_f32_16x16x32_bf16 v[172:175], a[128:131], a[0:3], v[172:175]\n"
    "v_exp_f32 v160, v160\n"
    "v_exp_f32 v161, v161\n"
    "v_exp_f32 v162, v162\n"
    "v_exp_f32 v163, v163\n"
    "v_exp_f32 v164, v164\n"
    "v_exp_f32 v165, v165\n"
    "v_exp_f32 v166, v166\n"
    "v_exp_f32 v167, v167\n"
    "v_cvt_pk_bf16_f32 v176, v160, v161\n"
    "v_cvt_pk_bf16_f32 v177, v162, v163\n"
    "v_exp_f32 v168, v168\n"
    "v_exp_f32 v169, v169\n"
    "v_exp_f32 v170, v170\n"
    "v_exp_f32 v171, v171\n"
    "s_waitcnt vmcnt(8)\n"
    "ds_read_b128 v[144:147], v220 offset:4096\n"
    "ds_read_b128 v[148:151], v221 offset:4096\n"
    "ds_read_b128 v[152:155], v222 offset:4096\n"
    "ds_read_b128 v[156:159], v223 offset:4096\n"
    "s_waitcnt lgkmcnt(3)\n"
    "v_lshlrev_b32 v244, 16, v144\n"
    "buffer_load_dwordx4 v226, s[44:47], s55 offen offset:2048 lds\n"
    "v_and_b32 v245, s80, v144\n"
    "v_lshlrev_b32 v246, 16, v145\n"
    "v_and_b32 v247, s80, v145\n"
    "v_lshlrev_b32 v248, 16, v146\n"
    "v_and_b32 v249, s80, v146\n"
    "v_lshlrev_b32 v250, 16, v147\n"
    "v_and_b32 v251, s80, v147\n"
    "v_pk_mul_f32 v[244:245], v[244:245], s[76:77] op_sel_hi:[1,0]\n"
    "v_pk_mul_f32 v[246:247], v[246:247], s[76:77] op_sel_hi:[1,0]\n"
    "v_pk_mul_f32 v[248:249], v[248:249], s[76:77] op_sel_hi:[1,0]\n"
    "v_pk_mul_f32 v[250:251], v[250:251], s[76:77] op_sel_hi:[1,0]\n"
    "v_cvt_pk_bf16_f32 v244, v244, v245\n"
    "v_cvt_pk_bf16_f32 v246, v246, v247\n"
    "v_cvt_pk_bf16_f32 v248, v248, v249\n"
    "v_cvt_pk_bf16_f32 v250, v250, v251\n"
    "v_accvgpr_write_b32 a16, v244\n"
    "buffer_load_dwordx4 v227, s[44:47], s55 offen offset:3072 lds\n"
    "v_accvgpr_write_b32 a17, v246\n"
    "v_accvgpr_write_b32 a18, v248\n"
    "v_accvgpr_write_b32 a19, v250\n"
    "s_waitcnt lgkmcnt(2)\n"
    "v_lshlrev_b32 v244, 16, v148\n"
    "v_and_b32 v245, s80, v148\n"
    "v_lshlrev_b32 v246, 16, v149\n"
    "v_and_b32 v247, s80, v149\n"
    "v_lshlrev_b32 v248, 16, v150\n"
    "v_and_b32 v249, s80, v150\n"
    "v_lshlrev_b32 v250, 16, v151\n"
    "v_and_b32 v251, s80, v151\n"
    "v_pk_mul_f32 v[244:245], v[244:245], s[76:77] op_sel_hi:[1,0]\n"
    "v_pk_mul_f32 v[246:247], v[246:247], s[76:77] op_sel_hi:[1,0]\n"
    "v_pk_mul_f32 v[248:249], v[248:249], s[76:77] op_sel_hi:[1,0]\n"
    "v_pk_mul_f32 v[250:251], v[250:251], s[76:77] op_sel_hi:[1,0]\n"
    "v_cvt_pk_bf16_f32 v244, v244, v245\n"
    "s_add_u32 s55, s55, 4096\n"
    "s_add_u32 m0, s63, 16384\n"
    "s_nop 0\n"
    "buffer_load_dwordx4 v224, s[44:47], s55 offen lds\n"
    "v_cvt_pk_bf16_f32 v246, v246, v247\n"
    "v_cvt_pk_bf16_f32 v248, v248, v249\n"
    "v_cvt_pk_bf16_f32 v250, v250, v251\n"
    "v_accvgpr_write_b32 a20, v244\n"
    "v_accvgpr_write_b32 a21, v246\n"
    "v_accvgpr_write_b32 a22, v248\n"
    "v_accvgpr_write_b32 a23, v250\n"
    "s_waitcnt lgkmcnt(1)\n"
    "v_lshlrev_b32 v244, 16, v152\n"
    "v_and_b32 v245, s80, v152\n"
    "v_lshlrev_b32 v246, 16, v153\n"
    "v_and_b32 v247, s80, v153\n"
    "v_lshlrev_b32 v248, 16, v154\n"
    "v_and_b32 v249, s80, v154\n"
    "v_lshlrev_b32 v250, 16, v155\n"
    "v_and_b32 v251, s80, v155\n"
    "v_pk_mul_f32 v[244:245], v[244:245], s[76:77] op_sel_hi:[1,0]\n"
    "buffer_load_dwordx4 v225, s[44:47], s55 offen offset:1024 lds\n"
    "v_pk_mul_f32 v[246:247], v[246:247], s[76:77] op_sel_hi:[1,0]\n"
    "v_pk_mul_f32 v[248:249], v[248:249], s[76:77] op_sel_hi:[1,0]\n"
    "v_pk_mul_f32 v[250:251], v[250:251], s[76:77] op_sel_hi:[1,0]\n"
    "v_cvt_pk_bf16_f32 v244, v244, v245\n"
    "v_cvt_pk_bf16_f32 v246, v246, v247\n"
    "v_cvt_pk_bf16_f32 v248, v248, v249\n"
    "v_cvt_pk_bf16_f32 v250, v250, v251\n"
    "v_accvgpr_write_b32 a24, v244\n"
    "v_accvgpr_write_b32 a25, v246\n"
    "v_accvgpr_write_b32 a26, v248\n"
    "v_accvgpr_write_b32 a27, v250\n"
    "s_waitcnt lgkmcnt(0)\n"
    "v_lshlrev_b32 v244, 16, v156\n"
    "v_and_b32 v245, s80, v156\n"
    "v_lshlrev_b32 v246, 16, v157\n"
    "v_and_b32 v247, s80, v157\n"
    "v_lshlrev_b32 v248, 16, v158\n"
    "v_and_b32 v249, s80, v158\n"
    "v_lshlrev_b32 v250, 16, v159\n"
    "v_and_b32 v251, s80, v159\n"
    "v_pk_mul_f32 v[244:245], v[244:245], s[76:77] op_sel_hi:[1,0]\n"
    "v_pk_mul_f32 v[246:247], v[246:247], s[76:77] op_sel_hi:[1,0]\n"
    "v_pk_mul_f32 v[248:249], v[248:249], s[76:77] op_sel_hi:[1,0]\n"
    "v_pk_mul_f32 v[250:251], v[250:251], s[76:77] op_sel_hi:[1,0]\n"
    "v_cvt_pk_bf16_f32 v244, v244, v245\n"
    "v_cvt_pk_bf16_f32 v246, v246, v247\n"
    "v_cvt_pk_bf16_f32 v248, v248, v249\n"
    "v_cvt_pk_bf16_f32 v250, v250, v251\n"
    "v_accvgpr_write_b32 a28, v244\n"
    "v_accvgpr_write_b32 a29, v246\n"
    "v_accvgpr_write_b32 a30, v248\n"
    "v_accvgpr_write_b32 a31, v250\n"
    "s_nop 1\n"
    "v_mfma_f32_16x16x32_bf16 v[160:163], a[80:83], a[16:19], -4.0\n"
    "v_exp_f32 v172, v172\n"
    "v_mfma_f32_16x16x32_bf16 v[160:163], a[84:87], a[20:23], v[160:163]\n"
    "v_cvt_pk_bf16_f32 v178, v164, v165\n"
    "v_cvt_pk_bf16_f32 v179, v166, v167\n"
    "v_mfma_f32_16x16x32_bf16 v[160:163], a[88:91], a[24:27], v[160:163]\n"
    "v_exp_f32 v173, v173\n"
    "v_mfma_f32_16x16x32_bf16 v[160:163], a[92:95], a[28:31], v[160:163]\n"
    "ds_read_b64_tr_b16 a[216:217], v217 offset:49792\n"
    "ds_read_b64_tr_b16 a[218:219], v217 offset:53920\n"
    "v_mfma_f32_16x16x32_bf16 v[164:167], a[108:111], a[28:31], -4.0\n"
    "v_exp_f32 v174, v174\n"
    "v_mfma_f32_16x16x32_bf16 v[164:167], a[104:107], a[24:27], v[164:167]\n"
    "ds_read_b64_tr_b16 a[220:221], v217 offset:49800\n"
    "ds_read_b64_tr_b16 a[222:223], v217 offset:53928\n"
    "v_mfma_f32_16x16x32_bf16 v[164:167], a[100:103], a[20:23], v[164:167]\n"
    "v_exp_f32 v175, v175\n"
    "v_mfma_f32_16x16x32_bf16 v[164:167], a[96:99], a[16:19], v[164:167]\n"
    "ds_read_b64_tr_b16 a[224:225], v218 offset:49792\n"
    "v_cvt_pk_bf16_f32 v180, v168, v169\n"
    "v_cvt_pk_bf16_f32 v181, v170, v171\n"
    "v_mfma_f32_16x16x32_bf16 v[168:171], a[112:115], a[16:19], -4.0\n"
    "s_add_u32 s52, s52, 16384\n"
    "ds_read_b64_tr_b16 a[226:227], v218 offset:53920\n"
    "v_mfma_f32_16x16x32_bf16 v[168:171], a[116:119], a[20:23], v[168:171]\n"
    "ds_read_b64_tr_b16 a[228:229], v218 offset:49800\n"
    "v_exp_f32 v160, v160\n"
    "v_mfma_f32_16x16x32_bf16 v[168:171], a[120:123], a[24:27], v[168:171]\n"
    "s_add_u32 s54, s54, 16384\n"
    "ds_read_b64_tr_b16 a[230:231], v218 offset:53928\n"
    "v_cvt_pk_bf16_f32 v182, v172, v173\n"
    "v_mfma_f32_16x16x32_bf16 v[168:171], a[124:127], a[28:31], v[168:171]\n"
    "ds_read_b64_tr_b16 a[232:233], v219 offset:49792\n"
    "v_exp_f32 v161, v161\n"
    "v_cvt_pk_bf16_f32 v183, v174, v175\n"
    "v_mfma_f32_16x16x32_bf16 v[172:175], a[140:143], a[28:31], -4.0\n"
    "s_add_u32 s53, s53, 16384\n"
    "ds_read_b64_tr_b16 a[234:235], v219 offset:53920\n"
    "ds_read_b64_tr_b16 a[236:237], v219 offset:49800\n"
    "v_mfma_f32_16x16x32_bf16 v[172:175], a[136:139], a[24:27], v[172:175]\n"
    "v_exp_f32 v162, v162\n"
    "v_mfma_f32_16x16x32_bf16 v[172:175], a[132:135], a[20:23], v[172:175]\n"
    "ds_read_b64_tr_b16 a[238:239], v219 offset:53928\n"
    "v_cvt_pk_bf16_f32 v184, v160, v161\n"
    "v_mfma_f32_16x16x32_bf16 v[172:175], a[128:131], a[16:19], v[172:175]\n"
    "v_exp_f32 v163, v163\n"
    "v_smfmac_f32_16x16x64_bf16 v[192:195], a[240:243], v[176:183], v237\n"
    "v_cvt_pk_bf16_f32 v185, v162, v163\n"
    "v_mfma_f32_16x16x32_bf16 v[0:3], a[208:211], v[180:183], 0\n"
    "v_exp_f32 v164, v164\n"
    "v_mfma_f32_16x16x32_bf16 v[0:3], a[144:147], v[176:179], v[0:3]\n"
    "v_mfma_f32_16x16x32_bf16 v[4:7], a[148:151], v[176:179], 0\n"
    "v_exp_f32 v165, v165\n"
    "v_mfma_f32_16x16x32_bf16 v[4:7], a[212:215], v[180:183], v[4:7]\n"
    "s_waitcnt lgkmcnt(1)\n"
    "v_cvt_pk_bf16_f32 v186, v164, v165\n"
    "v_mfma_f32_16x16x32_bf16 v[8:11], a[216:219], v[180:183], 0\n"
    "v_exp_f32 v166, v166\n"
    "v_mfma_f32_16x16x32_bf16 v[8:11], a[152:155], v[176:179], v[8:11]\n"
    "v_mfma_f32_16x16x32_bf16 v[12:15], a[156:159], v[176:179], 0\n"
    "s_waitcnt vmcnt(10) lgkmcnt(0)\n"
    "v_exp_f32 v167, v167\n"
    "v_mfma_f32_16x16x32_bf16 v[12:15], a[220:223], v[180:183], v[12:15]\n"
    "s_barrier\n"
    "v_mfma_f32_16x16x32_bf16 v[16:19], a[224:227], v[180:183], 0\n"
    "v_exp_f32 v168, v168\n"
    "v_mfma_f32_16x16x32_bf16 v[16:19], a[160:163], v[176:179], v[16:19]\n"
    "buffer_load_dwordx4 v226, s[44:47], s55 offen offset:2048 lds\n"
    "v_mfma_f32_16x16x32_bf16 v[20:23], a[164:167], v[176:179], 0\n"
    "v_exp_f32 v169, v169\n"
    "v_mfma_f32_16x16x32_bf16 v[20:23], a[228:231], v[180:183], v[20:23]\n"
    "v_mfma_f32_16x16x32_bf16 v[24:27], a[232:235], v[180:183], 0\n"
    "v_exp_f32 v170, v170\n"
    "v_mfma_f32_16x16x32_bf16 v[24:27], a[168:171], v[176:179], v[24:27]\n"
    "buffer_load_dwordx4 v227, s[44:47], s55 offen offset:3072 lds\n"
    "s_add_u32 s55, s55, 4096\n"
    "v_cvt_pk_bf16_f32 v187, v166, v167\n"
    "v_cvt_pk_bf16_f32 v188, v168, v169\n"
    "v_mfma_f32_16x16x32_bf16 v[28:31], a[172:175], v[176:179], 0\n"
    "v_exp_f32 v171, v171\n"
    "v_mfma_f32_16x16x32_bf16 v[28:31], a[236:239], v[180:183], v[28:31]\n"
    "v_cvt_pk_bf16_f32 v189, v170, v171\n"
    "s_waitcnt vmcnt(8)\n"
    "ds_read_b128 v[128:131], v220 offset:8192\n"
    "ds_read_b128 v[132:135], v221 offset:8192\n"
    "ds_read_b128 v[136:139], v222 offset:8192\n"
    "ds_read_b128 v[140:143], v223 offset:8192\n"
    "s_waitcnt lgkmcnt(3)\n"
    "v_lshlrev_b32 v244, 16, v128\n"
    "s_add_u32 m0, s57, 16640\n"
    "s_nop 0\n"
    "buffer_load_dwordx4 v229, s[36:39], s52 offen lds\n"
    "v_and_b32 v245, s80, v128\n"
    "v_lshlrev_b32 v246, 16, v129\n"
    "v_and_b32 v247, s80, v129\n"
    "v_lshlrev_b32 v248, 16, v130\n"
    "v_and_b32 v249, s80, v130\n"
    "v_lshlrev_b32 v250, 16, v131\n"
    "v_and_b32 v251, s80, v131\n"
    "v_pk_mul_f32 v[244:245], v[244:245], s[76:77] op_sel_hi:[1,0]\n"
    "v_pk_mul_f32 v[246:247], v[246:247], s[76:77] op_sel_hi:[1,0]\n"
    "v_pk_mul_f32 v[248:249], v[248:249], s[76:77] op_sel_hi:[1,0]\n"
    "v_pk_mul_f32 v[250:251], v[250:251], s[76:77] op_sel_hi:[1,0]\n"
    "v_cvt_pk_bf16_f32 v244, v244, v245\n"
    "v_cvt_pk_bf16_f32 v246, v246, v247\n"
    "v_cvt_pk_bf16_f32 v248, v248, v249\n"
    "v_cvt_pk_bf16_f32 v250, v250, v251\n"
    "v_accvgpr_write_b32 a32, v244\n"
    "buffer_load_dwordx4 v229, s[36:39], s52 offen offset:1024 lds\n"
    "v_accvgpr_write_b32 a33, v246\n"
    "v_accvgpr_write_b32 a34, v248\n"
    "v_accvgpr_write_b32 a35, v250\n"
    "s_waitcnt lgkmcnt(2)\n"
    "v_lshlrev_b32 v244, 16, v132\n"
    "v_and_b32 v245, s80, v132\n"
    "v_lshlrev_b32 v246, 16, v133\n"
    "v_and_b32 v247, s80, v133\n"
    "v_lshlrev_b32 v248, 16, v134\n"
    "v_and_b32 v249, s80, v134\n"
    "v_lshlrev_b32 v250, 16, v135\n"
    "v_and_b32 v251, s80, v135\n"
    "v_pk_mul_f32 v[244:245], v[244:245], s[76:77] op_sel_hi:[1,0]\n"
    "v_pk_mul_f32 v[246:247], v[246:247], s[76:77] op_sel_hi:[1,0]\n"
    "v_pk_mul_f32 v[248:249], v[248:249], s[76:77] op_sel_hi:[1,0]\n"
    "v_pk_mul_f32 v[250:251], v[250:251], s[76:77] op_sel_hi:[1,0]\n"
    "v_cvt_pk_bf16_f32 v244, v244, v245\n"
    "s_add_u32 m0, s57, 16672\n"
    "s_nop 0\n"
    "buffer_load_dwordx4 v229, s[36:39], s52 offen offset:2048 lds\n"
    "v_cvt_pk_bf16_f32 v246, v246, v247\n"
    "v_cvt_pk_bf16_f32 v248, v248, v249\n"
    "v_cvt_pk_bf16_f32 v250, v250, v251\n"
    "v_accvgpr_write_b32 a36, v244\n"
    "v_accvgpr_write_b32 a37, v246\n"
    "v_accvgpr_write_b32 a38, v248\n"
    "v_accvgpr_write_b32 a39, v250\n"
    "s_waitcnt lgkmcnt(1)\n"
    "v_lshlrev_b32 v244, 16, v136\n"
    "v_and_b32 v245, s80, v136\n"
    "v_lshlrev_b32 v246, 16, v137\n"
    "v_and_b32 v247, s80, v137\n"
    "v_lshlrev_b32 v248, 16, v138\n"
    "v_and_b32 v249, s80, v138\n"
    "v_lshlrev_b32 v250, 16, v139\n"
    "v_and_b32 v251, s80, v139\n"
    "v_pk_mul_f32 v[244:245], v[244:245], s[76:77] op_sel_hi:[1,0]\n"
    "buffer_load_dwordx4 v229, s[36:39], s52 offen offset:3072 lds\n"
    "s_add_u32 s52, s52, 16384\n"
    "v_pk_mul_f32 v[246:247], v[246:247], s[76:77] op_sel_hi:[1,0]\n"
    "v_pk_mul_f32 v[248:249], v[248:249], s[76:77] op_sel_hi:[1,0]\n"
    "v_pk_mul_f32 v[250:251], v[250:251], s[76:77] op_sel_hi:[1,0]\n"
    "v_cvt_pk_bf16_f32 v244, v244, v245\n"
    "v_cvt_pk_bf16_f32 v246, v246, v247\n"
    "v_cvt_pk_bf16_f32 v248, v248, v249\n"
    "v_cvt_pk_bf16_f32 v250, v250, v251\n"
    "v_accvgpr_write_b32 a40, v244\n"
    "v_accvgpr_write_b32 a41, v246\n"
    "v_accvgpr_write_b32 a42, v248\n"
    "v_accvgpr_write_b32 a43, v250\n"
    "s_waitcnt lgkmcnt(0)\n"
    "v_lshlrev_b32 v244, 16, v140\n"
    "v_and_b32 v245, s80, v140\n"
    "v_lshlrev_b32 v246, 16, v141\n"
    "v_and_b32 v247, s80, v141\n"
    "v_lshlrev_b32 v248, 16, v142\n"
    "v_and_b32 v249, s80, v142\n"
    "v_lshlrev_b32 v250, 16, v143\n"
    "v_and_b32 v251, s80, v143\n"
    "v_pk_mul_f32 v[244:245], v[244:245], s[76:77] op_sel_hi:[1,0]\n"
    "v_pk_mul_f32 v[246:247], v[246:247], s[76:77] op_sel_hi:[1,0]\n"
    "v_pk_mul_f32 v[248:249], v[248:249], s[76:77] op_sel_hi:[1,0]\n"
    "v_pk_mul_f32 v[250:251], v[250:251], s[76:77] op_sel_hi:[1,0]\n"
    "v_cvt_pk_bf16_f32 v244, v244, v245\n"
    "v_cvt_pk_bf16_f32 v246, v246, v247\n"
    "v_cvt_pk_bf16_f32 v248, v248, v249\n"
    "v_cvt_pk_bf16_f32 v250, v250, v251\n"
    "v_accvgpr_write_b32 a44, v244\n"
    "v_accvgpr_write_b32 a45, v246\n"
    "v_accvgpr_write_b32 a46, v248\n"
    "v_accvgpr_write_b32 a47, v250\n"
    "s_nop 1\n"
    "v_mfma_f32_16x16x32_bf16 v[160:163], a[80:83], a[32:35], -4.0\n"
    "v_exp_f32 v172, v172\n"
    "v_mfma_f32_16x16x32_bf16 v[160:163], a[84:87], a[36:39], v[160:163]\n"
    "v_mfma_f32_16x16x32_bf16 v[160:163], a[88:91], a[40:43], v[160:163]\n"
    "v_exp_f32 v173, v173\n"
    "v_mfma_f32_16x16x32_bf16 v[160:163], a[92:95], a[44:47], v[160:163]\n"
    "v_mfma_f32_16x16x32_bf16 v[164:167], a[108:111], a[44:47], -4.0\n"
    "v_exp_f32 v174, v174\n"
    "v_mfma_f32_16x16x32_bf16 v[164:167], a[104:107], a[40:43], v[164:167]\n"
    "v_cvt_pk_bf16_f32 v190, v172, v173\n"
    "v_mfma_f32_16x16x32_bf16 v[164:167], a[100:103], a[36:39], v[164:167]\n"
    "v_exp_f32 v175, v175\n"
    "v_mfma_f32_16x16x32_bf16 v[164:167], a[96:99], a[32:35], v[164:167]\n"
    "v_cvt_pk_bf16_f32 v191, v174, v175\n"
    "v_mfma_f32_16x16x32_bf16 v[168:171], a[112:115], a[32:35], -4.0\n"
    "v_mfma_f32_16x16x32_bf16 v[168:171], a[116:119], a[36:39], v[168:171]\n"
    "v_exp_f32 v160, v160\n"
    "v_mfma_f32_16x16x32_bf16 v[168:171], a[120:123], a[40:43], v[168:171]\n"
    "v_mfma_f32_16x16x32_bf16 v[168:171], a[124:127], a[44:47], v[168:171]\n"
    "v_exp_f32 v161, v161\n"
    "v_mfma_f32_16x16x32_bf16 v[172:175], a[140:143], a[44:47], -4.0\n"
    "v_mfma_f32_16x16x32_bf16 v[172:175], a[136:139], a[40:43], v[172:175]\n"
    "v_exp_f32 v162, v162\n"
    "v_mfma_f32_16x16x32_bf16 v[172:175], a[132:135], a[36:39], v[172:175]\n"
    "v_cvt_pk_bf16_f32 v176, v160, v161\n"
    "v_mfma_f32_16x16x32_bf16 v[172:175], a[128:131], a[32:35], v[172:175]\n"
    "v_exp_f32 v163, v163\n"
    "v_smfmac_f32_16x16x64_bf16 v[196:199], a[240:243], v[184:191], v237\n"
    "ds_read_b64_tr_b16 a[176:177], v216 offset:41536\n"
    "v_cvt_pk_bf16_f32 v177, v162, v163\n"
    "v_mfma_f32_16x16x32_bf16 v[32:35], a[208:211], v[188:191], 0\n"
    "v_exp_f32 v164, v164\n"
    "v_mfma_f32_16x16x32_bf16 v[32:35], a[144:147], v[184:187], v[32:35]\n"
    "ds_read_b64_tr_b16 a[178:179], v216 offset:45664\n"
    "ds_read_b64_tr_b16 a[180:181], v216 offset:41544\n"
    "v_mfma_f32_16x16x32_bf16 v[36:39], a[148:151], v[184:187], 0\n"
    "v_exp_f32 v165, v165\n"
    "v_mfma_f32_16x16x32_bf16 v[36:39], a[212:215], v[188:191], v[36:39]\n"
    "v_mfma_f32_16x16x32_bf16 v[40:43], a[216:219], v[188:191], 0\n"
    "v_exp_f32 v166, v166\n"
    "v_mfma_f32_16x16x32_bf16 v[40:43], a[152:155], v[184:187], v[40:43]\n"
    "ds_read_b64_tr_b16 a[182:183], v216 offset:45672\n"
    "v_cvt_pk_bf16_f32 v178, v164, v165\n"
    "v_mfma_f32_16x16x32_bf16 v[44:47], a[156:159], v[184:187], 0\n"
    "v_exp_f32 v167, v167\n"
    "v_mfma_f32_16x16x32_bf16 v[44:47], a[220:223], v[188:191], v[44:47]\n"
    "ds_read_b64_tr_b16 a[184:185], v217 offset:41536\n"
    "v_cvt_pk_bf16_f32 v179, v166, v167\n"
    "v_mfma_f32_16x16x32_bf16 v[48:51], a[224:227], v[188:191], 0\n"
    "v_exp_f32 v168, v168\n"
    "v_mfma_f32_16x16x32_bf16 v[48:51], a[160:163], v[184:187], v[48:51]\n"
    "ds_read_b64_tr_b16 a[186:187], v217 offset:45664\n"
    "v_mfma_f32_16x16x32_bf16 v[52:55], a[164:167], v[184:187], 0\n"
    "v_exp_f32 v169, v169\n"
    "v_mfma_f32_16x16x32_bf16 v[52:55], a[228:231], v[188:191], v[52:55]\n"
    "v_cvt_pk_bf16_f32 v180, v168, v169\n"
    "v_mfma_f32_16x16x32_bf16 v[56:59], a[232:235], v[188:191], 0\n"
    "v_exp_f32 v170, v170\n"
    "v_mfma_f32_16x16x32_bf16 v[56:59], a[168:171], v[184:187], v[56:59]\n"
    "v_mfma_f32_16x16x32_bf16 v[60:63], a[172:175], v[184:187], 0\n"
    "v_exp_f32 v171, v171\n"
    "v_mfma_f32_16x16x32_bf16 v[60:63], a[236:239], v[188:191], v[60:63]\n"
    "ds_read_b64_tr_b16 a[188:189], v217 offset:41544\n"
    "v_cvt_pk_bf16_f32 v181, v170, v171\n"
    "s_waitcnt vmcnt(8)\n"
    "ds_read_b128 v[144:147], v220 offset:12288\n"
    "ds_read_b128 v[148:151], v221 offset:12288\n"
    "ds_read_b128 v[152:155], v222 offset:12288\n"
    "ds_read_b128 v[156:159], v223 offset:12288\n"
    "s_waitcnt lgkmcnt(3)\n"
    "v_lshlrev_b32 v244, 16, v144\n"
    "s_add_u32 m0, s58, 24768\n"
    "s_nop 0\n"
    "buffer_load_dwordx4 v229, s[40:43], s54 offen lds\n"
    "v_and_b32 v245, s80, v144\n"
    "v_lshlrev_b32 v246, 16, v145\n"
    "v_and_b32 v247, s80, v145\n"
    "v_lshlrev_b32 v248, 16, v146\n"
    "v_and_b32 v249, s80, v146\n"
    "v_lshlrev_b32 v250, 16, v147\n"
    "v_and_b32 v251, s80, v147\n"
    "v_pk_mul_f32 v[244:245], v[244:245], s[76:77] op_sel_hi:[1,0]\n"
    "v_pk_mul_f32 v[246:247], v[246:247], s[76:77] op_sel_hi:[1,0]\n"
    "v_pk_mul_f32 v[248:249], v[248:249], s[76:77] op_sel_hi:[1,0]\n"
    "v_pk_mul_f32 v[250:251], v[250:251], s[76:77] op_sel_hi:[1,0]\n"
    "v_cvt_pk_bf16_f32 v244, v244, v245\n"
    "v_cvt_pk_bf16_f32 v246, v246, v247\n"
    "v_cvt_pk_bf16_f32 v248, v248, v249\n"
    "v_cvt_pk_bf16_f32 v250, v250, v251\n"
    "v_accvgpr_write_b32 a48, v244\n"
    "s_add_u32 m0, s58, 24776\n"
    "s_nop 0\n"
    "buffer_load_dwordx4 v229, s[40:43], s54 offen offset:1024 lds\n"
    "s_add_u32 s54, s54, 16384\n"
    "v_accvgpr_write_b32 a49, v246\n"
    "v_accvgpr_write_b32 a50, v248\n"
    "v_accvgpr_write_b32 a51, v250\n"
    "s_waitcnt lgkmcnt(2)\n"
    "v_lshlrev_b32 v244, 16, v148\n"
    "v_and_b32 v245, s80, v148\n"
    "v_lshlrev_b32 v246, 16, v149\n"
    "v_and_b32 v247, s80, v149\n"
    "v_lshlrev_b32 v248, 16, v150\n"
    "v_and_b32 v249, s80, v150\n"
    "v_lshlrev_b32 v250, 16, v151\n"
    "v_and_b32 v251, s80, v151\n"
    "v_pk_mul_f32 v[244:245], v[244:245], s[76:77] op_sel_hi:[1,0]\n"
    "v_pk_mul_f32 v[246:247], v[246:247], s[76:77] op_sel_hi:[1,0]\n"
    "v_pk_mul_f32 v[248:249], v[248:249], s[76:77] op_sel_hi:[1,0]\n"
    "v_pk_mul_f32 v[250:251], v[250:251], s[76:77] op_sel_hi:[1,0]\n"
    "v_cvt_pk_bf16_f32 v244, v244, v245\n"
    "s_getpc_b64 s[84:85]\n"
    "LPF%=:\n"
    "s_and_b32 s85, s85, 0xffff\n"
    "s_mov_b32 s86, LEND%=-LPF%=\n"
    "s_mov_b32 s87, 0x20000\n"
    "s_lshl_b32 s83, %[w], 13\n"
    "v_add_u32 v236, s83, v236\n"
    "buffer_load_dword v235, v236, s[84:87], 0 offen\n"
    "v_add_u32 v236, LTAIL%=-LPF%=, v236\n"
    "v_cvt_pk_bf16_f32 v246, v246, v247\n"
    "v_cvt_pk_bf16_f32 v248, v248, v249\n"
    "v_cvt_pk_bf16_f32 v250, v250, v251\n"
    "v_accvgpr_write_b32 a52, v244\n"
    "v_accvgpr_write_b32 a53, v246\n"
    "v_accvgpr_write_b32 a54, v248\n"
    "v_accvgpr_write_b32 a55, v250\n"
    "s_waitcnt lgkmcnt(1)\n"
    "v_lshlrev_b32 v244, 16, v152\n"
    "v_and_b32 v245, s80, v152\n"
    "v_lshlrev_b32 v246, 16, v153\n"
    "v_and_b32 v247, s80, v153\n"
    "v_lshlrev_b32 v248, 16, v154\n"
    "v_and_b32 v249, s80, v154\n"
    "v_lshlrev_b32 v250, 16, v155\n"
    "v_and_b32 v251, s80, v155\n"
    "v_pk_mul_f32 v[244:245], v[244:245], s[76:77] op_sel_hi:[1,0]\n"
    "v_pk_mul_f32 v[246:247], v[246:247], s[76:77] op_sel_hi:[1,0]\n"
    "v_pk_mul_f32 v[248:249], v[248:249], s[76:77] op_sel_hi:[1,0]\n"
    "v_pk_mul_f32 v[250:251], v[250:251], s[76:77] op_sel_hi:[1,0]\n"
    "v_cvt_pk_bf16_f32 v244, v244, v245\n"
    "v_cvt_pk_bf16_f32 v246, v246, v247\n"
    "v_cvt_pk_bf16_f32 v248, v248, v249\n"
    "v_cvt_pk_bf16_f32 v250, v250, v251\n"
    "v_accvgpr_write_b32 a56, v244\n"
    "v_accvgpr_write_b32 a57, v246\n"
    "v_accvgpr_write_b32 a58, v248\n"
    "v_accvgpr_write_b32 a59, v250\n"
    "s_waitcnt lgkmcnt(0)\n"
    "v_lshlrev_b32 v244, 16, v156\n"
    "v_and_b32 v245, s80, v156\n"
    "v_lshlrev_b32 v246, 16, v157\n"
    "v_and_b32 v247, s80, v157\n"
    "v_lshlrev_b32 v248, 16, v158\n"
    "v_and_b32 v249, s80, v158\n"
    "v_lshlrev_b32 v250, 16, v159\n"
    "v_and_b32 v251, s80, v159\n"
    "v_pk_mul_f32 v[244:245], v[244:245], s[76:77] op_sel_hi:[1,0]\n"
    "v_pk_mul_f32 v[246:247], v[246:247], s[76:77] op_sel_hi:[1,0]\n"
    "v_pk_mul_f32 v[248:249], v[248:249], s[76:77] op_sel_hi:[1,0]\n"
    "v_pk_mul_f32 v[250:251], v[250:251], s[76:77] op_sel_hi:[1,0]\n"
    "v_cvt_pk_bf16_f32 v244, v244, v245\n"
    "v_cvt_pk_bf16_f32 v246, v246, v247\n"
    "v_cvt_pk_bf16_f32 v248, v248, v249\n"
    "v_cvt_pk_bf16_f32 v250, v250, v251\n"
    "v_accvgpr_write_b32 a60, v244\n"
    "v_accvgpr_write_b32 a61, v246\n"
    "v_accvgpr_write_b32 a62, v248\n"
    "v_accvgpr_write_b32 a63, v250\n"
    "s_nop 1\n"
    "v_mfma_f32_16x16x32_bf16 v[160:163], a[80:83], a[48:51], -4.0\n"
    "v_exp_f32 v172, v172\n"
    "v_mfma_f32_16x16x32_bf16 v[160:163], a[84:87], a[52:55], v[160:163]\n"
    "ds_read_b64_tr_b16 a[190:191], v217 offset:45672\n"
    "ds_read_b64_tr_b16 a[192:193], v218 offset:41536\n"
    "v_mfma_f32_16x16x32_bf16 v[160:163], a[88:91], a[56:59], v[160:163]\n"
    "v_exp_f32 v173, v173\n"
    "v_mfma_f32_16x16x32_bf16 v[160:163], a[92:95], a[60:63], v[160:163]\n"
    "v_cvt_pk_bf16_f32 v182, v172, v173\n"
    "v_mfma_f32_16x16x32_bf16 v[164:167], a[108:111], a[60:63], -4.0\n"
    "v_exp_f32 v174, v174\n"
    "v_mfma_f32_16x16x32_bf16 v[164:167], a[104:107], a[56:59], v[164:167]\n"
    "v_mfma_f32_16x16x32_bf16 v[164:167], a[100:103], a[52:55], v[164:167]\n"
    "v_exp_f32 v175, v175\n"
    "v_mfma_f32_16x16x32_bf16 v[164:167], a[96:99], a[48:51], v[164:167]\n"
    "v_mfma_f32_16x16x32_bf16 v[168:171], a[112:115], a[48:51], -4.0\n"
    "v_cvt_pk_bf16_f32 v183, v174, v175\n"
    "v_mfma_f32_16x16x32_bf16 v[168:171], a[116:119], a[52:55], v[168:171]\n"
    "v_exp_f32 v160, v160\n"
    "v_mfma_f32_16x16x32_bf16 v[168:171], a[120:123], a[56:59], v[168:171]\n"
    "v_mfma_f32_16x16x32_bf16 v[168:171], a[124:127], a[60:63], v[168:171]\n"
    "v_exp_f32 v161, v161\n"
    "v_mfma_f32_16x16x32_bf16 v[172:175], a[140:143], a[60:63], -4.0\n"
    "ds_read_b64_tr_b16 a[194:195], v218 offset:45664\n"
    "ds_read_b64_tr_b16 a[196:197], v218 offset:41544\n"
    "v_mfma_f32_16x16x32_bf16 v[172:175], a[136:139], a[56:59], v[172:175]\n"
    "v_exp_f32 v162, v162\n"
    "v_mfma_f32_16x16x32_bf16 v[172:175], a[132:135], a[52:55], v[172:175]\n"
    "v_mfma_f32_16x16x32_bf16 v[172:175], a[128:131], a[48:51], v[172:175]\n"
    "v_exp_f32 v163, v163\n"
    "v_smfmac_f32_16x16x64_bf16 v[200:203], a[240:243], v[176:183], v237\n"
    "ds_read_b64_tr_b16 a[198:199], v218 offset:45672\n"
    "v_mfma_f32_16x16x32_bf16 v[64:67], a[208:211], v[180:183], 0\n"
    "v_exp_f32 v164, v164\n"
    "v_mfma_f32_16x16x32_bf16 v[64:67], a[144:147], v[176:179], v[64:67]\n"
    "ds_read_b64_tr_b16 a[200:201], v219 offset:41536\n"
    "v_mfma_f32_16x16x32_bf16 v[68:71], a[148:151], v[176:179], 0\n"
    "v_exp_f32 v165, v165\n"
    "v_mfma_f32_16x16x32_bf16 v[68:71], a[212:215], v[180:183], v[68:71]\n"
    "v_mfma_f32_16x16x32_bf16 v[72:75], a[216:219], v[180:183], 0\n"
    "v_exp_f32 v166, v166\n"
    "v_mfma_f32_16x16x32_bf16 v[72:75], a[152:155], v[176:179], v[72:75]\n"
    "v_mfma_f32_16x16x32_bf16 v[76:79], a[156:159], v[176:179], 0\n"
    "v_exp_f32 v167, v167\n"
    "v_mfma_f32_16x16x32_bf16 v[76:79], a[220:223], v[180:183], v[76:79]\n"
    "ds_read_b64_tr_b16 a[202:203], v219 offset:45664\n"
    "ds_read_b64_tr_b16 a[204:205], v219 offset:41544\n"
    "v_mfma_f32_16x16x32_bf16 v[80:83], a[224:227], v[180:183], 0\n"
    "s_add_u32 m0, s58, 0\n"
    "s_nop 0\n"
    "buffer_load_dwordx4 v229, s[40:43], s53 offen lds\n"
    "v_exp_f32 v168, v168\n"
    "v_mfma_f32_16x16x32_bf16 v[80:83], a[160:163], v[176:179], v[80:83]\n"
    "ds_read_b64_tr_b16 a[206:207], v219 offset:45672\n"
    "v_cvt_pk_bf16_f32 v184, v160, v161\n"
    "v_mfma_f32_16x16x32_bf16 v[84:87], a[164:167], v[176:179], 0\n"
    "v_exp_f32 v169, v169\n"
    "v_mfma_f32_16x16x32_bf16 v[84:87], a[228:231], v[180:183], v[84:87]\n"
    "v_cvt_pk_bf16_f32 v185, v162, v163\n"
    "v_cvt_pk_bf16_f32 v186, v164, v165\n"
    "v_mfma_f32_16x16x32_bf16 v[88:91], a[232:235], v[180:183], 0\n"
    "s_add_u32 m0, s58, 8\n"
    "s_nop 0\n"
    "buffer_load_dwordx4 v229, s[40:43], s53 offen offset:1024 lds\n"
    "s_add_u32 s53, s53, 16384\n"
    "s_mov_b32 s55, s26\n"
    "v_exp_f32 v170, v170\n"
    "v_mfma_f32_16x16x32_bf16 v[88:91], a[168:171], v[176:179], v[88:91]\n"
    "v_cvt_pk_bf16_f32 v187, v166, v167\n"
    "v_cvt_pk_bf16_f32 v188, v168, v169\n"
    "v_mfma_f32_16x16x32_bf16 v[92:95], a[172:175], v[176:179], 0\n"
    "v_exp_f32 v171, v171\n"
    "v_mfma_f32_16x16x32_bf16 v[92:95], a[236:239], v[180:183], v[92:95]\n"
    "v_cvt_pk_bf16_f32 v189, v170, v171\n"
    "s_waitcnt vmcnt(9)\n"
    "ds_read_b128 v[128:131], v220 offset:16384\n"
    "ds_read_b128 v[132:135], v221 offset:16384\n"
    "ds_read_b128 v[136:139], v222 offset:16384\n"
    "ds_read_b128 v[140:143], v223 offset:16384\n"
    "s_waitcnt lgkmcnt(3)\n"
    "v_lshlrev_b32 v244, 16, v128\n"
    "v_and_b32 v245, s80, v128\n"
    "v_lshlrev_b32 v246, 16, v129\n"
    "v_and_b32 v247, s80, v129\n"
    "v_lshlrev_b32 v248, 16, v130\n"
    "v_and_b32 v249, s80, v130\n"
    "v_lshlrev_b32 v250, 16, v131\n"
    "v_and_b32 v251, s80, v131\n"
    "v_pk_mul_f32 v[244:245], v[244:245], s[76:77] op_sel_hi:[1,0]\n"
    "v_pk_mul_f32 v[246:247], v[246:247], s[76:77] op_sel_hi:[1,0]\n"
    "v_pk_mul_f32 v[248:249], v[248:249], s[76:77] op_sel_hi:[1,0]\n"
    "v_pk_mul_f32 v[250:251], v[250:251], s[76:77] op_sel_hi:[1,0]\n"
    "v_cvt_pk_bf16_f32 v244, v244, v245\n"
    "v_cvt_pk_bf16_f32 v246, v246, v247\n"
    "v_cvt_pk_bf16_f32 v248, v248, v249\n"
    "v_cvt_pk_bf16_f32 v250, v250, v251\n"
    "v_accvgpr_write_b32 a64, v244\n"
    "v_accvgpr_write_b32 a65, v246\n"
    "v_accvgpr_write_b32 a66, v248\n"
    "v_accvgpr_write_b32 a67, v250\n"
    "s_waitcnt lgkmcnt(2)\n"
    "v_lshlrev_b32 v244, 16, v132\n"
    "v_and_b32 v245, s80, v132\n"
    "v_lshlrev_b32 v246, 16, v133\n"
    "v_and_b32 v247, s80, v133\n"
    "v_lshlrev_b32 v248, 16, v134\n"
    "v_and_b32 v249, s80, v134\n"
    "v_lshlrev_b32 v250, 16, v135\n"
    "v_and_b32 v251, s80, v135\n"
    "v_pk_mul_f32 v[244:245], v[244:245], s[76:77] op_sel_hi:[1,0]\n"
    "v_pk_mul_f32 v[246:247], v[246:247], s[76:77] op_sel_hi:[1,0]\n"
    "v_pk_mul_f32 v[248:249], v[248:249], s[76:77] op_sel_hi:[1,0]\n"
    "v_pk_mul_f32 v[250:251], v[250:251], s[76:77] op_sel_hi:[1,0]\n"
    "v_cvt_pk_bf16_f32 v244, v244, v245\n"
    "v_cvt_pk_bf16_f32 v246, v246, v247\n"
    "v_cvt_pk_bf16_f32 v248, v248, v249\n"
    "v_cvt_pk_bf16_f32 v250, v250, v251\n"
    "v_accvgpr_write_b32 a68, v244\n"
    "v_accvgpr_write_b32 a69, v246\n"
    "v_accvgpr_write_b32 a70, v248\n"
    "v_accvgpr_write_b32 a71, v250\n"
    "s_waitcnt lgkmcnt(1)\n"
    "v_lshlrev_b32 v244, 16, v136\n"
    "v_and_b32 v245, s80, v136\n"
    "v_lshlrev_b32 v246, 16, v137\n"
    "v_and_b32 v247, s80, v137\n"
    "v_lshlrev_b32 v248, 16, v138\n"
    "v_and_b32 v249, s80, v138\n"
    "v_lshlrev_b32 v250, 16, v139\n"
    "v_and_b32 v251, s80, v139\n"
    "v_pk_mul_f32 v[244:245], v[244:245], s[76:77] op_sel_hi:[1,0]\n"
    "v_pk_mul_f32 v[246:247], v[246:247], s[76:77] op_sel_hi:[1,0]\n"
    "v_pk_mul_f32 v[248:249], v[248:249], s[76:77] op_sel_hi:[1,0]\n"
    "v_pk_mul_f32 v[250:251], v[250:251], s[76:77] op_sel_hi:[1,0]\n"
    "v_cvt_pk_bf16_f32 v244, v244, v245\n"
    "v_cvt_pk_bf16_f32 v246, v246, v247\n"
    "v_cvt_pk_bf16_f32 v248, v248, v249\n"
    "v_cvt_pk_bf16_f32 v250, v250, v251\n"
    "v_accvgpr_write_b32 a72, v244\n"
    "v_accvgpr_write_b32 a73, v246\n"
    "v_accvgpr_write_b32 a74, v248\n"
    "v_accvgpr_write_b32 a75, v250\n"
    "s_waitcnt lgkmcnt(0)\n"
    "v_lshlrev_b32 v244, 16, v140\n"
    "v_and_b32 v245, s80, v140\n"
    "v_lshlrev_b32 v246, 16, v141\n"
    "v_and_b32 v247, s80, v141\n"
    "v_lshlrev_b32 v248, 16, v142\n"
    "v_and_b32 v249, s80, v142\n"
    "v_lshlrev_b32 v250, 16, v143\n"
    "v_and_b32 v251, s80, v143\n"
    "v_pk_mul_f32 v[244:245], v[244:245], s[76:77] op_sel_hi:[1,0]\n"
    "v_pk_mul_f32 v[246:247], v[246:247], s[76:77] op_sel_hi:[1,0]\n"
    "v_pk_mul_f32 v[248:249], v[248:249], s[76:77] op_sel_hi:[1,0]\n"
    "v_pk_mul_f32 v[250:251], v[250:251], s[76:77] op_sel_hi:[1,0]\n"
    "v_cvt_pk_bf16_f32 v244, v244, v245\n"
    "v_cvt_pk_bf16_f32 v246, v246, v247\n"
    "v_cvt_pk_bf16_f32 v248, v248, v249\n"
    "v_cvt_pk_bf16_f32 v250, v250, v251\n"
    "v_accvgpr_write_b32 a76, v244\n"
    "v_accvgpr_write_b32 a77, v246\n"
    "v_accvgpr_write_b32 a78, v248\n"
    "v_accvgpr_write_b32 a79, v250\n"
    "s_nop 1\n"
    "v_mfma_f32_16x16x32_bf16 v[160:163], a[80:83], a[64:67], -4.0\n"
    "v_exp_f32 v172, v172\n"
    "v_mfma_f32_16x16x32_bf16 v[160:163], a[84:87], a[68:71], v[160:163]\n"
    "s_add_u32 s56, s64, 0\n"
    "buffer_store_dwordx4 a[244:247], v228, s[48:51], s56 offen nt\n"
    "v_mfma_f32_16x16x32_bf16 v[160:163], a[88:91], a[72:75], v[160:163]\n"
    "v_exp_f32 v173, v173\n"
    "v_mfma_f32_16x16x32_bf16 v[160:163], a[92:95], a[76:79], v[160:163]\n"
    "v_cvt_pk_bf16_f32 v190, v172, v173\n"
    "v_mfma_f32_16x16x32_bf16 v[164:167], a[108:111], a[76:79], -4.0\n"
    "v_exp_f32 v174, v174\n"
    "v_mfma_f32_16x16x32_bf16 v[164:167], a[104:107], a[72:75], v[164:167]\n"
    "s_add_u32 m0, s63, 20480\n"
    "s_nop 0\n"
    "buffer_load_dwordx4 v224, s[44:47], s55 offen lds\n"
    "v_mfma_f32_16x16x32_bf16 v[164:167], a[100:103], a[68:71], v[164:167]\n"
    "v_exp_f32 v175, v175\n"
    "v_mfma_f32_16x16x32_bf16 v[164:167], a[96:99], a[64:67], v[164:167]\n"
    "s_waitcnt vmcnt(5)\n"
    "v_cvt_pk_bf16_f32 v191, v174, v175\n"
    "v_mfma_f32_16x16x32_bf16 v[168:171], a[112:115], a[64:67], -4.0\n"
    "s_barrier\n"
    "v_mfma_f32_16x16x32_bf16 v[168:171], a[116:119], a[68:71], v[168:171]\n"
    "v_exp_f32 v160, v160\n"
    "v_mfma_f32_16x16x32_bf16 v[168:171], a[120:123], a[72:75], v[168:171]\n"
    "s_add_u32 m0, s57, 0\n"
    "s_nop 0\n"
    "buffer_load_dwordx4 v229, s[36:39], s52 offen lds\n"
    "v_mfma_f32_16x16x32_bf16 v[168:171], a[124:127], a[76:79], v[168:171]\n"
    "v_exp_f32 v161, v161\n"
    "v_mfma_f32_16x16x32_bf16 v[172:175], a[140:143], a[76:79], -4.0\n"
    "v_mfma_f32_16x16x32_bf16 v[172:175], a[136:139], a[72:75], v[172:175]\n"
    "v_exp_f32 v162, v162\n"
    "v_mfma_f32_16x16x32_bf16 v[172:175], a[132:135], a[68:71], v[172:175]\n"
    "s_add_u32 m0, s57, 0\n"
    "s_nop 0\n"
    "buffer_load_dwordx4 v229, s[36:39], s52 offen offset:1024 lds\n"
    "ds_read_b128 a[80:83], v212 offset:16640\n"
    "ds_read_b128 a[84:87], v213 offset:16640\n"
    "v_mfma_f32_16x16x32_bf16 v[172:175], a[128:131], a[64:67], v[172:175]\n"
    "v_exp_f32 v163, v163\n"
    "v_smfmac_f32_16x16x64_bf16 v[204:207], a[240:243], v[184:191], v237\n"
    "ds_read_b128 a[88:91], v214 offset:16640\n"
    "v_mfma_f32_16x16x32_bf16 v[96:99], a[208:211], v[188:191], 0\n"
    "v_exp_f32 v164, v164\n"
    "v_mfma_f32_16x16x32_bf16 v[96:99], a[144:147], v[184:187], v[96:99]\n"
    "s_add_u32 m0, s57, 32\n"
    "s_nop 0\n"
    "buffer_load_dwordx4 v229, s[36:39], s52 offen offset:2048 lds\n"
    "v_mfma_f32_16x16x32_bf16 v[100:103], a[148:151], v[184:187], 0\n"
    "v_exp_f32 v165, v165\n"
    "v_mfma_f32_16x16x32_bf16 v[100:103], a[212:215], v[188:191], v[100:103]\n"
    "ds_read_b128 a[92:95], v215 offset:16640\n"
    "v_mfma_f32_16x16x32_bf16 v[104:107], a[216:219], v[188:191], 0\n"
    "v_exp_f32 v166, v166\n"
    "v_mfma_f32_16x16x32_bf16 v[104:107], a[152:155], v[184:187], v[104:107]\n"
    "buffer_store_dwordx4 a[248:251], v228, s[48:51], s56 offen offset:1024 nt\n"
    "ds_read_b128 a[108:111], v215 offset:20800\n"
    "ds_read_b128 a[104:107], v214 offset:20800\n"
    "v_mfma_f32_16x16x32_bf16 v[108:111], a[156:159], v[184:187], 0\n"
    "v_exp_f32 v167, v167\n"
    "v_mfma_f32_16x16x32_bf16 v[108:111], a[220:223], v[188:191], v[108:111]\n"
    "v_mfma_f32_16x16x32_bf16 v[112:115], a[224:227], v[188:191], 0\n"
    "v_exp_f32 v168, v168\n"
    "v_mfma_f32_16x16x32_bf16 v[112:115], a[160:163], v[184:187], v[112:115]\n"
    "s_add_u32 m0, s57, 32\n"
    "s_nop 0\n"
    "buffer_load_dwordx4 v229, s[36:39], s52 offen offset:3072 lds\n"
    "v_mfma_f32_16x16x32_bf16 v[116:119], a[164:167], v[184:187], 0\n"
    "v_exp_f32 v169, v169\n"
    "v_mfma_f32_16x16x32_bf16 v[116:119], a[228:231], v[188:191], v[116:119]\n"
    "ds_read_b128 a[100:103], v213 offset:20800\n"
    "ds_read_b128 a[96:99], v212 offset:20800\n"
    "v_mfma_f32_16x16x32_bf16 v[120:123], a[232:235], v[188:191], 0\n"
    "v_exp_f32 v170, v170\n"
    "v_mfma_f32_16x16x32_bf16 v[120:123], a[168:171], v[184:187], v[120:123]\n"
    "s_add_u32 m0, s58, 16512\n"
    "s_nop 0\n"
    "buffer_load_dwordx4 v229, s[40:43], s54 offen lds\n"
    "v_cvt_pk_bf16_f32 v176, v160, v161\n"
    "v_cvt_pk_bf16_f32 v177, v162, v163\n"
    "v_mfma_f32_16x16x32_bf16 v[124:127], a[172:175], v[184:187], 0\n"
    "v_exp_f32 v171, v171\n"
    "v_mfma_f32_16x16x32_bf16 v[124:127], a[236:239], v[188:191], v[124:127]\n"
    "s_waitcnt lgkmcnt(2)\n"
    "ds_read_b128 a[112:115], v212 offset:24960\n"
    "v_mfma_f32_16x16x32_bf16 v[160:163], a[80:83], a[0:3], -4.0\n"
    "ds_read_b128 a[116:119], v213 offset:24960\n"
    "v_exp_f32 v172, v172\n"
    "v_mfma_f32_16x16x32_bf16 v[160:163], a[84:87], a[4:7], v[160:163]\n"
    "s_add_u32 m0, s58, 16520\n"
    "s_nop 0\n"
    "buffer_load_dwordx4 v229, s[40:43], s54 offen offset:1024 lds\n"
    "ds_read_b128 a[120:123], v214 offset:24960\n"
    "ds_read_b128 a[124:127], v215 offset:24960\n"
    "v_mfma_f32_16x16x32_bf16 v[160:163], a[88:91], a[8:11], v[160:163]\n"
    "v_exp_f32 v173, v173\n"
    "v_mfma_f32_16x16x32_bf16 v[160:163], a[92:95], a[12:15], v[160:163]\n"
    "s_add_u32 m0, s63, 20480\n"
    "s_nop 0\n"
    "buffer_load_dwordx4 v225, s[44:47], s55 offen offset:1024 lds\n"
    "ds_read_b128 a[140:143], v215 offset:29120\n"
    "v_cvt_pk_bf16_f32 v178, v164, v165\n"
    "v_cvt_pk_bf16_f32 v179, v166, v167\n"
    "v_mfma_f32_16x16x32_bf16 v[164:167], a[108:111], a[12:15], -4.0\n"
    "ds_read_b128 a[136:139], v214 offset:29120\n"
    "v_exp_f32 v174, v174\n"
    "v_mfma_f32_16x16x32_bf16 v[164:167], a[104:107], a[8:11], v[164:167]\n"
    "s_waitcnt lgkmcnt(6)\n"
    "buffer_store_dwordx4 a[252:255], v228, s[48:51], s56 offen offset:2048 nt\n"
    "ds_read_b128 a[132:135], v213 offset:29120\n"
    "ds_read_b128 a[128:131], v212 offset:29120\n"
    "v_mfma_f32_16x16x32_bf16 v[164:167], a[100:103], a[4:7], v[164:167]\n"
    "v_exp_f32 v175, v175\n"
    "v_mfma_f32_16x16x32_bf16 v[164:167], a[96:99], a[0:3], v[164:167]\n"
    "s_waitcnt lgkmcnt(7)\n"
    "s_add_u32 m0, s63, 20480\n"
    "s_nop 0\n"
    "buffer_load_dwordx4 v226, s[44:47], s55 offen offset:2048 lds\n"
    "v_cvt_pk_bf16_f32 v180, v168, v169\n"
    "v_cvt_pk_bf16_f32 v181, v170, v171\n"
    "v_mfma_f32_16x16x32_bf16 v[168:171], a[112:115], a[0:3], -4.0\n"
    "s_waitcnt lgkmcnt(6)\n"
    "v_cvt_pk_bf16_f32 v182, v172, v173\n"
    "v_cvt_pk_bf16_f32 v183, v174, v175\n"
    "v_mfma_f32_16x16x32_bf16 v[168:171], a[116:119], a[4:7], v[168:171]\n"
    "s_waitcnt lgkmcnt(4)\n"
    "v_exp_f32 v160, v160\n"
    "v_mfma_f32_16x16x32_bf16 v[168:171], a[120:123], a[8:11], v[168:171]\n"
    "s_add_u32 m0, s58, 8256\n"
    "s_nop 0\n"
    "buffer_load_dwordx4 v229, s[40:43], s53 offen lds\n"
    "v_mfma_f32_16x16x32_bf16 v[168:171], a[124:127], a[12:15], v[168:171]\n"
    "s_waitcnt lgkmcnt(3)\n"
    "v_exp_f32 v161, v161\n"
    "v_mfma_f32_16x16x32_bf16 v[172:175], a[140:143], a[12:15], -4.0\n"
    "s_waitcnt lgkmcnt(2)\n"
    "s_add_u32 m0, s58, 8264\n"
    "s_nop 0\n"
    "buffer_load_dwordx4 v229, s[40:43], s53 offen offset:1024 lds\n"
    "v_mfma_f32_16x16x32_bf16 v[172:175], a[136:139], a[8:11], v[172:175]\n"
    "s_waitcnt lgkmcnt(0)\n"
    "v_exp_f32 v162, v162\n"
    "v_mfma_f32_16x16x32_bf16 v[172:175], a[132:135], a[4:7], v[172:175]\n"
    "v_mfma_f32_16x16x32_bf16 v[172:175], a[128:131], a[0:3], v[172:175]\n"
    "v_exp_f32 v163, v163\n"
    "v_smfmac_f32_16x16x64_bf16 v[208:211], a[240:243], v[176:183], v237\n"
    "v_mfma_f32_16x16x32_bf16 v[128:131], a[208:211], v[180:183], 0\n"
    "v_exp_f32 v164, v164\n"
    "v_mfma_f32_16x16x32_bf16 v[128:131], a[144:147], v[176:179], v[128:131]\n"
    "v_mfma_f32_16x16x32_bf16 v[132:135], a[148:151], v[176:179], 0\n"
    "v_exp_f32 v165, v165\n"
    "v_mfma_f32_16x16x32_bf16 v[132:135], a[212:215], v[180:183], v[132:135]\n"
    "v_mfma_f32_16x16x32_bf16 v[136:139], a[216:219], v[180:183], 0\n"
    "v_exp_f32 v166, v166\n"
    "v_mfma_f32_16x16x32_bf16 v[136:139], a[152:155], v[176:179], v[136:139]\n"
    "v_mfma_f32_16x16x32_bf16 v[140:143], a[156:159], v[176:179], 0\n"
    "v_exp_f32 v167, v167\n"
    "v_mfma_f32_16x16x32_bf16 v[140:143], a[220:223], v[180:183], v[140:143]\n"
    "v_mfma_f32_16x16x32_bf16 v[144:147], a[224:227], v[180:183], 0\n"
    "v_exp_f32 v168, v168\n"
    "v_mfma_f32_16x16x32_bf16 v[144:147], a[160:163], v[176:179], v[144:147]\n"
    "v_mfma_f32_16x16x32_bf16 v[148:151], a[164:167], v[176:179], 0\n"
    "v_exp_f32 v169, v169\n"
    "v_mfma_f32_16x16x32_bf16 v[148:151], a[228:231], v[180:183], v[148:151]\n"
    "v_cvt_pk_bf16_f32 v184, v160, v161\n"
    "v_cvt_pk_bf16_f32 v185, v162, v163\n"
    "v_mfma_f32_16x16x32_bf16 v[152:155], a[232:235], v[180:183], 0\n"
    "v_exp_f32 v170, v170\n"
    "v_mfma_f32_16x16x32_bf16 v[152:155], a[168:171], v[176:179], v[152:155]\n"
    "ds_read_b64_tr_b16 a[208:209], v216 offset:58048\n"
    "ds_read_b64_tr_b16 a[210:211], v216 offset:62176\n"
    "v_mfma_f32_16x16x32_bf16 v[156:159], a[172:175], v[176:179], 0\n"
    "v_exp_f32 v171, v171\n"
    "v_mfma_f32_16x16x32_bf16 v[156:159], a[236:239], v[180:183], v[156:159]\n"
    "ds_read_b64_tr_b16 a[212:213], v216 offset:58056\n"
    "ds_read_b64_tr_b16 a[214:215], v216 offset:62184\n"
    "LBODY%=:\n"
    "v_mfma_f32_16x16x32_bf16 v[160:163], a[80:83], a[16:19], -4.0\n"
    "v_exp_f32 v172, v172\n"
    "v_mfma_f32_16x16x32_bf16 v[160:163], a[84:87], a[20:23], v[160:163]\n"
    "s_mov_b32 s62, s65\n"
    "v_cvt_pk_bf16_f32 v186, v164, v165\n"
    "v_cvt_pk_bf16_f32 v187, v166, v167\n"
    "v_mfma_f32_16x16x32_bf16 v[160:163], a[88:91], a[24:27], v[160:163]\n"
    "v_exp_f32 v173, v173\n"
    "v_mfma_f32_16x16x32_bf16 v[160:163], a[92:95], a[28:31], v[160:163]\n"
    "s_mov_b32 s70, s71\n"
    "ds_read_b64_tr_b16 a[216:217], v217 offset:58048\n"
    "ds_read_b64_tr_b16 a[218:219], v217 offset:62176\n"
    "v_mfma_f32_16x16x32_bf16 v[164:167], a[108:111], a[28:31], -4.0\n"
    "v_exp_f32 v174, v174\n"
    "v_mfma_f32_16x16x32_bf16 v[164:167], a[104:107], a[24:27], v[164:167]\n"
    "s_mov_b32 s71, s72\n"
    "ds_read_b128 a[244:247], v224 offset:0\n"
    "ds_read_b64_tr_b16 a[220:221], v217 offset:58056\n"
    "v_mfma_f32_16x16x32_bf16 v[164:167], a[100:103], a[20:23], v[164:167]\n"
    "ds_read_b64_tr_b16 a[222:223], v217 offset:62184\n"
    "v_exp_f32 v175, v175\n"
    "v_mfma_f32_16x16x32_bf16 v[164:167], a[96:99], a[16:19], v[164:167]\n"
    "s_mov_b32 s72, s73\n"
    "ds_read_b64_tr_b16 a[224:225], v218 offset:58048\n"
    "v_cvt_pk_bf16_f32 v188, v168, v169\n"
    "v_cvt_pk_bf16_f32 v189, v170, v171\n"
    "v_mfma_f32_16x16x32_bf16 v[168:171], a[112:115], a[16:19], -4.0\n"
    "s_add_u32 s52, s52, 16384\n"
    "ds_read_b64_tr_b16 a[226:227], v218 offset:62176\n"
    "ds_read_b64_tr_b16 a[228:229], v218 offset:58056\n"
    "v_mfma_f32_16x16x32_bf16 v[168:171], a[116:119], a[20:23], v[168:171]\n"
    "s_mov_b32 s73, s74\n"
    "v_exp_f32 v160, v160\n"
    "v_mfma_f32_16x16x32_bf16 v[168:171], a[120:123], a[24:27], v[168:171]\n"
    "s_add_u32 s54, s54, 16384\n"
    "ds_read_b64_tr_b16 a[230:231], v218 offset:62184\n"
    "v_cvt_pk_bf16_f32 v190, v172, v173\n"
    "v_mfma_f32_16x16x32_bf16 v[168:171], a[124:127], a[28:31], v[168:171]\n"
    "s_waitcnt vmcnt(12)\n"
    "s_mov_b32 s74, s82\n"
    "ds_read_b64_tr_b16 a[232:233], v219 offset:58048\n"
    "ds_read_b128 v[244:247], v228 offset:20480\n"
    "v_exp_f32 v161, v161\n"
    "v_cvt_pk_bf16_f32 v191, v174, v175\n"
    "v_mfma_f32_16x16x32_bf16 v[172:175], a[140:143], a[28:31], -4.0\n"
    "s_add_u32 s53, s53, 16384\n"
    "ds_read_b64_tr_b16 a[234:235], v219 offset:62176\n"
    "ds_read_b64_tr_b16 a[236:237], v219 offset:58056\n"
    "v_mfma_f32_16x16x32_bf16 v[172:175], a[136:139], a[24:27], v[172:175]\n"
    "s_mov_b32 s65, s66\n"
    "v_exp_f32 v162, v162\n"
    "v_mfma_f32_16x16x32_bf16 v[172:175], a[132:135], a[20:23], v[172:175]\n"
    "s_add_u32 s56, s64, 16384\n"
    "ds_read_b64_tr_b16 a[238:239], v219 offset:62184\n"
    "v_cvt_pk_bf16_f32 v176, v160, v161\n"
    "v_mfma_f32_16x16x32_bf16 v[172:175], a[128:131], a[16:19], v[172:175]\n"
    "s_waitcnt lgkmcnt(8)\n"
    "s_mov_b32 s66, s67\n"
    "buffer_store_dwordx4 a[244:247], v228, s[48:51], s56 offen nt\n"
    "v_exp_f32 v163, v163\n"
    "v_smfmac_f32_16x16x64_bf16 v[192:195], a[240:243], v[184:191], v237\n"
    "v_cvt_pk_bf16_f32 v177, v162, v163\n"
    "v_mfma_f32_16x16x32_bf16 v[0:3], a[208:211], v[188:191], v[0:3]\n"
    "s_mov_b32 s67, s68\n"
    "v_exp_f32 v164, v164\n"
    "v_mfma_f32_16x16x32_bf16 v[0:3], a[176:179], v[184:187], v[0:3]\n"
    "v_mfma_f32_16x16x32_bf16 v[4:7], a[180:183], v[184:187], v[4:7]\n"
    "s_mov_b32 s68, s69\n"
    "s_add_u32 m0, s63, 17408\n"
    "v_exp_f32 v165, v165\n"
    "v_mfma_f32_16x16x32_bf16 v[4:7], a[212:215], v[188:191], v[4:7]\n"
    "s_waitcnt lgkmcnt(1)\n"
    "buffer_load_dwordx4 v227, s[44:47], s55 offen offset:3072 lds\n"
    "v_mfma_f32_16x16x32_bf16 v[8:11], a[216:219], v[188:191], v[8:11]\n"
    "s_add_u32 s55, s55, 4096\n"
    "v_exp_f32 v166, v166\n"
    "v_mfma_f32_16x16x32_bf16 v[8:11], a[184:187], v[184:187], v[8:11]\n"
    "v_lshlrev_b32 v238, 16, v244\n"
    "v_and_b32 v244, s80, v244\n"
    "v_mfma_f32_16x16x32_bf16 v[12:15], a[188:191], v[184:187], v[12:15]\n"
    "s_waitcnt lgkmcnt(0)\n"
    "v_exp_f32 v167, v167\n"
    "v_mfma_f32_16x16x32_bf16 v[12:15], a[220:223], v[188:191], v[12:15]\n"
    "s_barrier\n"
    "v_mfma_f32_16x16x32_bf16 v[16:19], a[224:227], v[188:191], v[16:19]\n"
    "s_add_u32 m0, s57, 16640\n"
    "v_exp_f32 v168, v168\n"
    "v_mfma_f32_16x16x32_bf16 v[16:19], a[192:195], v[184:187], v[16:19]\n"
    "buffer_load_dwordx4 v229, s[36:39], s52 offen lds\n"
    "v_mfma_f32_16x16x32_bf16 v[20:23], a[196:199], v[184:187], v[20:23]\n"
    "v_exp_f32 v169, v169\n"
    "v_mfma_f32_16x16x32_bf16 v[20:23], a[228:231], v[188:191], v[20:23]\n"
    "s_waitcnt vmcnt(7)\n"
    "ds_read_b128 v[248:251], v228 offset:21504\n"
    "v_cvt_pk_bf16_f32 v178, v164, v165\n"
    "v_mfma_f32_16x16x32_bf16 v[24:27], a[232:235], v[188:191], v[24:27]\n"
    "v_exp_f32 v170, v170\n"
    "v_mfma_f32_16x16x32_bf16 v[24:27], a[200:203], v[184:187], v[24:27]\n"
    "v_cvt_pk_bf16_f32 v179, v166, v167\n"
    "v_cvt_pk_bf16_f32 v180, v168, v169\n"
    "v_mfma_f32_16x16x32_bf16 v[28:31], a[204:207], v[184:187], v[28:31]\n"
    "v_exp_f32 v171, v171\n"
    "v_mfma_f32_16x16x32_bf16 v[28:31], a[236:239], v[188:191], v[28:31]\n"
    "v_cvt_pk_bf16_f32 v181, v170, v171\n"
    "v_lshlrev_b32 v239, 16, v245\n"
    "v_mfma_f32_16x16x32_bf16 v[160:163], a[80:83], a[32:35], -4.0\n"
    "v_exp_f32 v172, v172\n"
    "v_mfma_f32_16x16x32_bf16 v[160:163], a[84:87], a[36:39], v[160:163]\n"
    "v_and_b32 v245, s80, v245\n"
    "v_lshlrev_b32 v240, 16, v246\n"
    "v_mfma_f32_16x16x32_bf16 v[160:163], a[88:91], a[40:43], v[160:163]\n"
    "s_add_u32 m0, s57, 16640\n"
    "v_exp_f32 v173, v173\n"
    "v_mfma_f32_16x16x32_bf16 v[160:163], a[92:95], a[44:47], v[160:163]\n"
    "buffer_load_dwordx4 v229, s[36:39], s52 offen offset:1024 lds\n"
    "v_mfma_f32_16x16x32_bf16 v[164:167], a[108:111], a[44:47], -4.0\n"
    "v_exp_f32 v174, v174\n"
    "v_mfma_f32_16x16x32_bf16 v[164:167], a[104:107], a[40:43], v[164:167]\n"
    "v_cvt_pk_bf16_f32 v182, v172, v173\n"
    "v_and_b32 v246, s80, v246\n"
    "v_mfma_f32_16x16x32_bf16 v[164:167], a[100:103], a[36:39], v[164:167]\n"
    "v_exp_f32 v175, v175\n"
    "v_mfma_f32_16x16x32_bf16 v[164:167], a[96:99], a[32:35], v[164:167]\n"
    "v_cvt_pk_bf16_f32 v183, v174, v175\n"
    "v_lshlrev_b32 v241, 16, v247\n"
    "v_mfma_f32_16x16x32_bf16 v[168:171], a[112:115], a[32:35], -4.0\n"
    "v_and_b32 v247, s80, v247\n"
    "v_pk_mul_f32 v[238:239], v[238:239], s[76:77] op_sel_hi:[1,0]\n"
    "v_pk_mul_f32 v[244:245], v[244:245], s[76:77] op_sel_hi:[1,0]\n"
    "v_pk_mul_f32 v[240:241], v[240:241], s[76:77] op_sel_hi:[1,0]\n"
    "v_pk_mul_f32 v[246:247], v[246:247], s[76:77] op_sel_hi:[1,0]\n"
    "v_mfma_f32_16x16x32_bf16 v[168:171], a[116:119], a[36:39], v[168:171]\n"
    "v_exp_f32 v160, v160\n"
    "v_mfma_f32_16x16x32_bf16 v[168:171], a[120:123], a[40:43], v[168:171]\n"
    "v_cvt_pk_bf16_f32 v244, v238, v244\n"
    "v_cvt_pk_bf16_f32 v245, v239, v245\n"
    "v_mfma_f32_16x16x32_bf16 v[168:171], a[124:127], a[44:47], v[168:171]\n"
    "s_add_u32 m0, s57, 16672\n"
    "v_exp_f32 v161, v161\n"
    "v_mfma_f32_16x16x32_bf16 v[172:175], a[140:143], a[44:47], -4.0\n"
    "buffer_load_dwordx4 v229, s[36:39], s52 offen offset:2048 lds\n"
    "v_mfma_f32_16x16x32_bf16 v[172:175], a[136:139], a[40:43], v[172:175]\n"
    "v_exp_f32 v162, v162\n"
    "v_mfma_f32_16x16x32_bf16 v[172:175], a[132:135], a[36:39], v[172:175]\n"
    "v_cvt_pk_bf16_f32 v246, v240, v246\n"
    "v_cvt_pk_bf16_f32 v247, v241, v247\n"
    "v_mfma_f32_16x16x32_bf16 v[172:175], a[128:131], a[32:35], v[172:175]\n"
    "v_exp_f32 v163, v163\n"
    "v_smfmac_f32_16x16x64_bf16 v[196:199], a[240:243], v[176:183], v237\n"
    "ds_read_b128 a[248:251], v225 offset:1024\n"
    "v_cvt_pk_bf16_f32 v184, v160, v161\n"
    "v_mfma_f32_16x16x32_bf16 v[32:35], a[208:211], v[180:183], v[32:35]\n"
    "v_exp_f32 v164, v164\n"
    "v_mfma_f32_16x16x32_bf16 v[32:35], a[176:179], v[176:179], v[32:35]\n"
    "s_waitcnt lgkmcnt(1)\n"
    "v_cvt_pk_bf16_f32 v185, v162, v163\n"
    "v_lshlrev_b32 v238, 16, v248\n"
    "v_mfma_f32_16x16x32_bf16 v[36:39], a[180:183], v[176:179], v[36:39]\n"
    "s_add_u32 m0, s57, 16672\n"
    "v_exp_f32 v165, v165\n"
    "v_mfma_f32_16x16x32_bf16 v[36:39], a[212:215], v[180:183], v[36:39]\n"
    "buffer_load_dwordx4 v229, s[36:39], s52 offen offset:3072 lds\n"
    "v_mfma_f32_16x16x32_bf16 v[40:43], a[216:219], v[180:183], v[40:43]\n"
    "v_exp_f32 v166, v166\n"
    "v_mfma_f32_16x16x32_bf16 v[40:43], a[184:187], v[176:179], v[40:43]\n"
    "ds_read_b64_tr_b16 a[144:145], v216 offset:33280\n"
    "v_and_b32 v248, s80, v248\n"
    "v_mfma_f32_16x16x32_bf16 v[44:47], a[188:191], v[176:179], v[44:47]\n"
    "s_add_u32 m0, s63, 21504\n"
    "v_exp_f32 v167, v167\n"
    "v_mfma_f32_16x16x32_bf16 v[44:47], a[220:223], v[180:183], v[44:47]\n"
    "buffer_load_dwordx4 v224, s[44:47], s55 offen lds\n"
    "v_mfma_f32_16x16x32_bf16 v[48:51], a[224:227], v[180:183], v[48:51]\n"
    "s_waitcnt lgkmcnt(1)\n"
    "buffer_store_dwordx4 a[248:251], v228, s[48:51], s56 offen offset:1024 nt\n"
    "v_exp_f32 v168, v168\n"
    "v_mfma_f32_16x16x32_bf16 v[48:51], a[192:195], v[176:179], v[48:51]\n"
    "v_cvt_pk_bf16_f32 v186, v164, v165\n"
    "v_cvt_pk_bf16_f32 v187, v166, v167\n"
    "v_mfma_f32_16x16x32_bf16 v[52:55], a[196:199], v[176:179], v[52:55]\n"
    "v_exp_f32 v169, v169\n"
    "v_mfma_f32_16x16x32_bf16 v[52:55], a[228:231], v[180:183], v[52:55]\n"
    "ds_read_b64_tr_b16 a[146:147], v216 offset:37408\n"
    "ds_read_b64_tr_b16 a[148:149], v216 offset:33288\n"
    "v_mfma_f32_16x16x32_bf16 v[56:59], a[232:235], v[180:183], v[56:59]\n"
    "s_add_u32 m0, s58, 24768\n"
    "v_exp_f32 v170, v170\n"
    "v_mfma_f32_16x16x32_bf16 v[56:59], a[200:203], v[176:179], v[56:59]\n"
    "buffer_load_dwordx4 v229, s[40:43], s54 offen lds\n"
    "v_mfma_f32_16x16x32_bf16 v[60:63], a[204:207], v[176:179], v[60:63]\n"
    "v_exp_f32 v171, v171\n"
    "v_mfma_f32_16x16x32_bf16 v[60:63], a[236:239], v[180:183], v[60:63]\n"
    "v_cvt_pk_bf16_f32 v188, v168, v169\n"
    "v_cvt_pk_bf16_f32 v189, v170, v171\n"
    "v_mfma_f32_16x16x32_bf16 v[160:163], a[80:83], a[48:51], -4.0\n"
    "v_exp_f32 v172, v172\n"
    "v_mfma_f32_16x16x32_bf16 v[160:163], a[84:87], a[52:55], v[160:163]\n"
    "ds_read_b64_tr_b16 a[150:151], v216 offset:37416\n"
    "ds_read_b64_tr_b16 a[152:153], v217 offset:33280\n"
    "v_mfma_f32_16x16x32_bf16 v[160:163], a[88:91], a[56:59], v[160:163]\n"
    "v_exp_f32 v173, v173\n"
    "v_mfma_f32_16x16x32_bf16 v[160:163], a[92:95], a[60:63], v[160:163]\n"
    "ds_read_b64_tr_b16 a[154:155], v217 offset:37408\n"
    "v_cvt_pk_bf16_f32 v190, v172, v173\n"
    "v_mfma_f32_16x16x32_bf16 v[164:167], a[108:111], a[60:63], -4.0\n"
    "s_add_u32 m0, s58, 24776\n"
    "v_exp_f32 v174, v174\n"
    "v_mfma_f32_16x16x32_bf16 v[164:167], a[104:107], a[56:59], v[164:167]\n"
    "buffer_load_dwordx4 v229, s[40:43], s54 offen offset:1024 lds\n"
    "v_mfma_f32_16x16x32_bf16 v[164:167], a[100:103], a[52:55], v[164:167]\n"
    "v_exp_f32 v175, v175\n"
    "v_mfma_f32_16x16x32_bf16 v[164:167], a[96:99], a[48:51], v[164:167]\n"
    "ds_read_b64_tr_b16 a[156:157], v217 offset:33288\n"
    "v_cvt_pk_bf16_f32 v191, v174, v175\n"
    "v_mfma_f32_16x16x32_bf16 v[168:171], a[112:115], a[48:51], -4.0\n"
    "s_waitcnt vmcnt(12)\n"
    "ds_read_b128 v[252:255], v228 offset:22528\n"
    "ds_read_b64_tr_b16 a[158:159], v217 offset:37416\n"
    "v_mfma_f32_16x16x32_bf16 v[168:171], a[116:119], a[52:55], v[168:171]\n"
    "v_exp_f32 v160, v160\n"
    "v_mfma_f32_16x16x32_bf16 v[168:171], a[120:123], a[56:59], v[168:171]\n"
    "ds_read_b64_tr_b16 a[160:161], v218 offset:33280\n"
    "ds_read_b64_tr_b16 a[162:163], v218 offset:37408\n"
    "v_mfma_f32_16x16x32_bf16 v[168:171], a[124:127], a[60:63], v[168:171]\n"
    "v_exp_f32 v161, v161\n"
    "v_mfma_f32_16x16x32_bf16 v[172:175], a[140:143], a[60:63], -4.0\n"
    "ds_read_b64_tr_b16 a[164:165], v218 offset:33288\n"
    "ds_read_b64_tr_b16 a[166:167], v218 offset:37416\n"
    "v_mfma_f32_16x16x32_bf16 v[172:175], a[136:139], a[56:59], v[172:175]\n"
    "v_exp_f32 v162, v162\n"
    "v_mfma_f32_16x16x32_bf16 v[172:175], a[132:135], a[52:55], v[172:175]\n"
    "ds_read_b64_tr_b16 a[168:169], v219 offset:33280\n"
    "v_lshlrev_b32 v239, 16, v249\n"
    "v_mfma_f32_16x16x32_bf16 v[172:175], a[128:131], a[48:51], v[172:175]\n"
    "v_exp_f32 v163, v163\n"
    "v_smfmac_f32_16x16x64_bf16 v[200:203], a[240:243], v[184:191], v237\n"
    "ds_read_b64_tr_b16 a[170:171], v219 offset:37408\n"
    "v_and_b32 v249, s80, v249\n"
    "v_mfma_f32_16x16x32_bf16 v[64:67], a[208:211], v[188:191], v[64:67]\n"
    "v_exp_f32 v164, v164\n"
    "v_mfma_f32_16x16x32_bf16 v[64:67], a[176:179], v[184:187], v[64:67]\n"
    "ds_read_b64_tr_b16 a[172:173], v219 offset:33288\n"
    "v_lshlrev_b32 v240, 16, v250\n"
    "v_mfma_f32_16x16x32_bf16 v[68:71], a[180:183], v[184:187], v[68:71]\n"
    "v_exp_f32 v165, v165\n"
    "v_mfma_f32_16x16x32_bf16 v[68:71], a[212:215], v[188:191], v[68:71]\n"
    "ds_read_b64_tr_b16 a[174:175], v219 offset:37416\n"
    "v_cvt_pk_bf16_f32 v176, v160, v161\n"
    "v_mfma_f32_16x16x32_bf16 v[72:75], a[216:219], v[188:191], v[72:75]\n"
    "v_exp_f32 v166, v166\n"
    "v_mfma_f32_16x16x32_bf16 v[72:75], a[184:187], v[184:187], v[72:75]\n"
    "v_cvt_pk_bf16_f32 v177, v162, v163\n"
    "v_and_b32 v250, s80, v250\n"
    "v_mfma_f32_16x16x32_bf16 v[76:79], a[188:191], v[184:187], v[76:79]\n"
    "v_exp_f32 v167, v167\n"
    "v_mfma_f32_16x16x32_bf16 v[76:79], a[220:223], v[188:191], v[76:79]\n"
    "v_cvt_pk_bf16_f32 v178, v164, v165\n"
    "v_cvt_pk_bf16_f32 v179, v166, v167\n"
    "v_mfma_f32_16x16x32_bf16 v[80:83], a[224:227], v[188:191], v[80:83]\n"
    "v_exp_f32 v168, v168\n"
    "v_mfma_f32_16x16x32_bf16 v[80:83], a[192:195], v[184:187], v[80:83]\n"
    "ds_write_b128 v228, v[244:247] offset:0\n"
    "v_lshlrev_b32 v241, 16, v251\n"
    "v_and_b32 v251, s80, v251\n"
    "v_pk_mul_f32 v[238:239], v[238:239], s[76:77] op_sel_hi:[1,0]\n"
    "v_pk_mul_f32 v[248:249], v[248:249], s[76:77] op_sel_hi:[1,0]\n"
    "v_pk_mul_f32 v[240:241], v[240:241], s[76:77] op_sel_hi:[1,0]\n"
    "v_pk_mul_f32 v[250:251], v[250:251], s[76:77] op_sel_hi:[1,0]\n"
    "v_mfma_f32_16x16x32_bf16 v[84:87], a[196:199], v[184:187], v[84:87]\n"
    "v_exp_f32 v169, v169\n"
    "v_mfma_f32_16x16x32_bf16 v[84:87], a[228:231], v[188:191], v[84:87]\n"
    "v_cvt_pk_bf16_f32 v180, v168, v169\n"
    "v_cvt_pk_bf16_f32 v248, v238, v248\n"
    "v_mfma_f32_16x16x32_bf16 v[88:91], a[232:235], v[188:191], v[88:91]\n"
    "v_exp_f32 v170, v170\n"
    "v_mfma_f32_16x16x32_bf16 v[88:91], a[200:203], v[184:187], v[88:91]\n"
    "v_cvt_pk_bf16_f32 v249, v239, v249\n"
    "v_cvt_pk_bf16_f32 v250, v240, v250\n"
    "v_mfma_f32_16x16x32_bf16 v[92:95], a[204:207], v[184:187], v[92:95]\n"
    "v_exp_f32 v171, v171\n"
    "v_mfma_f32_16x16x32_bf16 v[92:95], a[236:239], v[188:191], v[92:95]\n"
    "v_cvt_pk_bf16_f32 v181, v170, v171\n"
    "v_cvt_pk_bf16_f32 v251, v241, v251\n"
    "v_mfma_f32_16x16x32_bf16 v[160:163], a[80:83], a[64:67], -4.0\n"
    "v_exp_f32 v172, v172\n"
    "v_mfma_f32_16x16x32_bf16 v[160:163], a[84:87], a[68:71], v[160:163]\n"
    "s_waitcnt lgkmcnt(0)\n"
    "v_lshlrev_b32 v238, 16, v252\n"
    "v_and_b32 v252, s80, v252\n"
    "v_mfma_f32_16x16x32_bf16 v[160:163], a[88:91], a[72:75], v[160:163]\n"
    "v_exp_f32 v173, v173\n"
    "v_mfma_f32_16x16x32_bf16 v[160:163], a[92:95], a[76:79], v[160:163]\n"
    "v_cvt_pk_bf16_f32 v182, v172, v173\n"
    "v_lshlrev_b32 v239, 16, v253\n"
    "v_mfma_f32_16x16x32_bf16 v[164:167], a[108:111], a[76:79], -4.0\n"
    "v_exp_f32 v174, v174\n"
    "v_mfma_f32_16x16x32_bf16 v[164:167], a[104:107], a[72:75], v[164:167]\n"
    "v_and_b32 v253, s80, v253\n"
    "v_lshlrev_b32 v240, 16, v254\n"
    "v_mfma_f32_16x16x32_bf16 v[164:167], a[100:103], a[68:71], v[164:167]\n"
    "s_add_u32 m0, s63, 21504\n"
    "v_exp_f32 v175, v175\n"
    "v_mfma_f32_16x16x32_bf16 v[164:167], a[96:99], a[64:67], v[164:167]\n"
    "buffer_load_dwordx4 v225, s[44:47], s55 offen offset:1024 lds\n"
    "v_mfma_f32_16x16x32_bf16 v[168:171], a[112:115], a[64:67], -4.0\n"
    "s_barrier\n"
    "v_mfma_f32_16x16x32_bf16 v[168:171], a[116:119], a[68:71], v[168:171]\n"
    "v_exp_f32 v160, v160\n"
    "v_mfma_f32_16x16x32_bf16 v[168:171], a[120:123], a[72:75], v[168:171]\n"
    "ds_read_b128 a[252:255], v226 offset:2048\n"
    "v_cvt_pk_bf16_f32 v183, v174, v175\n"
    "v_mfma_f32_16x16x32_bf16 v[168:171], a[124:127], a[76:79], v[168:171]\n"
    "v_exp_f32 v161, v161\n"
    "v_mfma_f32_16x16x32_bf16 v[172:175], a[140:143], a[76:79], -4.0\n"
    "ds_read_b128 a[80:83], v212 offset:0\n"
    "v_and_b32 v254, s80, v254\n"
    "v_mfma_f32_16x16x32_bf16 v[172:175], a[136:139], a[72:75], v[172:175]\n"
    "v_exp_f32 v162, v162\n"
    "v_mfma_f32_16x16x32_bf16 v[172:175], a[132:135], a[68:71], v[172:175]\n"
    "ds_read_b128 a[84:87], v213 offset:0\n"
    "ds_read_b128 a[88:91], v214 offset:0\n"
    "v_mfma_f32_16x16x32_bf16 v[172:175], a[128:131], a[64:67], v[172:175]\n"
    "v_exp_f32 v163, v163\n"
    "v_smfmac_f32_16x16x64_bf16 v[204:207], a[240:243], v[176:183], v237\n"
    "ds_read_b128 a[92:95], v215 offset:0\n"
    "ds_read_b128 a[108:111], v215 offset:4160\n"
    "v_mfma_f32_16x16x32_bf16 v[96:99], a[208:211], v[180:183], v[96:99]\n"
    "v_exp_f32 v164, v164\n"
    "v_mfma_f32_16x16x32_bf16 v[96:99], a[176:179], v[176:179], v[96:99]\n"
    "ds_read_b128 a[104:107], v214 offset:4160\n"
    "ds_read_b128 a[100:103], v213 offset:4160\n"
    "v_mfma_f32_16x16x32_bf16 v[100:103], a[180:183], v[176:179], v[100:103]\n"
    "s_waitcnt lgkmcnt(7)\n"
    "buffer_store_dwordx4 a[252:255], v228, s[48:51], s56 offen offset:2048 nt\n"
    "v_exp_f32 v165, v165\n"
    "v_mfma_f32_16x16x32_bf16 v[100:103], a[212:215], v[180:183], v[100:103]\n"
    "ds_read_b128 a[96:99], v212 offset:4160\n"
    "v_cvt_pk_bf16_f32 v184, v160, v161\n"
    "v_mfma_f32_16x16x32_bf16 v[104:107], a[216:219], v[180:183], v[104:107]\n"
    "s_add_u32 m0, s58, 0\n"
    "v_exp_f32 v166, v166\n"
    "v_mfma_f32_16x16x32_bf16 v[104:107], a[184:187], v[176:179], v[104:107]\n"
    "buffer_load_dwordx4 v229, s[40:43], s53 offen lds\n"
    "v_mfma_f32_16x16x32_bf16 v[108:111], a[188:191], v[176:179], v[108:111]\n"
    "v_exp_f32 v167, v167\n"
    "v_mfma_f32_16x16x32_bf16 v[108:111], a[220:223], v[180:183], v[108:111]\n"
    "ds_read_b128 a[112:115], v212 offset:8320\n"
    "v_cvt_pk_bf16_f32 v185, v162, v163\n"
    "v_mfma_f32_16x16x32_bf16 v[112:115], a[224:227], v[180:183], v[112:115]\n"
    "s_add_u32 m0, s58, 8\n"
    "v_exp_f32 v168, v168\n"
    "v_mfma_f32_16x16x32_bf16 v[112:115], a[192:195], v[176:179], v[112:115]\n"
    "buffer_load_dwordx4 v229, s[40:43], s53 offen offset:1024 lds\n"
    "v_mfma_f32_16x16x32_bf16 v[116:119], a[196:199], v[176:179], v[116:119]\n"
    "v_exp_f32 v169, v169\n"
    "v_mfma_f32_16x16x32_bf16 v[116:119], a[228:231], v[180:183], v[116:119]\n"
    "ds_read_b128 a[116:119], v213 offset:8320\n"
    "ds_read_b128 a[120:123], v214 offset:8320\n"
    "v_mfma_f32_16x16x32_bf16 v[120:123], a[232:235], v[180:183], v[120:123]\n"
    "v_exp_f32 v170, v170\n"
    "v_mfma_f32_16x16x32_bf16 v[120:123], a[200:203], v[176:179], v[120:123]\n"
    "ds_read_b128 a[124:127], v215 offset:8320\n"
    "v_cvt_pk_bf16_f32 v186, v164, v165\n"
    "v_mfma_f32_16x16x32_bf16 v[124:127], a[204:207], v[176:179], v[124:127]\n"
    "v_exp_f32 v171, v171\n"
    "v_mfma_f32_16x16x32_bf16 v[124:127], a[236:239], v[180:183], v[124:127]\n"
    "s_waitcnt lgkmcnt(3)\n"
    "ds_read_b128 a[140:143], v215 offset:12480\n"
    "v_cvt_pk_bf16_f32 v187, v166, v167\n"
    "v_mfma_f32_16x16x32_bf16 v[160:163], a[80:83], a[0:3], -4.0\n"
    "v_exp_f32 v172, v172\n"
    "v_mfma_f32_16x16x32_bf16 v[160:163], a[84:87], a[4:7], v[160:163]\n"
    "ds_read_b128 a[136:139], v214 offset:12480\n"
    "ds_read_b128 a[132:135], v213 offset:12480\n"
    "v_mfma_f32_16x16x32_bf16 v[160:163], a[88:91], a[8:11], v[160:163]\n"
    "v_exp_f32 v173, v173\n"
    "v_mfma_f32_16x16x32_bf16 v[160:163], a[92:95], a[12:15], v[160:163]\n"
    "ds_read_b128 a[128:131], v212 offset:12480\n"
    "v_cvt_pk_bf16_f32 v188, v168, v169\n"
    "v_mfma_f32_16x16x32_bf16 v[164:167], a[108:111], a[12:15], -4.0\n"
    "v_exp_f32 v174, v174\n"
    "v_mfma_f32_16x16x32_bf16 v[164:167], a[104:107], a[8:11], v[164:167]\n"
    "v_cvt_pk_bf16_f32 v189, v170, v171\n"
    "v_cvt_pk_bf16_f32 v190, v172, v173\n"
    "v_mfma_f32_16x16x32_bf16 v[164:167], a[100:103], a[4:7], v[164:167]\n"
    "v_exp_f32 v175, v175\n"
    "v_mfma_f32_16x16x32_bf16 v[164:167], a[96:99], a[0:3], v[164:167]\n"
    "s_waitcnt vmcnt(12)\n"
    "ds_read_b128 v[244:247], v228 offset:20480\n"
    "v_cvt_pk_bf16_f32 v191, v174, v175\n"
    "v_mfma_f32_16x16x32_bf16 v[168:171], a[112:115], a[0:3], -4.0\n"
    "s_waitcnt lgkmcnt(4)\n"
    "ds_write_b128 v228, v[248:251] offset:1024\n"
    "v_lshlrev_b32 v241, 16, v255\n"
    "v_and_b32 v255, s80, v255\n"
    "v_pk_mul_f32 v[238:239], v[238:239], s[76:77] op_sel_hi:[1,0]\n"
    "v_pk_mul_f32 v[252:253], v[252:253], s[76:77] op_sel_hi:[1,0]\n"
    "v_pk_mul_f32 v[240:241], v[240:241], s[76:77] op_sel_hi:[1,0]\n"
    "v_pk_mul_f32 v[254:255], v[254:255], s[76:77] op_sel_hi:[1,0]\n"
    "v_mfma_f32_16x16x32_bf16 v[168:171], a[116:119], a[4:7], v[168:171]\n"
    "v_exp_f32 v160, v160\n"
    "v_mfma_f32_16x16x32_bf16 v[168:171], a[120:123], a[8:11], v[168:171]\n"
    "v_cvt_pk_bf16_f32 v252, v238, v252\n"
    "v_cvt_pk_bf16_f32 v253, v239, v253\n"
    "v_mfma_f32_16x16x32_bf16 v[168:171], a[124:127], a[12:15], v[168:171]\n"
    "v_exp_f32 v161, v161\n"
    "v_mfma_f32_16x16x32_bf16 v[172:175], a[140:143], a[12:15], -4.0\n"
    "s_waitcnt lgkmcnt(2)\n"
    "v_cvt_pk_bf16_f32 v254, v240, v254\n"
    "v_cvt_pk_bf16_f32 v255, v241, v255\n"
    "v_mfma_f32_16x16x32_bf16 v[172:175], a[136:139], a[8:11], v[172:175]\n"
    "v_exp_f32 v162, v162\n"
    "v_mfma_f32_16x16x32_bf16 v[172:175], a[132:135], a[4:7], v[172:175]\n"
    "ds_read_b128 a[244:247], v227 offset:3072\n"
    "v_cvt_pk_bf16_f32 v176, v160, v161\n"
    "v_mfma_f32_16x16x32_bf16 v[172:175], a[128:131], a[0:3], v[172:175]\n"
    "v_exp_f32 v163, v163\n"
    "v_smfmac_f32_16x16x64_bf16 v[208:211], a[240:243], v[184:191], v237\n"
    "v_cvt_pk_bf16_f32 v177, v162, v163\n"
    "v_mfma_f32_16x16x32_bf16 v[128:131], a[208:211], v[188:191], v[128:131]\n"
    "v_exp_f32 v164, v164\n"
    "v_mfma_f32_16x16x32_bf16 v[128:131], a[176:179], v[184:187], v[128:131]\n"
    "s_waitcnt lgkmcnt(1)\n"
    "ds_read_b64_tr_b16 a[208:209], v216 offset:49792\n"
    "v_lshlrev_b32 v238, 16, v244\n"
    "v_mfma_f32_16x16x32_bf16 v[132:135], a[180:183], v[184:187], v[132:135]\n"
    "v_exp_f32 v165, v165\n"
    "v_mfma_f32_16x16x32_bf16 v[132:135], a[212:215], v[188:191], v[132:135]\n"
    "v_and_b32 v244, s80, v244\n"
    "v_lshlrev_b32 v239, 16, v245\n"
    "v_mfma_f32_16x16x32_bf16 v[136:139], a[216:219], v[188:191], v[136:139]\n"
    "v_exp_f32 v166, v166\n"
    "v_mfma_f32_16x16x32_bf16 v[136:139], a[184:187], v[184:187], v[136:139]\n"
    "s_waitcnt lgkmcnt(1)\n"
    "ds_read_b64_tr_b16 a[210:211], v216 offset:53920\n"
    "ds_read_b64_tr_b16 a[212:213], v216 offset:49800\n"
    "v_mfma_f32_16x16x32_bf16 v[140:143], a[188:191], v[184:187], v[140:143]\n"
    "s_add_u32 m0, s63, 18432\n"
    "buffer_store_dwordx4 a[244:247], v228, s[48:51], s56 offen offset:3072 nt\n"
    "v_exp_f32 v167, v167\n"
    "v_mfma_f32_16x16x32_bf16 v[140:143], a[220:223], v[188:191], v[140:143]\n"
    "buffer_load_dwordx4 v226, s[44:47], s55 offen offset:2048 lds\n"
    "v_mfma_f32_16x16x32_bf16 v[144:147], a[224:227], v[188:191], v[144:147]\n"
    "v_exp_f32 v168, v168\n"
    "v_mfma_f32_16x16x32_bf16 v[144:147], a[192:195], v[184:187], v[144:147]\n"
    "ds_read_b64_tr_b16 a[214:215], v216 offset:53928\n"
    "v_cvt_pk_bf16_f32 v178, v164, v165\n"
    "v_mfma_f32_16x16x32_bf16 v[148:151], a[196:199], v[184:187], v[148:151]\n"
    "v_exp_f32 v169, v169\n"
    "v_mfma_f32_16x16x32_bf16 v[148:151], a[228:231], v[188:191], v[148:151]\n"
    "ds_read_b64_tr_b16 a[216:217], v217 offset:49792\n"
    "v_cvt_pk_bf16_f32 v179, v166, v167\n"
    "v_mfma_f32_16x16x32_bf16 v[152:155], a[232:235], v[188:191], v[152:155]\n"
    "v_exp_f32 v170, v170\n"
    "v_mfma_f32_16x16x32_bf16 v[152:155], a[200:203], v[184:187], v[152:155]\n"
    "ds_read_b64_tr_b16 a[218:219], v217 offset:53920\n"
    "ds_read_b64_tr_b16 a[220:221], v217 offset:49800\n"
    "v_mfma_f32_16x16x32_bf16 v[156:159], a[204:207], v[184:187], v[156:159]\n"
    "v_exp_f32 v171, v171\n"
    "v_mfma_f32_16x16x32_bf16 v[156:159], a[236:239], v[188:191], v[156:159]\n"
    "ds_read_b64_tr_b16 a[222:223], v217 offset:53928\n"
    "v_cvt_pk_bf16_f32 v180, v168, v169\n"
    "s_sub_u32 s79, s79, 1\n"
    "s_cmp_eq_u32 s79, 0\n"
    "s_cbranch_scc1 LTAIL%=\n"
    "v_mfma_f32_16x16x32_bf16 v[160:163], a[80:83], a[16:19], -4.0\n"
    "v_exp_f32 v172, v172\n"
    "v_mfma_f32_16x16x32_bf16 v[160:163], a[84:87], a[20:23], v[160:163]\n"
    "ds_read_b64_tr_b16 a[224:225], v218 offset:49792\n"
    "v_cvt_pk_bf16_f32 v181, v170, v171\n"
    "v_mfma_f32_16x16x32_bf16 v[160:163], a[88:91], a[24:27], v[160:163]\n"
    "v_exp_f32 v173, v173\n"
    "v_mfma_f32_16x16x32_bf16 v[160:163], a[92:95], a[28:31], v[160:163]\n"
    "ds_read_b64_tr_b16 a[226:227], v218 offset:53920\n"
    "ds_read_b64_tr_b16 a[228:229], v218 offset:49800\n"
    "v_mfma_f32_16x16x32_bf16 v[164:167], a[108:111], a[28:31], -4.0\n"
    "v_exp_f32 v174, v174\n"
    "v_mfma_f32_16x16x32_bf16 v[164:167], a[104:107], a[24:27], v[164:167]\n"
    "ds_read_b64_tr_b16 a[230:231], v218 offset:53928\n"
    "v_cvt_pk_bf16_f32 v182, v172, v173\n"
    "v_mfma_f32_16x16x32_bf16 v[164:167], a[100:103], a[20:23], v[164:167]\n"
    "v_exp_f32 v175, v175\n"
    "v_mfma_f32_16x16x32_bf16 v[164:167], a[96:99], a[16:19], v[164:167]\n"
    "ds_read_b64_tr_b16 a[232:233], v219 offset:49792\n"
    "v_cvt_pk_bf16_f32 v183, v174, v175\n"
    "v_mfma_f32_16x16x32_bf16 v[168:171], a[112:115], a[16:19], -4.0\n"
    "s_add_u32 s52, s52, 16384\n"
    "ds_read_b64_tr_b16 a[234:235], v219 offset:53920\n"
    "ds_read_b64_tr_b16 a[236:237], v219 offset:49800\n"
    "v_mfma_f32_16x16x32_bf16 v[168:171], a[116:119], a[20:23], v[168:171]\n"
    "s_waitcnt vmcnt(9)\n"
    "v_exp_f32 v160, v160\n"
    "v_mfma_f32_16x16x32_bf16 v[168:171], a[120:123], a[24:27], v[168:171]\n"
    "s_add_u32 s54, s54, 16384\n"
    "ds_read_b64_tr_b16 a[238:239], v219 offset:53928\n"
    "ds_read_b128 v[248:251], v228 offset:21504\n"
    "v_mfma_f32_16x16x32_bf16 v[168:171], a[124:127], a[28:31], v[168:171]\n"
    "v_exp_f32 v161, v161\n"
    "v_mfma_f32_16x16x32_bf16 v[172:175], a[140:143], a[28:31], -4.0\n"
    "s_add_u32 s53, s53, 16384\n"
    "v_and_b32 v245, s80, v245\n"
    "v_lshlrev_b32 v240, 16, v246\n"
    "v_mfma_f32_16x16x32_bf16 v[172:175], a[136:139], a[24:27], v[172:175]\n"
    "v_exp_f32 v162, v162\n"
    "v_mfma_f32_16x16x32_bf16 v[172:175], a[132:135], a[20:23], v[172:175]\n"
    "v_and_b32 v246, s80, v246\n"
    "v_lshlrev_b32 v241, 16, v247\n"
    "v_mfma_f32_16x16x32_bf16 v[172:175], a[128:131], a[16:19], v[172:175]\n"
    "v_exp_f32 v163, v163\n"
    "v_smfmac_f32_16x16x64_bf16 v[192:195], a[240:243], v[176:183], v237\n"
    "s_waitcnt lgkmcnt(2)\n"
    "ds_write_b128 v228, v[252:255] offset:2048\n"
    "v_and_b32 v247, s80, v247\n"
    "v_pk_mul_f32 v[238:239], v[238:239], s[76:77] op_sel_hi:[1,0]\n"
    "v_pk_mul_f32 v[244:245], v[244:245], s[76:77] op_sel_hi:[1,0]\n"
    "v_pk_mul_f32 v[240:241], v[240:241], s[76:77] op_sel_hi:[1,0]\n"
    "v_pk_mul_f32 v[246:247], v[246:247], s[76:77] op_sel_hi:[1,0]\n"
    "v_mfma_f32_16x16x32_bf16 v[0:3], a[208:211], v[180:183], v[0:3]\n"
    "v_exp_f32 v164, v164\n"
    "v_mfma_f32_16x16x32_bf16 v[0:3], a[144:147], v[176:179], v[0:3]\n"
    "v_cvt_pk_bf16_f32 v244, v238, v244\n"
    "v_cvt_pk_bf16_f32 v245, v239, v245\n"
    "v_mfma_f32_16x16x32_bf16 v[4:7], a[148:151], v[176:179], v[4:7]\n"
    "v_exp_f32 v165, v165\n"
    "v_mfma_f32_16x16x32_bf16 v[4:7], a[212:215], v[180:183], v[4:7]\n"
    "v_cvt_pk_bf16_f32 v184, v160, v161\n"
    "v_cvt_pk_bf16_f32 v185, v162, v163\n"
    "v_mfma_f32_16x16x32_bf16 v[8:11], a[216:219], v[180:183], v[8:11]\n"
    "v_exp_f32 v166, v166\n"
    "v_mfma_f32_16x16x32_bf16 v[8:11], a[152:155], v[176:179], v[8:11]\n"
    "v_cvt_pk_bf16_f32 v246, v240, v246\n"
    "v_cvt_pk_bf16_f32 v247, v241, v247\n"
    "v_mfma_f32_16x16x32_bf16 v[12:15], a[156:159], v[176:179], v[12:15]\n"
    "s_waitcnt lgkmcnt(1)\n"
    "v_exp_f32 v167, v167\n"
    "v_mfma_f32_16x16x32_bf16 v[12:15], a[220:223], v[180:183], v[12:15]\n"
    "s_barrier\n"
    "v_mfma_f32_16x16x32_bf16 v[16:19], a[224:227], v[180:183], v[16:19]\n"
    "s_add_u32 m0, s57, 0\n"
    "v_exp_f32 v168, v168\n"
    "v_mfma_f32_16x16x32_bf16 v[16:19], a[160:163], v[176:179], v[16:19]\n"
    "buffer_load_dwordx4 v229, s[36:39], s52 offen lds\n"
    "v_mfma_f32_16x16x32_bf16 v[20:23], a[164:167], v[176:179], v[20:23]\n"
    "v_exp_f32 v169, v169\n"
    "v_mfma_f32_16x16x32_bf16 v[20:23], a[228:231], v[180:183], v[20:23]\n"
    "v_cvt_pk_bf16_f32 v186, v164, v165\n"
    "v_cvt_pk_bf16_f32 v187, v166, v167\n"
    "v_mfma_f32_16x16x32_bf16 v[24:27], a[232:235], v[180:183], v[24:27]\n"
    "v_exp_f32 v170, v170\n"
    "v_mfma_f32_16x16x32_bf16 v[24:27], a[168:171], v[176:179], v[24:27]\n"
    "v_cvt_pk_bf16_f32 v188, v168, v169\n"
    "v_lshlrev_b32 v238, 16, v248\n"
    "v_mfma_f32_16x16x32_bf16 v[28:31], a[172:175], v[176:179], v[28:31]\n"
    "v_exp_f32 v171, v171\n"
    "v_mfma_f32_16x16x32_bf16 v[28:31], a[236:239], v[180:183], v[28:31]\n"
    "v_cvt_pk_bf16_f32 v189, v170, v171\n"
    "v_and_b32 v248, s80, v248\n"
    "v_mfma_f32_16x16x32_bf16 v[160:163], a[80:83], a[32:35], -4.0\n"
    "v_exp_f32 v172, v172\n"
    "v_mfma_f32_16x16x32_bf16 v[160:163], a[84:87], a[36:39], v[160:163]\n"
    "v_lshlrev_b32 v239, 16, v249\n"
    "v_and_b32 v249, s80, v249\n"
    "v_mfma_f32_16x16x32_bf16 v[160:163], a[88:91], a[40:43], v[160:163]\n"
    "s_add_u32 m0, s57, 0\n"
    "v_exp_f32 v173, v173\n"
    "v_mfma_f32_16x16x32_bf16 v[160:163], a[92:95], a[44:47], v[160:163]\n"
    "buffer_load_dwordx4 v229, s[36:39], s52 offen offset:1024 lds\n"
    "v_mfma_f32_16x16x32_bf16 v[164:167], a[108:111], a[44:47], -4.0\n"
    "v_exp_f32 v174, v174\n"
    "v_mfma_f32_16x16x32_bf16 v[164:167], a[104:107], a[40:43], v[164:167]\n"
    "v_cvt_pk_bf16_f32 v190, v172, v173\n"
    "v_lshlrev_b32 v240, 16, v250\n"
    "v_mfma_f32_16x16x32_bf16 v[164:167], a[100:103], a[36:39], v[164:167]\n"
    "s_add_u32 m0, s63, 18432\n"
    "v_exp_f32 v175, v175\n"
    "v_mfma_f32_16x16x32_bf16 v[164:167], a[96:99], a[32:35], v[164:167]\n"
    "buffer_load_dwordx4 v227, s[44:47], s55 offen offset:3072 lds\n"
    "v_mfma_f32_16x16x32_bf16 v[168:171], a[112:115], a[32:35], -4.0\n"
    "s_add_u32 s55, s55, 4096\n"
    "v_cvt_pk_bf16_f32 v191, v174, v175\n"
    "v_and_b32 v250, s80, v250\n"
    "v_mfma_f32_16x16x32_bf16 v[168:171], a[116:119], a[36:39], v[168:171]\n"
    "v_exp_f32 v160, v160\n"
    "v_mfma_f32_16x16x32_bf16 v[168:171], a[120:123], a[40:43], v[168:171]\n"
    "ds_write_b128 v228, v[244:247] offset:3072\n"
    "v_lshlrev_b32 v241, 16, v251\n"
    "v_and_b32 v251, s80, v251\n"
    "v_pk_mul_f32 v[238:239], v[238:239], s[76:77] op_sel_hi:[1,0]\n"
    "v_pk_mul_f32 v[248:249], v[248:249], s[76:77] op_sel_hi:[1,0]\n"
    "v_pk_mul_f32 v[240:241], v[240:241], s[76:77] op_sel_hi:[1,0]\n"
    "v_pk_mul_f32 v[250:251], v[250:251], s[76:77] op_sel_hi:[1,0]\n"
    "v_mfma_f32_16x16x32_bf16 v[168:171], a[124:127], a[44:47], v[168:171]\n"
    "s_add_u32 m0, s57, 32\n"
    "v_exp_f32 v161, v161\n"
    "v_mfma_f32_16x16x32_bf16 v[172:175], a[140:143], a[44:47], -4.0\n"
    "buffer_load_dwordx4 v229, s[36:39], s52 offen offset:2048 lds\n"
    "v_mfma_f32_16x16x32_bf16 v[172:175], a[136:139], a[40:43], v[172:175]\n"
    "v_exp_f32 v162, v162\n"
    "v_mfma_f32_16x16x32_bf16 v[172:175], a[132:135], a[36:39], v[172:175]\n"
    "v_cvt_pk_bf16_f32 v248, v238, v248\n"
    "v_cvt_pk_bf16_f32 v249, v239, v249\n"
    "v_mfma_f32_16x16x32_bf16 v[172:175], a[128:131], a[32:35], v[172:175]\n"
    "v_exp_f32 v163, v163\n"
    "v_smfmac_f32_16x16x64_bf16 v[196:199], a[240:243], v[184:191], v237\n"
    "ds_read_b128 a[248:251], v227 offset:7168\n"
    "v_cvt_pk_bf16_f32 v250, v240, v250\n"
    "v_mfma_f32_16x16x32_bf16 v[32:35], a[208:211], v[188:191], v[32:35]\n"
    "v_exp_f32 v164, v164\n"
    "v_mfma_f32_16x16x32_bf16 v[32:35], a[144:147], v[184:187], v[32:35]\n"
    "v_cvt_pk_bf16_f32 v251, v241, v251\n"
    "v_cvt_pk_bf16_f32 v176, v160, v161\n"
    "v_mfma_f32_16x16x32_bf16 v[36:39], a[148:151], v[184:187], v[36:39]\n"
    "s_add_u32 m0, s57, 32\n"
    "v_exp_f32 v165, v165\n"
    "v_mfma_f32_16x16x32_bf16 v[36:39], a[212:215], v[188:191], v[36:39]\n"
    "buffer_load_dwordx4 v229, s[36:39], s52 offen offset:3072 lds\n"
    "v_mfma_f32_16x16x32_bf16 v[40:43], a[216:219], v[188:191], v[40:43]\n"
    "v_exp_f32 v166, v166\n"
    "v_mfma_f32_16x16x32_bf16 v[40:43], a[152:155], v[184:187], v[40:43]\n"
    "ds_read_b64_tr_b16 a[176:177], v216 offset:41536\n"
    "v_cvt_pk_bf16_f32 v177, v162, v163\n"
    "v_mfma_f32_16x16x32_bf16 v[44:47], a[156:159], v[184:187], v[44:47]\n"
    "s_waitcnt vmcnt(10)\n"
    "v_exp_f32 v167, v167\n"
    "v_mfma_f32_16x16x32_bf16 v[44:47], a[220:223], v[188:191], v[44:47]\n"
    "s_add_u32 s56, s64, 0\n"
    "ds_read_b128 v[252:255], v228 offset:22528\n"
    "v_cvt_pk_bf16_f32 v178, v164, v165\n"
    "v_mfma_f32_16x16x32_bf16 v[48:51], a[224:227], v[188:191], v[48:51]\n"
    "s_waitcnt lgkmcnt(2)\n"
    "buffer_store_dwordx4 a[248:251], v228, s[48:51], s56 offen offset:3072 nt\n"
    "v_exp_f32 v168, v168\n"
    "v_mfma_f32_16x16x32_bf16 v[48:51], a[160:163], v[184:187], v[48:51]\n"
    "ds_read_b64_tr_b16 a[178:179], v216 offset:45664\n"
    "v_cvt_pk_bf16_f32 v179, v166, v167\n"
    "v_mfma_f32_16x16x32_bf16 v[52:55], a[164:167], v[184:187], v[52:55]\n"
    "v_exp_f32 v169, v169\n"
    "v_mfma_f32_16x16x32_bf16 v[52:55], a[228:231], v[188:191], v[52:55]\n"
    "ds_read_b64_tr_b16 a[180:181], v216 offset:41544\n"
    "v_cvt_pk_bf16_f32 v180, v168, v169\n"
    "v_mfma_f32_16x16x32_bf16 v[56:59], a[232:235], v[188:191], v[56:59]\n"
    "s_add_u32 m0, s58, 16512\n"
    "v_exp_f32 v170, v170\n"
    "v_mfma_f32_16x16x32_bf16 v[56:59], a[168:171], v[184:187], v[56:59]\n"
    "buffer_load_dwordx4 v229, s[40:43], s54 offen lds\n"
    "v_mfma_f32_16x16x32_bf16 v[60:63], a[172:175], v[184:187], v[60:63]\n"
    "v_exp_f32 v171, v171\n"
    "v_mfma_f32_16x16x32_bf16 v[60:63], a[236:239], v[188:191], v[60:63]\n"
    "ds_read_b64_tr_b16 a[182:183], v216 offset:45672\n"
    "v_cvt_pk_bf16_f32 v181, v170, v171\n"
    "v_mfma_f32_16x16x32_bf16 v[160:163], a[80:83], a[48:51], -4.0\n"
    "v_exp_f32 v172, v172\n"
    "v_mfma_f32_16x16x32_bf16 v[160:163], a[84:87], a[52:55], v[160:163]\n"
    "ds_read_b64_tr_b16 a[184:185], v217 offset:41536\n"
    "ds_read_b64_tr_b16 a[186:187], v217 offset:45664\n"
    "v_mfma_f32_16x16x32_bf16 v[160:163], a[88:91], a[56:59], v[160:163]\n"
    "v_exp_f32 v173, v173\n"
    "v_mfma_f32_16x16x32_bf16 v[160:163], a[92:95], a[60:63], v[160:163]\n"
    "ds_read_b64_tr_b16 a[188:189], v217 offset:41544\n"
    "v_cvt_pk_bf16_f32 v182, v172, v173\n"
    "v_mfma_f32_16x16x32_bf16 v[164:167], a[108:111], a[60:63], -4.0\n"
    "s_add_u32 m0, s58, 16520\n"
    "v_exp_f32 v174, v174\n"
    "v_mfma_f32_16x16x32_bf16 v[164:167], a[104:107], a[56:59], v[164:167]\n"
    "buffer_load_dwordx4 v229, s[40:43], s54 offen offset:1024 lds\n"
    "v_mfma_f32_16x16x32_bf16 v[164:167], a[100:103], a[52:55], v[164:167]\n"
    "v_exp_f32 v175, v175\n"
    "v_mfma_f32_16x16x32_bf16 v[164:167], a[96:99], a[48:51], v[164:167]\n"
    "ds_read_b64_tr_b16 a[190:191], v217 offset:45672\n"
    "v_cvt_pk_bf16_f32 v183, v174, v175\n"
    "v_mfma_f32_16x16x32_bf16 v[168:171], a[112:115], a[48:51], -4.0\n"
    "ds_read_b64_tr_b16 a[192:193], v218 offset:41536\n"
    "ds_read_b64_tr_b16 a[194:195], v218 offset:45664\n"
    "v_mfma_f32_16x16x32_bf16 v[168:171], a[116:119], a[52:55], v[168:171]\n"
    "v_exp_f32 v160, v160\n"
    "v_mfma_f32_16x16x32_bf16 v[168:171], a[120:123], a[56:59], v[168:171]\n"
    "ds_read_b64_tr_b16 a[196:197], v218 offset:41544\n"
    "ds_read_b64_tr_b16 a[198:199], v218 offset:45672\n"
    "v_mfma_f32_16x16x32_bf16 v[168:171], a[124:127], a[60:63], v[168:171]\n"
    "v_exp_f32 v161, v161\n"
    "v_mfma_f32_16x16x32_bf16 v[172:175], a[140:143], a[60:63], -4.0\n"
    "ds_read_b64_tr_b16 a[200:201], v219 offset:41536\n"
    "ds_read_b64_tr_b16 a[202:203], v219 offset:45664\n"
    "v_mfma_f32_16x16x32_bf16 v[172:175], a[136:139], a[56:59], v[172:175]\n"
    "v_exp_f32 v162, v162\n"
    "v_mfma_f32_16x16x32_bf16 v[172:175], a[132:135], a[52:55], v[172:175]\n"
    "ds_read_b64_tr_b16 a[204:205], v219 offset:41544\n"
    "v_cvt_pk_bf16_f32 v184, v160, v161\n"
    "v_mfma_f32_16x16x32_bf16 v[172:175], a[128:131], a[48:51], v[172:175]\n"
    "v_exp_f32 v163, v163\n"
    "v_smfmac_f32_16x16x64_bf16 v[200:203], a[240:243], v[176:183], v237\n"
    "v_cvt_pk_bf16_f32 v185, v162, v163\n"
    "v_mfma_f32_16x16x32_bf16 v[64:67], a[208:211], v[180:183], v[64:67]\n"
    "v_exp_f32 v164, v164\n"
    "v_mfma_f32_16x16x32_bf16 v[64:67], a[144:147], v[176:179], v[64:67]\n"
    "s_waitcnt lgkmcnt(3)\n"
    "ds_read_b64_tr_b16 a[206:207], v219 offset:45672\n"
    "v_lshlrev_b32 v238, 16, v252\n"
    "v_mfma_f32_16x16x32_bf16 v[68:71], a[148:151], v[176:179], v[68:71]\n"
    "v_exp_f32 v165, v165\n"
    "v_mfma_f32_16x16x32_bf16 v[68:71], a[212:215], v[180:183], v[68:71]\n"
    "v_and_b32 v252, s80, v252\n"
    "v_lshlrev_b32 v239, 16, v253\n"
    "v_mfma_f32_16x16x32_bf16 v[72:75], a[216:219], v[180:183], v[72:75]\n"
    "v_exp_f32 v166, v166\n"
    "v_mfma_f32_16x16x32_bf16 v[72:75], a[152:155], v[176:179], v[72:75]\n"
    "v_and_b32 v253, s80, v253\n"
    "v_lshlrev_b32 v240, 16, v254\n"
    "v_mfma_f32_16x16x32_bf16 v[76:79], a[156:159], v[176:179], v[76:79]\n"
    "s_add_u32 m0, s63, 22528\n"
    "v_exp_f32 v167, v167\n"
    "v_mfma_f32_16x16x32_bf16 v[76:79], a[220:223], v[180:183], v[76:79]\n"
    "buffer_load_dwordx4 v224, s[44:47], s55 offen lds\n"
    "v_mfma_f32_16x16x32_bf16 v[80:83], a[224:227], v[180:183], v[80:83]\n"
    "v_exp_f32 v168, v168\n"
    "v_mfma_f32_16x16x32_bf16 v[80:83], a[160:163], v[176:179], v[80:83]\n"
    "v_cvt_pk_bf16_f32 v186, v164, v165\n"
    "v_cvt_pk_bf16_f32 v187, v166, v167\n"
    "v_mfma_f32_16x16x32_bf16 v[84:87], a[164:167], v[176:179], v[84:87]\n"
    "v_exp_f32 v169, v169\n"
    "v_mfma_f32_16x16x32_bf16 v[84:87], a[228:231], v[180:183], v[84:87]\n"
    "v_cvt_pk_bf16_f32 v188, v168, v169\n"
    "v_and_b32 v254, s80, v254\n"
    "v_mfma_f32_16x16x32_bf16 v[88:91], a[232:235], v[180:183], v[88:91]\n"
    "v_exp_f32 v170, v170\n"
    "v_mfma_f32_16x16x32_bf16 v[88:91], a[168:171], v[176:179], v[88:91]\n"
    "ds_write_b128 v228, v[248:251] offset:4096\n"
    "v_lshlrev_b32 v241, 16, v255\n"
    "v_and_b32 v255, s80, v255\n"
    "v_pk_mul_f32 v[238:239], v[238:239], s[76:77] op_sel_hi:[1,0]\n"
    "v_pk_mul_f32 v[252:253], v[252:253], s[76:77] op_sel_hi:[1,0]\n"
    "v_pk_mul_f32 v[240:241], v[240:241], s[76:77] op_sel_hi:[1,0]\n"
    "v_pk_mul_f32 v[254:255], v[254:255], s[76:77] op_sel_hi:[1,0]\n"
    "v_mfma_f32_16x16x32_bf16 v[92:95], a[172:175], v[176:179], v[92:95]\n"
    "v_exp_f32 v171, v171\n"
    "v_mfma_f32_16x16x32_bf16 v[92:95], a[236:239], v[180:183], v[92:95]\n"
    "v_cvt_pk_bf16_f32 v189, v170, v171\n"
    "v_cvt_pk_bf16_f32 v252, v238, v252\n"
    "v_mfma_f32_16x16x32_bf16 v[160:163], a[80:83], a[64:67], -4.0\n"
    "v_exp_f32 v172, v172\n"
    "v_mfma_f32_16x16x32_bf16 v[160:163], a[84:87], a[68:71], v[160:163]\n"
    "v_cvt_pk_bf16_f32 v253, v239, v253\n"
    "v_cvt_pk_bf16_f32 v254, v240, v254\n"
    "v_mfma_f32_16x16x32_bf16 v[160:163], a[88:91], a[72:75], v[160:163]\n"
    "v_exp_f32 v173, v173\n"
    "v_mfma_f32_16x16x32_bf16 v[160:163], a[92:95], a[76:79], v[160:163]\n"
    "v_cvt_pk_bf16_f32 v190, v172, v173\n"
    "v_cvt_pk_bf16_f32 v255, v241, v255\n"
    "v_mfma_f32_16x16x32_bf16 v[164:167], a[108:111], a[76:79], -4.0\n"
    "v_exp_f32 v174, v174\n"
    "v_mfma_f32_16x16x32_bf16 v[164:167], a[104:107], a[72:75], v[164:167]\n"
    "ds_read_b128 a[252:255], v224 offset:8192\n"
    "v_mfma_f32_16x16x32_bf16 v[164:167], a[100:103], a[68:71], v[164:167]\n"
    "v_exp_f32 v175, v175\n"
    "v_mfma_f32_16x16x32_bf16 v[164:167], a[96:99], a[64:67], v[164:167]\n"
    "s_waitcnt vmcnt(9) lgkmcnt(1)\n"
    "ds_read_b128 v[244:247], v228 offset:20480\n"
    "v_cvt_pk_bf16_f32 v191, v174, v175\n"
    "v_mfma_f32_16x16x32_bf16 v[168:171], a[112:115], a[64:67], -4.0\n"
    "s_barrier\n"
    "v_mfma_f32_16x16x32_bf16 v[168:171], a[116:119], a[68:71], v[168:171]\n"
    "v_exp_f32 v160, v160\n"
    "v_mfma_f32_16x16x32_bf16 v[168:171], a[120:123], a[72:75], v[168:171]\n"
    "ds_read_b128 a[80:83], v212 offset:16640\n"
    "ds_read_b128 a[84:87], v213 offset:16640\n"
    "v_mfma_f32_16x16x32_bf16 v[168:171], a[124:127], a[76:79], v[168:171]\n"
    "v_exp_f32 v161, v161\n"
    "v_mfma_f32_16x16x32_bf16 v[172:175], a[140:143], a[76:79], -4.0\n"
    "ds_read_b128 a[88:91], v214 offset:16640\n"
    "ds_read_b128 a[92:95], v215 offset:16640\n"
    "v_mfma_f32_16x16x32_bf16 v[172:175], a[136:139], a[72:75], v[172:175]\n"
    "v_exp_f32 v162, v162\n"
    "v_mfma_f32_16x16x32_bf16 v[172:175], a[132:135], a[68:71], v[172:175]\n"
    "s_add_u32 s56, s64, 4096\n"
    "ds_read_b128 a[108:111], v215 offset:20800\n"
    "ds_read_b128 a[104:107], v214 offset:20800\n"
    "v_mfma_f32_16x16x32_bf16 v[172:175], a[128:131], a[64:67], v[172:175]\n"
    "s_waitcnt lgkmcnt(6)\n"
    "buffer_store_dwordx4 a[252:255], v228, s[48:51], s56 offen nt\n"
    "v_exp_f32 v163, v163\n"
    "v_smfmac_f32_16x16x64_bf16 v[204:207], a[240:243], v[184:191], v237\n"
    "ds_read_b128 a[100:103], v213 offset:20800\n"
    "ds_read_b128 a[96:99], v212 offset:20800\n"
    "v_mfma_f32_16x16x32_bf16 v[96:99], a[208:211], v[188:191], v[96:99]\n"
    "v_exp_f32 v164, v164\n"
    "v_mfma_f32_16x16x32_bf16 v[96:99], a[144:147], v[184:187], v[96:99]\n"
    "v_cvt_pk_bf16_f32 v176, v160, v161\n"
    "v_cvt_pk_bf16_f32 v177, v162, v163\n"
    "v_mfma_f32_16x16x32_bf16 v[100:103], a[148:151], v[184:187], v[100:103]\n"
    "v_exp_f32 v165, v165\n"
    "v_mfma_f32_16x16x32_bf16 v[100:103], a[212:215], v[188:191], v[100:103]\n"
    "ds_read_b128 a[112:115], v212 offset:24960\n"
    "ds_read_b128 a[116:119], v213 offset:24960\n"
    "v_mfma_f32_16x16x32_bf16 v[104:107], a[216:219], v[188:191], v[104:107]\n"
    "s_add_u32 m0, s58, 8256\n"
    "v_exp_f32 v166, v166\n"
    "v_mfma_f32_16x16x32_bf16 v[104:107], a[152:155], v[184:187], v[104:107]\n"
    "buffer_load_dwordx4 v229, s[40:43], s53 offen lds\n"
    "v_mfma_f32_16x16x32_bf16 v[108:111], a[156:159], v[184:187], v[108:111]\n"
    "s_add_u32 m0, s58, 8264\n"
    "v_exp_f32 v167, v167\n"
    "v_mfma_f32_16x16x32_bf16 v[108:111], a[220:223], v[188:191], v[108:111]\n"
    "buffer_load_dwordx4 v229, s[40:43], s53 offen offset:1024 lds\n"
    "v_mfma_f32_16x16x32_bf16 v[112:115], a[224:227], v[188:191], v[112:115]\n"
    "v_exp_f32 v168, v168\n"
    "v_mfma_f32_16x16x32_bf16 v[112:115], a[160:163], v[184:187], v[112:115]\n"
    "ds_read_b128 a[120:123], v214 offset:24960\n"
    "ds_read_b128 a[124:127], v215 offset:24960\n"
    "v_mfma_f32_16x16x32_bf16 v[116:119], a[164:167], v[184:187], v[116:119]\n"
    "v_exp_f32 v169, v169\n"
    "v_mfma_f32_16x16x32_bf16 v[116:119], a[228:231], v[188:191], v[116:119]\n"
    "v_cvt_pk_bf16_f32 v178, v164, v165\n"
    "v_cvt_pk_bf16_f32 v179, v166, v167\n"
    "v_mfma_f32_16x16x32_bf16 v[120:123], a[232:235], v[188:191], v[120:123]\n"
    "v_exp_f32 v170, v170\n"
    "v_mfma_f32_16x16x32_bf16 v[120:123], a[168:171], v[184:187], v[120:123]\n"
    "ds_read_b128 a[140:143], v215 offset:29120\n"
    "ds_read_b128 a[136:139], v214 offset:29120\n"
    "v_mfma_f32_16x16x32_bf16 v[124:127], a[172:175], v[184:187], v[124:127]\n"
    "v_exp_f32 v171, v171\n"
    "v_mfma_f32_16x16x32_bf16 v[124:127], a[236:239], v[188:191], v[124:127]\n"
    "s_waitcnt lgkmcnt(4)\n"
    "ds_read_b128 a[132:135], v213 offset:29120\n"
    "ds_read_b128 a[128:131], v212 offset:29120\n"
    "v_mfma_f32_16x16x32_bf16 v[160:163], a[80:83], a[0:3], -4.0\n"
    "v_exp_f32 v172, v172\n"
    "v_mfma_f32_16x16x32_bf16 v[160:163], a[84:87], a[4:7], v[160:163]\n"
    "v_cvt_pk_bf16_f32 v180, v168, v169\n"
    "v_cvt_pk_bf16_f32 v181, v170, v171\n"
    "v_mfma_f32_16x16x32_bf16 v[160:163], a[88:91], a[8:11], v[160:163]\n"
    "v_exp_f32 v173, v173\n"
    "v_mfma_f32_16x16x32_bf16 v[160:163], a[92:95], a[12:15], v[160:163]\n"
    "v_cvt_pk_bf16_f32 v182, v172, v173\n"
    "v_lshlrev_b32 v238, 16, v244\n"
    "v_mfma_f32_16x16x32_bf16 v[164:167], a[108:111], a[12:15], -4.0\n"
    "v_exp_f32 v174, v174\n"
    "v_mfma_f32_16x16x32_bf16 v[164:167], a[104:107], a[8:11], v[164:167]\n"
    "v_and_b32 v244, s80, v244\n"
    "v_lshlrev_b32 v239, 16, v245\n"
    "v_mfma_f32_16x16x32_bf16 v[164:167], a[100:103], a[4:7], v[164:167]\n"
    "s_add_u32 m0, s63, 19456\n"
    "v_exp_f32 v175, v175\n"
    "v_mfma_f32_16x16x32_bf16 v[164:167], a[96:99], a[0:3], v[164:167]\n"
    "buffer_load_dwordx4 v225, s[44:47], s55 offen offset:1024 lds\n"
    "v_mfma_f32_16x16x32_bf16 v[168:171], a[112:115], a[0:3], -4.0\n"
    "v_cvt_pk_bf16_f32 v183, v174, v175\n"
    "v_and_b32 v245, s80, v245\n"
    "v_mfma_f32_16x16x32_bf16 v[168:171], a[116:119], a[4:7], v[168:171]\n"
    "s_waitcnt lgkmcnt(0)\n"
    "v_exp_f32 v160, v160\n"
    "v_mfma_f32_16x16x32_bf16 v[168:171], a[120:123], a[8:11], v[168:171]\n"
    "v_lshlrev_b32 v240, 16, v246\n"
    "v_and_b32 v246, s80, v246\n"
    "v_mfma_f32_16x16x32_bf16 v[168:171], a[124:127], a[12:15], v[168:171]\n"
    "v_exp_f32 v161, v161\n"
    "v_mfma_f32_16x16x32_bf16 v[172:175], a[140:143], a[12:15], -4.0\n"
    "ds_write_b128 v228, v[252:255] offset:5120\n"
    "v_lshlrev_b32 v241, 16, v247\n"
    "v_and_b32 v247, s80, v247\n"
    "v_pk_mul_f32 v[238:239], v[238:239], s[76:77] op_sel_hi:[1,0]\n"
    "v_pk_mul_f32 v[244:245], v[244:245], s[76:77] op_sel_hi:[1,0]\n"
    "v_pk_mul_f32 v[240:241], v[240:241], s[76:77] op_sel_hi:[1,0]\n"
    "v_pk_mul_f32 v[246:247], v[246:247], s[76:77] op_sel_hi:[1,0]\n"
    "v_mfma_f32_16x16x32_bf16 v[172:175], a[136:139], a[8:11], v[172:175]\n"
    "v_exp_f32 v162, v162\n"
    "v_mfma_f32_16x16x32_bf16 v[172:175], a[132:135], a[4:7], v[172:175]\n"
    "v_cvt_pk_bf16_f32 v244, v238, v244\n"
    "v_cvt_pk_bf16_f32 v245, v239, v245\n"
    "v_mfma_f32_16x16x32_bf16 v[172:175], a[128:131], a[0:3], v[172:175]\n"
    "v_exp_f32 v163, v163\n"
    "v_smfmac_f32_16x16x64_bf16 v[208:211], a[240:243], v[176:183], v237\n"
    "ds_read_b128 a[244:247], v225 offset:9216\n"
    "v_cvt_pk_bf16_f32 v246, v240, v246\n"
    "v_mfma_f32_16x16x32_bf16 v[128:131], a[208:211], v[180:183], v[128:131]\n"
    "v_exp_f32 v164, v164\n"
    "v_mfma_f32_16x16x32_bf16 v[128:131], a[144:147], v[176:179], v[128:131]\n"
    "v_cvt_pk_bf16_f32 v247, v241, v247\n"
    "v_cvt_pk_bf16_f32 v184, v160, v161\n"
    "v_mfma_f32_16x16x32_bf16 v[132:135], a[148:151], v[176:179], v[132:135]\n"
    "v_exp_f32 v165, v165\n"
    "v_mfma_f32_16x16x32_bf16 v[132:135], a[212:215], v[180:183], v[132:135]\n"
    "ds_read_b64_tr_b16 a[208:209], v216 offset:58048\n"
    "v_cvt_pk_bf16_f32 v185, v162, v163\n"
    "v_mfma_f32_16x16x32_bf16 v[136:139], a[216:219], v[180:183], v[136:139]\n"
    "v_exp_f32 v166, v166\n"
    "v_mfma_f32_16x16x32_bf16 v[136:139], a[152:155], v[176:179], v[136:139]\n"
    "ds_read_b64_tr_b16 a[210:211], v216 offset:62176\n"
    "ds_read_b64_tr_b16 a[212:213], v216 offset:58056\n"
    "v_mfma_f32_16x16x32_bf16 v[140:143], a[156:159], v[176:179], v[140:143]\n"
    "v_exp_f32 v167, v167\n"
    "v_mfma_f32_16x16x32_bf16 v[140:143], a[220:223], v[180:183], v[140:143]\n"
    "s_waitcnt vmcnt(10)\n"
    "ds_read_b128 v[248:251], v228 offset:21504\n"
    "ds_read_b64_tr_b16 a[214:215], v216 offset:62184\n"
    "v_mfma_f32_16x16x32_bf16 v[144:147], a[224:227], v[180:183], v[144:147]\n"
    "s_waitcnt lgkmcnt(5)\n"
    "buffer_store_dwordx4 a[244:247], v228, s[48:51], s56 offen offset:1024 nt\n"
    "v_exp_f32 v168, v168\n"
    "v_mfma_f32_16x16x32_bf16 v[144:147], a[160:163], v[176:179], v[144:147]\n"
    "v_cvt_pk_bf16_f32 v186, v164, v165\n"
    "v_cvt_pk_bf16_f32 v187, v166, v167\n"
    "v_mfma_f32_16x16x32_bf16 v[148:151], a[164:167], v[176:179], v[148:151]\n"
    "v_exp_f32 v169, v169\n"
    "v_mfma_f32_16x16x32_bf16 v[148:151], a[228:231], v[180:183], v[148:151]\n"
    "ds_read_b64_tr_b16 a[216:217], v217 offset:58048\n"
    "ds_read_b64_tr_b16 a[218:219], v217 offset:62176\n"
    "v_mfma_f32_16x16x32_bf16 v[152:155], a[232:235], v[180:183], v[152:155]\n"
    "v_exp_f32 v170, v170\n"
    "v_mfma_f32_16x16x32_bf16 v[152:155], a[168:171], v[176:179], v[152:155]\n"
    "ds_read_b64_tr_b16 a[220:221], v217 offset:58056\n"
    "ds_read_b64_tr_b16 a[222:223], v217 offset:62184\n"
    "v_mfma_f32_16x16x32_bf16 v[156:159], a[172:175], v[176:179], v[156:159]\n"
    "v_exp_f32 v171, v171\n"
    "v_mfma_f32_16x16x32_bf16 v[156:159], a[236:239], v[180:183], v[156:159]\n"
    "v_cvt_pk_bf16_f32 v188, v168, v169\n"
    "v_cvt_pk_bf16_f32 v189, v170, v171\n"
    "v_mfma_f32_16x16x32_bf16 v[160:163], a[80:83], a[16:19], -4.0\n"
    "v_exp_f32 v172, v172\n"
    "v_mfma_f32_16x16x32_bf16 v[160:163], a[84:87], a[20:23], v[160:163]\n"
    "ds_read_b64_tr_b16 a[224:225], v218 offset:58048\n"
    "ds_read_b64_tr_b16 a[226:227], v218 offset:62176\n"
    "v_mfma_f32_16x16x32_bf16 v[160:163], a[88:91], a[24:27], v[160:163]\n"
    "v_exp_f32 v173, v173\n"
    "v_mfma_f32_16x16x32_bf16 v[160:163], a[92:95], a[28:31], v[160:163]\n"
    "ds_read_b64_tr_b16 a[228:229], v218 offset:58056\n"
    "ds_read_b64_tr_b16 a[230:231], v218 offset:62184\n"
    "v_mfma_f32_16x16x32_bf16 v[164:167], a[108:111], a[28:31], -4.0\n"
    "v_exp_f32 v174, v174\n"
    "v_mfma_f32_16x16x32_bf16 v[164:167], a[104:107], a[24:27], v[164:167]\n"
    "ds_read_b64_tr_b16 a[232:233], v219 offset:58048\n"
    "v_cvt_pk_bf16_f32 v190, v172, v173\n"
    "v_mfma_f32_16x16x32_bf16 v[164:167], a[100:103], a[20:23], v[164:167]\n"
    "v_exp_f32 v175, v175\n"
    "v_mfma_f32_16x16x32_bf16 v[164:167], a[96:99], a[16:19], v[164:167]\n"
    "ds_read_b64_tr_b16 a[234:235], v219 offset:62176\n"
    "v_cvt_pk_bf16_f32 v191, v174, v175\n"
    "v_mfma_f32_16x16x32_bf16 v[168:171], a[112:115], a[16:19], -4.0\n"
    "s_add_u32 s52, s52, 16384\n"
    "ds_read_b64_tr_b16 a[236:237], v219 offset:58056\n"
    "ds_read_b64_tr_b16 a[238:239], v219 offset:62184\n"
    "v_mfma_f32_16x16x32_bf16 v[168:171], a[116:119], a[20:23], v[168:171]\n"
    "s_waitcnt lgkmcnt(6)\n"
    "v_exp_f32 v160, v160\n"
    "v_mfma_f32_16x16x32_bf16 v[168:171], a[120:123], a[24:27], v[168:171]\n"
    "s_add_u32 s54, s54, 16384\n"
    "v_lshlrev_b32 v238, 16, v248\n"
    "v_and_b32 v248, s80, v248\n"
    "v_mfma_f32_16x16x32_bf16 v[168:171], a[124:127], a[28:31], v[168:171]\n"
    "v_exp_f32 v161, v161\n"
    "v_mfma_f32_16x16x32_bf16 v[172:175], a[140:143], a[28:31], -4.0\n"
    "s_add_u32 s53, s53, 16384\n"
    "v_cvt_pk_bf16_f32 v176, v160, v161\n"
    "v_lshlrev_b32 v239, 16, v249\n"
    "v_mfma_f32_16x16x32_bf16 v[172:175], a[136:139], a[24:27], v[172:175]\n"
    "v_exp_f32 v162, v162\n"
    "v_mfma_f32_16x16x32_bf16 v[172:175], a[132:135], a[20:23], v[172:175]\n"
    "v_and_b32 v249, s80, v249\n"
    "v_lshlrev_b32 v240, 16, v250\n"
    "v_mfma_f32_16x16x32_bf16 v[172:175], a[128:131], a[16:19], v[172:175]\n"
    "v_exp_f32 v163, v163\n"
    "v_smfmac_f32_16x16x64_bf16 v[192:195], a[240:243], v[184:191], v237\n"
    "v_cvt_pk_bf16_f32 v177, v162, v163\n"
    "v_mfma_f32_16x16x32_bf16 v[0:3], a[208:211], v[188:191], v[0:3]\n"
    "v_exp_f32 v164, v164\n"
    "v_mfma_f32_16x16x32_bf16 v[0:3], a[176:179], v[184:187], v[0:3]\n"
    "v_and_b32 v250, s80, v250\n"
    "v_lshlrev_b32 v241, 16, v251\n"
    "v_mfma_f32_16x16x32_bf16 v[4:7], a[180:183], v[184:187], v[4:7]\n"
    "v_exp_f32 v165, v165\n"
    "v_mfma_f32_16x16x32_bf16 v[4:7], a[212:215], v[188:191], v[4:7]\n"
    "ds_write_b128 v228, v[244:247] offset:6144\n"
    "v_and_b32 v251, s80, v251\n"
    "v_cvt_pk_bf16_f32 v178, v164, v165\n"
    "v_pk_mul_f32 v[238:239], v[238:239], s[76:77] op_sel_hi:[1,0]\n"
    "v_pk_mul_f32 v[248:249], v[248:249], s[76:77] op_sel_hi:[1,0]\n"
    "v_pk_mul_f32 v[240:241], v[240:241], s[76:77] op_sel_hi:[1,0]\n"
    "v_pk_mul_f32 v[250:251], v[250:251], s[76:77] op_sel_hi:[1,0]\n"
    "v_mfma_f32_16x16x32_bf16 v[8:11], a[216:219], v[188:191], v[8:11]\n"
    "v_exp_f32 v166, v166\n"
    "v_mfma_f32_16x16x32_bf16 v[8:11], a[184:187], v[184:187], v[8:11]\n"
    "v_cvt_pk_bf16_f32 v248, v238, v248\n"
    "v_cvt_pk_bf16_f32 v249, v239, v249\n"
    "v_mfma_f32_16x16x32_bf16 v[12:15], a[188:191], v[184:187], v[12:15]\n"
    "s_waitcnt lgkmcnt(1)\n"
    "v_exp_f32 v167, v167\n"
    "v_mfma_f32_16x16x32_bf16 v[12:15], a[220:223], v[188:191], v[12:15]\n"
    "s_barrier\n"
    "v_mfma_f32_16x16x32_bf16 v[16:19], a[224:227], v[188:191], v[16:19]\n"
    "s_add_u32 m0, s63, 19456\n"
    "v_exp_f32 v168, v168\n"
    "v_mfma_f32_16x16x32_bf16 v[16:19], a[192:195], v[184:187], v[16:19]\n"
    "buffer_load_dwordx4 v226, s[44:47], s55 offen offset:2048 lds\n"
    "v_mfma_f32_16x16x32_bf16 v[20:23], a[196:199], v[184:187], v[20:23]\n"
    "s_add_u32 m0, s57, 16640\n"
    "v_exp_f32 v169, v169\n"
    "v_mfma_f32_16x16x32_bf16 v[20:23], a[228:231], v[188:191], v[20:23]\n"
    "buffer_load_dwordx4 v229, s[36:39], s52 offen lds\n"
    "v_mfma_f32_16x16x32_bf16 v[24:27], a[232:235], v[188:191], v[24:27]\n"
    "v_exp_f32 v170, v170\n"
    "v_mfma_f32_16x16x32_bf16 v[24:27], a[200:203], v[184:187], v[24:27]\n"
    "v_cvt_pk_bf16_f32 v179, v166, v167\n"
    "v_cvt_pk_bf16_f32 v180, v168, v169\n"
    "v_mfma_f32_16x16x32_bf16 v[28:31], a[204:207], v[184:187], v[28:31]\n"
    "v_exp_f32 v171, v171\n"
    "v_mfma_f32_16x16x32_bf16 v[28:31], a[236:239], v[188:191], v[28:31]\n"
    "v_cvt_pk_bf16_f32 v181, v170, v171\n"
    "v_cvt_pk_bf16_f32 v250, v240, v250\n"
    "v_mfma_f32_16x16x32_bf16 v[160:163], a[80:83], a[32:35], -4.0\n"
    "v_exp_f32 v172, v172\n"
    "v_mfma_f32_16x16x32_bf16 v[160:163], a[84:87], a[36:39], v[160:163]\n"
    "v_cvt_pk_bf16_f32 v251, v241, v251\n"
    "v_mfma_f32_16x16x32_bf16 v[160:163], a[88:91], a[40:43], v[160:163]\n"
    "s_add_u32 m0, s57, 16640\n"
    "v_exp_f32 v173, v173\n"
    "v_mfma_f32_16x16x32_bf16 v[160:163], a[92:95], a[44:47], v[160:163]\n"
    "buffer_load_dwordx4 v229, s[36:39], s52 offen offset:1024 lds\n"
    "v_mfma_f32_16x16x32_bf16 v[164:167], a[108:111], a[44:47], -4.0\n"
    "v_exp_f32 v174, v174\n"
    "v_mfma_f32_16x16x32_bf16 v[164:167], a[104:107], a[40:43], v[164:167]\n"
    "ds_read_b64_tr_b16 a[144:145], v216 offset:33280\n"
    "v_cvt_pk_bf16_f32 v182, v172, v173\n"
    "v_mfma_f32_16x16x32_bf16 v[164:167], a[100:103], a[36:39], v[164:167]\n"
    "v_exp_f32 v175, v175\n"
    "v_mfma_f32_16x16x32_bf16 v[164:167], a[96:99], a[32:35], v[164:167]\n"
    "s_waitcnt vmcnt(8)\n"
    "ds_read_b128 v[252:255], v228 offset:22528\n"
    "v_cvt_pk_bf16_f32 v183, v174, v175\n"
    "v_mfma_f32_16x16x32_bf16 v[168:171], a[112:115], a[32:35], -4.0\n"
    "ds_read_b64_tr_b16 a[146:147], v216 offset:37408\n"
    "ds_read_b128 a[248:251], v226 offset:10240\n"
    "v_mfma_f32_16x16x32_bf16 v[168:171], a[116:119], a[36:39], v[168:171]\n"
    "v_exp_f32 v160, v160\n"
    "v_mfma_f32_16x16x32_bf16 v[168:171], a[120:123], a[40:43], v[168:171]\n"
    "v_mfma_f32_16x16x32_bf16 v[168:171], a[124:127], a[44:47], v[168:171]\n"
    "s_add_u32 m0, s57, 16672\n"
    "v_exp_f32 v161, v161\n"
    "v_mfma_f32_16x16x32_bf16 v[172:175], a[140:143], a[44:47], -4.0\n"
    "buffer_load_dwordx4 v229, s[36:39], s52 offen offset:2048 lds\n"
    "v_mfma_f32_16x16x32_bf16 v[172:175], a[136:139], a[40:43], v[172:175]\n"
    "v_exp_f32 v162, v162\n"
    "v_mfma_f32_16x16x32_bf16 v[172:175], a[132:135], a[36:39], v[172:175]\n"
    "ds_read_b64_tr_b16 a[148:149], v216 offset:33288\n"
    "v_cvt_pk_bf16_f32 v184, v160, v161\n"
    "v_mfma_f32_16x16x32_bf16 v[172:175], a[128:131], a[32:35], v[172:175]\n"
    "v_exp_f32 v163, v163\n"
    "v_smfmac_f32_16x16x64_bf16 v[196:199], a[240:243], v[176:183], v237\n"
    "ds_read_b64_tr_b16 a[150:151], v216 offset:37416\n"
    "v_cvt_pk_bf16_f32 v185, v162, v163\n"
    "v_mfma_f32_16x16x32_bf16 v[32:35], a[208:211], v[180:183], v[32:35]\n"
    "s_waitcnt lgkmcnt(2)\n"
    "buffer_store_dwordx4 a[248:251], v228, s[48:51], s56 offen offset:2048 nt\n"
    "v_exp_f32 v164, v164\n"
    "v_mfma_f32_16x16x32_bf16 v[32:35], a[176:179], v[176:179], v[32:35]\n"
    "ds_read_b64_tr_b16 a[152:153], v217 offset:33280\n"
    "v_lshlrev_b32 v238, 16, v252\n"
    "v_mfma_f32_16x16x32_bf16 v[36:39], a[180:183], v[176:179], v[36:39]\n"
    "s_add_u32 m0, s57, 16672\n"
    "v_exp_f32 v165, v165\n"
    "v_mfma_f32_16x16x32_bf16 v[36:39], a[212:215], v[180:183], v[36:39]\n"
    "buffer_load_dwordx4 v229, s[36:39], s52 offen offset:3072 lds\n"
    "v_mfma_f32_16x16x32_bf16 v[40:43], a[216:219], v[180:183], v[40:43]\n"
    "v_exp_f32 v166, v166\n"
    "v_mfma_f32_16x16x32_bf16 v[40:43], a[184:187], v[176:179], v[40:43]\n"
    "ds_read_b64_tr_b16 a[154:155], v217 offset:37408\n"
    "v_cvt_pk_bf16_f32 v186, v164, v165\n"
    "v_mfma_f32_16x16x32_bf16 v[44:47], a[188:191], v[176:179], v[44:47]\n"
    "v_exp_f32 v167, v167\n"
    "v_mfma_f32_16x16x32_bf16 v[44:47], a[220:223], v[180:183], v[44:47]\n"
    "ds_read_b64_tr_b16 a[156:157], v217 offset:33288\n"
    "v_cvt_pk_bf16_f32 v187, v166, v167\n"
    "v_mfma_f32_16x16x32_bf16 v[48:51], a[224:227], v[180:183], v[48:51]\n"
    "v_exp_f32 v168, v168\n"
    "v_mfma_f32_16x16x32_bf16 v[48:51], a[192:195], v[176:179], v[48:51]\n"
    "ds_read_b64_tr_b16 a[158:159], v217 offset:37416\n"
    "v_and_b32 v252, s80, v252\n"
    "v_mfma_f32_16x16x32_bf16 v[52:55], a[196:199], v[176:179], v[52:55]\n"
    "v_exp_f32 v169, v169\n"
    "v_mfma_f32_16x16x32_bf16 v[52:55], a[228:231], v[180:183], v[52:55]\n"
    "v_cvt_pk_bf16_f32 v188, v168, v169\n"
    "v_lshlrev_b32 v239, 16, v253\n"
    "v_mfma_f32_16x16x32_bf16 v[56:59], a[232:235], v[180:183], v[56:59]\n"
    "s_add_u32 m0, s58, 24768\n"
    "v_exp_f32 v170, v170\n"
    "v_mfma_f32_16x16x32_bf16 v[56:59], a[200:203], v[176:179], v[56:59]\n"
    "buffer_load_dwordx4 v229, s[40:43], s54 offen lds\n"
    "v_mfma_f32_16x16x32_bf16 v[60:63], a[204:207], v[176:179], v[60:63]\n"
    "v_exp_f32 v171, v171\n"
    "v_mfma_f32_16x16x32_bf16 v[60:63], a[236:239], v[180:183], v[60:63]\n"
    "v_cvt_pk_bf16_f32 v189, v170, v171\n"
    "v_and_b32 v253, s80, v253\n"
    "v_mfma_f32_16x16x32_bf16 v[160:163], a[80:83], a[48:51], -4.0\n"
    "v_exp_f32 v172, v172\n"
    "v_mfma_f32_16x16x32_bf16 v[160:163], a[84:87], a[52:55], v[160:163]\n"
    "v_lshlrev_b32 v240, 16, v254\n"
    "v_and_b32 v254, s80, v254\n"
    "v_mfma_f32_16x16x32_bf16 v[160:163], a[88:91], a[56:59], v[160:163]\n"
    "v_exp_f32 v173, v173\n"
    "v_mfma_f32_16x16x32_bf16 v[160:163], a[92:95], a[60:63], v[160:163]\n"
    "v_cvt_pk_bf16_f32 v190, v172, v173\n"
    "v_lshlrev_b32 v241, 16, v255\n"
    "v_mfma_f32_16x16x32_bf16 v[164:167], a[108:111], a[60:63], -4.0\n"
    "s_add_u32 m0, s58, 24776\n"
    "v_exp_f32 v174, v174\n"
    "v_mfma_f32_16x16x32_bf16 v[164:167], a[104:107], a[56:59], v[164:167]\n"
    "buffer_load_dwordx4 v229, s[40:43], s54 offen offset:1024 lds\n"
    "v_mfma_f32_16x16x32_bf16 v[164:167], a[100:103], a[52:55], v[164:167]\n"
    "s_add_u32 m0, s63, 19456\n"
    "v_exp_f32 v175, v175\n"
    "v_mfma_f32_16x16x32_bf16 v[164:167], a[96:99], a[48:51], v[164:167]\n"
    "buffer_load_dwordx4 v227, s[44:47], s55 offen offset:3072 lds\n"
    "v_mfma_f32_16x16x32_bf16 v[168:171], a[112:115], a[48:51], -4.0\n"
    "s_add_u32 s55, s55, 4096\n"
    "ds_read_b64_tr_b16 a[160:161], v218 offset:33280\n"
    "v_cvt_pk_bf16_f32 v191, v174, v175\n"
    "v_mfma_f32_16x16x32_bf16 v[168:171], a[116:119], a[52:55], v[168:171]\n"
    "v_exp_f32 v160, v160\n"
    "v_mfma_f32_16x16x32_bf16 v[168:171], a[120:123], a[56:59], v[168:171]\n"
    "ds_read_b64_tr_b16 a[162:163], v218 offset:37408\n"
    "ds_read_b64_tr_b16 a[164:165], v218 offset:33288\n"
    "v_mfma_f32_16x16x32_bf16 v[168:171], a[124:127], a[60:63], v[168:171]\n"
    "v_exp_f32 v161, v161\n"
    "v_mfma_f32_16x16x32_bf16 v[172:175], a[140:143], a[60:63], -4.0\n"
    "ds_read_b64_tr_b16 a[166:167], v218 offset:37416\n"
    "v_and_b32 v255, s80, v255\n"
    "v_mfma_f32_16x16x32_bf16 v[172:175], a[136:139], a[56:59], v[172:175]\n"
    "v_exp_f32 v162, v162\n"
    "v_mfma_f32_16x16x32_bf16 v[172:175], a[132:135], a[52:55], v[172:175]\n"
    "ds_read_b64_tr_b16 a[168:169], v219 offset:33280\n"
    "ds_read_b64_tr_b16 a[170:171], v219 offset:37408\n"
    "v_mfma_f32_16x16x32_bf16 v[172:175], a[128:131], a[48:51], v[172:175]\n"
    "v_exp_f32 v163, v163\n"
    "v_smfmac_f32_16x16x64_bf16 v[200:203], a[240:243], v[184:191], v237\n"
    "ds_write_b128 v228, v[248:251] offset:7168\n"
    "v_cvt_pk_bf16_f32 v176, v160, v161\n"
    "v_pk_mul_f32 v[238:239], v[238:239], s[76:77] op_sel_hi:[1,0]\n"
    "v_pk_mul_f32 v[252:253], v[252:253], s[76:77] op_sel_hi:[1,0]\n"
    "v_pk_mul_f32 v[240:241], v[240:241], s[76:77] op_sel_hi:[1,0]\n"
    "v_pk_mul_f32 v[254:255], v[254:255], s[76:77] op_sel_hi:[1,0]\n"
    "v_mfma_f32_16x16x32_bf16 v[64:67], a[208:211], v[188:191], v[64:67]\n"
    "v_exp_f32 v164, v164\n"
    "v_mfma_f32_16x16x32_bf16 v[64:67], a[176:179], v[184:187], v[64:67]\n"
    "ds_read_b64_tr_b16 a[172:173], v219 offset:33288\n"
    "v_cvt_pk_bf16_f32 v252, v238, v252\n"
    "v_mfma_f32_16x16x32_bf16 v[68:71], a[180:183], v[184:187], v[68:71]\n"
    "v_exp_f32 v165, v165\n"
    "v_mfma_f32_16x16x32_bf16 v[68:71], a[212:215], v[188:191], v[68:71]\n"
    "ds_read_b64_tr_b16 a[174:175], v219 offset:37416\n"
    "v_cvt_pk_bf16_f32 v177, v162, v163\n"
    "v_mfma_f32_16x16x32_bf16 v[72:75], a[216:219], v[188:191], v[72:75]\n"
    "v_exp_f32 v166, v166\n"
    "v_mfma_f32_16x16x32_bf16 v[72:75], a[184:187], v[184:187], v[72:75]\n"
    "v_cvt_pk_bf16_f32 v253, v239, v253\n"
    "v_cvt_pk_bf16_f32 v254, v240, v254\n"
    "v_mfma_f32_16x16x32_bf16 v[76:79], a[188:191], v[184:187], v[76:79]\n"
    "v_exp_f32 v167, v167\n"
    "v_mfma_f32_16x16x32_bf16 v[76:79], a[220:223], v[188:191], v[76:79]\n"
    "s_waitcnt vmcnt(10)\n"
    "ds_read_b128 v[244:247], v228 offset:20480\n"
    "v_cvt_pk_bf16_f32 v178, v164, v165\n"
    "v_mfma_f32_16x16x32_bf16 v[80:83], a[224:227], v[188:191], v[80:83]\n"
    "v_exp_f32 v168, v168\n"
    "v_mfma_f32_16x16x32_bf16 v[80:83], a[192:195], v[184:187], v[80:83]\n"
    "v_cvt_pk_bf16_f32 v179, v166, v167\n"
    "v_cvt_pk_bf16_f32 v255, v241, v255\n"
    "v_mfma_f32_16x16x32_bf16 v[84:87], a[196:199], v[184:187], v[84:87]\n"
    "v_exp_f32 v169, v169\n"
    "v_mfma_f32_16x16x32_bf16 v[84:87], a[228:231], v[188:191], v[84:87]\n"
    "v_cvt_pk_bf16_f32 v180, v168, v169\n"
    "v_mfma_f32_16x16x32_bf16 v[88:91], a[232:235], v[188:191], v[88:91]\n"
    "v_exp_f32 v170, v170\n"
    "v_mfma_f32_16x16x32_bf16 v[88:91], a[200:203], v[184:187], v[88:91]\n"
    "ds_read_b128 a[252:255], v227 offset:11264\n"
    "v_mfma_f32_16x16x32_bf16 v[92:95], a[204:207], v[184:187], v[92:95]\n"
    "v_exp_f32 v171, v171\n"
    "v_mfma_f32_16x16x32_bf16 v[92:95], a[236:239], v[188:191], v[92:95]\n"
    "v_cvt_pk_bf16_f32 v181, v170, v171\n"
    "v_mfma_f32_16x16x32_bf16 v[160:163], a[80:83], a[64:67], -4.0\n"
    "v_exp_f32 v172, v172\n"
    "v_mfma_f32_16x16x32_bf16 v[160:163], a[84:87], a[68:71], v[160:163]\n"
    "s_waitcnt lgkmcnt(1)\n"
    "v_lshlrev_b32 v238, 16, v244\n"
    "v_and_b32 v244, s80, v244\n"
    "v_mfma_f32_16x16x32_bf16 v[160:163], a[88:91], a[72:75], v[160:163]\n"
    "v_exp_f32 v173, v173\n"
    "v_mfma_f32_16x16x32_bf16 v[160:163], a[92:95], a[76:79], v[160:163]\n"
    "v_cvt_pk_bf16_f32 v182, v172, v173\n"
    "v_lshlrev_b32 v239, 16, v245\n"
    "v_mfma_f32_16x16x32_bf16 v[164:167], a[108:111], a[76:79], -4.0\n"
    "v_exp_f32 v174, v174\n"
    "v_mfma_f32_16x16x32_bf16 v[164:167], a[104:107], a[72:75], v[164:167]\n"
    "v_and_b32 v245, s80, v245\n"
    "v_lshlrev_b32 v240, 16, v246\n"
    "v_mfma_f32_16x16x32_bf16 v[164:167], a[100:103], a[68:71], v[164:167]\n"
    "s_waitcnt lgkmcnt(0)\n"
    "buffer_store_dwordx4 a[252:255], v228, s[48:51], s56 offen offset:3072 nt\n"
    "v_exp_f32 v175, v175\n"
    "v_mfma_f32_16x16x32_bf16 v[164:167], a[96:99], a[64:67], v[164:167]\n"
    "v_cvt_pk_bf16_f32 v183, v174, v175\n"
    "v_and_b32 v246, s80, v246\n"
    "v_mfma_f32_16x16x32_bf16 v[168:171], a[112:115], a[64:67], -4.0\n"
    "s_barrier\n"
    "v_mfma_f32_16x16x32_bf16 v[168:171], a[116:119], a[68:71], v[168:171]\n"
    "v_exp_f32 v160, v160\n"
    "v_mfma_f32_16x16x32_bf16 v[168:171], a[120:123], a[72:75], v[168:171]\n"
    "ds_read_b128 a[80:83], v212 offset:0\n"
    "ds_read_b128 a[84:87], v213 offset:0\n"
    "v_mfma_f32_16x16x32_bf16 v[168:171], a[124:127], a[76:79], v[168:171]\n"
    "v_exp_f32 v161, v161\n"
    "v_mfma_f32_16x16x32_bf16 v[172:175], a[140:143], a[76:79], -4.0\n"
    "ds_read_b128 a[88:91], v214 offset:0\n"
    "ds_read_b128 a[92:95], v215 offset:0\n"
    "v_mfma_f32_16x16x32_bf16 v[172:175], a[136:139], a[72:75], v[172:175]\n"
    "v_exp_f32 v162, v162\n"
    "v_mfma_f32_16x16x32_bf16 v[172:175], a[132:135], a[68:71], v[172:175]\n"
    "ds_read_b128 a[108:111], v215 offset:4160\n"
    "ds_read_b128 a[104:107], v214 offset:4160\n"
    "v_mfma_f32_16x16x32_bf16 v[172:175], a[128:131], a[64:67], v[172:175]\n"
    "v_exp_f32 v163, v163\n"
    "v_smfmac_f32_16x16x64_bf16 v[204:207], a[240:243], v[176:183], v237\n"
    "ds_read_b128 a[100:103], v213 offset:4160\n"
    "ds_read_b128 a[96:99], v212 offset:4160\n"
    "v_mfma_f32_16x16x32_bf16 v[96:99], a[208:211], v[180:183], v[96:99]\n"
    "v_exp_f32 v164, v164\n"
    "v_mfma_f32_16x16x32_bf16 v[96:99], a[176:179], v[176:179], v[96:99]\n"
    "v_cvt_pk_bf16_f32 v184, v160, v161\n"
    "v_cvt_pk_bf16_f32 v185, v162, v163\n"
    "v_mfma_f32_16x16x32_bf16 v[100:103], a[180:183], v[176:179], v[100:103]\n"
    "v_exp_f32 v165, v165\n"
    "v_mfma_f32_16x16x32_bf16 v[100:103], a[212:215], v[180:183], v[100:103]\n"
    "ds_read_b128 a[112:115], v212 offset:8320\n"
    "v_lshlrev_b32 v241, 16, v247\n"
    "v_mfma_f32_16x16x32_bf16 v[104:107], a[216:219], v[180:183], v[104:107]\n"
    "s_add_u32 m0, s58, 0\n"
    "v_exp_f32 v166, v166\n"
    "v_mfma_f32_16x16x32_bf16 v[104:107], a[184:187], v[176:179], v[104:107]\n"
    "buffer_load_dwordx4 v229, s[40:43], s53 offen lds\n"
    "v_mfma_f32_16x16x32_bf16 v[108:111], a[188:191], v[176:179], v[108:111]\n"
    "s_add_u32 m0, s63, 20480\n"
    "v_exp_f32 v167, v167\n"
    "v_mfma_f32_16x16x32_bf16 v[108:111], a[220:223], v[180:183], v[108:111]\n"
    "buffer_load_dwordx4 v224, s[44:47], s55 offen lds\n"
    "v_mfma_f32_16x16x32_bf16 v[112:115], a[224:227], v[180:183], v[112:115]\n"
    "s_add_u32 m0, s58, 8\n"
    "v_exp_f32 v168, v168\n"
    "v_mfma_f32_16x16x32_bf16 v[112:115], a[192:195], v[176:179], v[112:115]\n"
    "buffer_load_dwordx4 v229, s[40:43], s53 offen offset:1024 lds\n"
    "v_mfma_f32_16x16x32_bf16 v[116:119], a[196:199], v[176:179], v[116:119]\n"
    "v_exp_f32 v169, v169\n"
    "v_mfma_f32_16x16x32_bf16 v[116:119], a[228:231], v[180:183], v[116:119]\n"
    "ds_read_b128 a[116:119], v213 offset:8320\n"
    "ds_read_b128 a[120:123], v214 offset:8320\n"
    "v_mfma_f32_16x16x32_bf16 v[120:123], a[232:235], v[180:183], v[120:123]\n"
    "v_exp_f32 v170, v170\n"
    "v_mfma_f32_16x16x32_bf16 v[120:123], a[200:203], v[176:179], v[120:123]\n"
    "ds_read_b128 a[124:127], v215 offset:8320\n"
    "v_cvt_pk_bf16_f32 v186, v164, v165\n"
    "v_mfma_f32_16x16x32_bf16 v[124:127], a[204:207], v[176:179], v[124:127]\n"
    "v_exp_f32 v171, v171\n"
    "v_mfma_f32_16x16x32_bf16 v[124:127], a[236:239], v[180:183], v[124:127]\n"
    "s_waitcnt lgkmcnt(3)\n"
    "ds_read_b128 a[140:143], v215 offset:12480\n"
    "v_cvt_pk_bf16_f32 v187, v166, v167\n"
    "v_mfma_f32_16x16x32_bf16 v[160:163], a[80:83], a[0:3], -4.0\n"
    "v_exp_f32 v172, v172\n"
    "v_mfma_f32_16x16x32_bf16 v[160:163], a[84:87], a[4:7], v[160:163]\n"
    "ds_read_b128 a[136:139], v214 offset:12480\n"
    "ds_read_b128 a[132:135], v213 offset:12480\n"
    "v_mfma_f32_16x16x32_bf16 v[160:163], a[88:91], a[8:11], v[160:163]\n"
    "v_exp_f32 v173, v173\n"
    "v_mfma_f32_16x16x32_bf16 v[160:163], a[92:95], a[12:15], v[160:163]\n"
    "ds_read_b128 a[128:131], v212 offset:12480\n"
    "v_cvt_pk_bf16_f32 v188, v168, v169\n"
    "v_mfma_f32_16x16x32_bf16 v[164:167], a[108:111], a[12:15], -4.0\n"
    "v_exp_f32 v174, v174\n"
    "v_mfma_f32_16x16x32_bf16 v[164:167], a[104:107], a[8:11], v[164:167]\n"
    "v_cvt_pk_bf16_f32 v189, v170, v171\n"
    "v_cvt_pk_bf16_f32 v190, v172, v173\n"
    "v_mfma_f32_16x16x32_bf16 v[164:167], a[100:103], a[4:7], v[164:167]\n"
    "v_exp_f32 v175, v175\n"
    "v_mfma_f32_16x16x32_bf16 v[164:167], a[96:99], a[0:3], v[164:167]\n"
    "ds_write_b128 v228, v[252:255] offset:8192\n"
    "v_cvt_pk_bf16_f32 v191, v174, v175\n"
    "v_and_b32 v247, s80, v247\n"
    "v_pk_mul_f32 v[238:239], v[238:239], s[76:77] op_sel_hi:[1,0]\n"
    "v_pk_mul_f32 v[244:245], v[244:245], s[76:77] op_sel_hi:[1,0]\n"
    "v_pk_mul_f32 v[240:241], v[240:241], s[76:77] op_sel_hi:[1,0]\n"
    "v_pk_mul_f32 v[246:247], v[246:247], s[76:77] op_sel_hi:[1,0]\n"
    "v_mfma_f32_16x16x32_bf16 v[168:171], a[112:115], a[0:3], -4.0\n"
    "s_waitcnt lgkmcnt(4)\n"
    "v_cvt_pk_bf16_f32 v244, v238, v244\n"
    "v_cvt_pk_bf16_f32 v245, v239, v245\n"
    "v_mfma_f32_16x16x32_bf16 v[168:171], a[116:119], a[4:7], v[168:171]\n"
    "v_exp_f32 v160, v160\n"
    "v_mfma_f32_16x16x32_bf16 v[168:171], a[120:123], a[8:11], v[168:171]\n"
    "s_waitcnt vmcnt(12)\n"
    "ds_read_b128 v[248:251], v228 offset:21504\n"
    "v_cvt_pk_bf16_f32 v246, v240, v246\n"
    "v_mfma_f32_16x16x32_bf16 v[168:171], a[124:127], a[12:15], v[168:171]\n"
    "v_exp_f32 v161, v161\n"
    "v_mfma_f32_16x16x32_bf16 v[172:175], a[140:143], a[12:15], -4.0\n"
    "s_waitcnt lgkmcnt(2)\n"
    "v_cvt_pk_bf16_f32 v247, v241, v247\n"
    "v_cvt_pk_bf16_f32 v176, v160, v161\n"
    "v_mfma_f32_16x16x32_bf16 v[172:175], a[136:139], a[8:11], v[172:175]\n"
    "v_exp_f32 v162, v162\n"
    "v_mfma_f32_16x16x32_bf16 v[172:175], a[132:135], a[4:7], v[172:175]\n"
    "v_mfma_f32_16x16x32_bf16 v[172:175], a[128:131], a[0:3], v[172:175]\n"
    "v_exp_f32 v163, v163\n"
    "v_smfmac_f32_16x16x64_bf16 v[208:211], a[240:243], v[184:191], v237\n"
    "v_cvt_pk_bf16_f32 v177, v162, v163\n"
    "v_mfma_f32_16x16x32_bf16 v[128:131], a[208:211], v[188:191], v[128:131]\n"
    "v_exp_f32 v164, v164\n"
    "v_mfma_f32_16x16x32_bf16 v[128:131], a[176:179], v[184:187], v[128:131]\n"
    "ds_read_b64_tr_b16 a[208:209], v216 offset:49792\n"
    "ds_read_b64_tr_b16 a[210:211], v216 offset:53920\n"
    "v_mfma_f32_16x16x32_bf16 v[132:135], a[180:183], v[184:187], v[132:135]\n"
    "v_exp_f32 v165, v165\n"
    "v_mfma_f32_16x16x32_bf16 v[132:135], a[212:215], v[188:191], v[132:135]\n"
    "ds_read_b64_tr_b16 a[212:213], v216 offset:49800\n"
    "ds_read_b64_tr_b16 a[214:215], v216 offset:53928\n"
    "v_mfma_f32_16x16x32_bf16 v[136:139], a[216:219], v[188:191], v[136:139]\n"
    "v_exp_f32 v166, v166\n"
    "v_mfma_f32_16x16x32_bf16 v[136:139], a[184:187], v[184:187], v[136:139]\n"
    "ds_read_b64_tr_b16 a[216:217], v217 offset:49792\n"
    "v_cvt_pk_bf16_f32 v178, v164, v165\n"
    "v_mfma_f32_16x16x32_bf16 v[140:143], a[188:191], v[184:187], v[140:143]\n"
    "v_exp_f32 v167, v167\n"
    "v_mfma_f32_16x16x32_bf16 v[140:143], a[220:223], v[188:191], v[140:143]\n"
    "ds_read_b64_tr_b16 a[218:219], v217 offset:53920\n"
    "v_cvt_pk_bf16_f32 v179, v166, v167\n"
    "v_mfma_f32_16x16x32_bf16 v[144:147], a[224:227], v[188:191], v[144:147]\n"
    "v_exp_f32 v168, v168\n"
    "v_mfma_f32_16x16x32_bf16 v[144:147], a[192:195], v[184:187], v[144:147]\n"
    "ds_read_b64_tr_b16 a[220:221], v217 offset:49800\n"
    "ds_read_b64_tr_b16 a[222:223], v217 offset:53928\n"
    "v_mfma_f32_16x16x32_bf16 v[148:151], a[196:199], v[184:187], v[148:151]\n"
    "v_exp_f32 v169, v169\n"
    "v_mfma_f32_16x16x32_bf16 v[148:151], a[228:231], v[188:191], v[148:151]\n"
    "ds_read_b64_tr_b16 a[224:225], v218 offset:49792\n"
    "v_cvt_pk_bf16_f32 v180, v168, v169\n"
    "v_mfma_f32_16x16x32_bf16 v[152:155], a[232:235], v[188:191], v[152:155]\n"
    "v_exp_f32 v170, v170\n"
    "v_mfma_f32_16x16x32_bf16 v[152:155], a[200:203], v[184:187], v[152:155]\n"
    "ds_read_b64_tr_b16 a[226:227], v218 offset:53920\n"
    "ds_read_b64_tr_b16 a[228:229], v218 offset:49800\n"
    "v_mfma_f32_16x16x32_bf16 v[156:159], a[204:207], v[184:187], v[156:159]\n"
    "v_exp_f32 v171, v171\n"
    "v_mfma_f32_16x16x32_bf16 v[156:159], a[236:239], v[188:191], v[156:159]\n"
    "ds_read_b64_tr_b16 a[230:231], v218 offset:53928\n"
    "v_cvt_pk_bf16_f32 v181, v170, v171\n"
    "v_mfma_f32_16x16x32_bf16 v[160:163], a[80:83], a[16:19], -4.0\n"
    "v_exp_f32 v172, v172\n"
    "v_mfma_f32_16x16x32_bf16 v[160:163], a[84:87], a[20:23], v[160:163]\n"
    "ds_read_b64_tr_b16 a[232:233], v219 offset:49792\n"
    "ds_read_b64_tr_b16 a[234:235], v219 offset:53920\n"
    "v_mfma_f32_16x16x32_bf16 v[160:163], a[88:91], a[24:27], v[160:163]\n"
    "v_exp_f32 v173, v173\n"
    "v_mfma_f32_16x16x32_bf16 v[160:163], a[92:95], a[28:31], v[160:163]\n"
    "ds_read_b64_tr_b16 a[236:237], v219 offset:49800\n"
    "v_cvt_pk_bf16_f32 v182, v172, v173\n"
    "v_mfma_f32_16x16x32_bf16 v[164:167], a[108:111], a[28:31], -4.0\n"
    "v_exp_f32 v174, v174\n"
    "v_mfma_f32_16x16x32_bf16 v[164:167], a[104:107], a[24:27], v[164:167]\n"
    "s_waitcnt lgkmcnt(4)\n"
    "ds_read_b64_tr_b16 a[238:239], v219 offset:53928\n"
    "v_lshlrev_b32 v238, 16, v248\n"
    "v_mfma_f32_16x16x32_bf16 v[164:167], a[100:103], a[20:23], v[164:167]\n"
    "s_add_u32 m0, s63, 20480\n"
    "v_exp_f32 v175, v175\n"
    "v_mfma_f32_16x16x32_bf16 v[164:167], a[96:99], a[16:19], v[164:167]\n"
    "buffer_load_dwordx4 v225, s[44:47], s55 offen offset:1024 lds\n"
    "v_mfma_f32_16x16x32_bf16 v[168:171], a[112:115], a[16:19], -4.0\n"
    "s_add_u32 s52, s52, 16384\n"
    "v_cvt_pk_bf16_f32 v183, v174, v175\n"
    "v_and_b32 v248, s80, v248\n"
    "v_mfma_f32_16x16x32_bf16 v[168:171], a[116:119], a[20:23], v[168:171]\n"
    "v_exp_f32 v160, v160\n"
    "v_mfma_f32_16x16x32_bf16 v[168:171], a[120:123], a[24:27], v[168:171]\n"
    "s_add_u32 s54, s54, 16384\n"
    "ds_read_b128 a[244:247], v224 offset:12288\n"
    "v_lshlrev_b32 v239, 16, v249\n"
    "v_mfma_f32_16x16x32_bf16 v[168:171], a[124:127], a[28:31], v[168:171]\n"
    "v_exp_f32 v161, v161\n"
    "v_mfma_f32_16x16x32_bf16 v[172:175], a[140:143], a[28:31], -4.0\n"
    "s_add_u32 s53, s53, 16384\n"
    "v_and_b32 v249, s80, v249\n"
    "v_lshlrev_b32 v240, 16, v250\n"
    "v_mfma_f32_16x16x32_bf16 v[172:175], a[136:139], a[24:27], v[172:175]\n"
    "v_exp_f32 v162, v162\n"
    "v_mfma_f32_16x16x32_bf16 v[172:175], a[132:135], a[20:23], v[172:175]\n"
    "v_and_b32 v250, s80, v250\n"
    "v_lshlrev_b32 v241, 16, v251\n"
    "v_mfma_f32_16x16x32_bf16 v[172:175], a[128:131], a[16:19], v[172:175]\n"
    "v_exp_f32 v163, v163\n"
    "v_smfmac_f32_16x16x64_bf16 v[192:195], a[240:243], v[176:183], v237\n"
    "ds_write_b128 v228, v[244:247] offset:9216\n"
    "v_and_b32 v251, s80, v251\n"
    "v_pk_mul_f32 v[238:239], v[238:239], s[76:77] op_sel_hi:[1,0]\n"
    "v_pk_mul_f32 v[248:249], v[248:249], s[76:77] op_sel_hi:[1,0]\n"
    "v_pk_mul_f32 v[240:241], v[240:241], s[76:77] op_sel_hi:[1,0]\n"
    "v_pk_mul_f32 v[250:251], v[250:251], s[76:77] op_sel_hi:[1,0]\n"
    "v_mfma_f32_16x16x32_bf16 v[0:3], a[208:211], v[180:183], v[0:3]\n"
    "v_exp_f32 v164, v164\n"
    "v_mfma_f32_16x16x32_bf16 v[0:3], a[144:147], v[176:179], v[0:3]\n"
    "s_add_u32 s56, s64, 8192\n"
    "v_cvt_pk_bf16_f32 v248, v238, v248\n"
    "v_cvt_pk_bf16_f32 v249, v239, v249\n"
    "v_mfma_f32_16x16x32_bf16 v[4:7], a[148:151], v[176:179], v[4:7]\n"
    "s_waitcnt lgkmcnt(1)\n"
    "buffer_store_dwordx4 a[244:247], v228, s[48:51], s56 offen nt\n"
    "v_exp_f32 v165, v165\n"
    "v_mfma_f32_16x16x32_bf16 v[4:7], a[212:215], v[180:183], v[4:7]\n"
    "v_cvt_pk_bf16_f32 v184, v160, v161\n"
    "v_cvt_pk_bf16_f32 v185, v162, v163\n"
    "v_mfma_f32_16x16x32_bf16 v[8:11], a[216:219], v[180:183], v[8:11]\n"
    "v_exp_f32 v166, v166\n"
    "v_mfma_f32_16x16x32_bf16 v[8:11], a[152:155], v[176:179], v[8:11]\n"
    "v_cvt_pk_bf16_f32 v250, v240, v250\n"
    "v_cvt_pk_bf16_f32 v251, v241, v251\n"
    "v_mfma_f32_16x16x32_bf16 v[12:15], a[156:159], v[176:179], v[12:15]\n"
    "v_exp_f32 v167, v167\n"
    "v_mfma_f32_16x16x32_bf16 v[12:15], a[220:223], v[180:183], v[12:15]\n"
    "s_barrier\n"
    "v_mfma_f32_16x16x32_bf16 v[16:19], a[224:227], v[180:183], v[16:19]\n"
    "s_add_u32 m0, s57, 0\n"
    "v_exp_f32 v168, v168\n"
    "v_mfma_f32_16x16x32_bf16 v[16:19], a[160:163], v[176:179], v[16:19]\n"
    "buffer_load_dwordx4 v229, s[36:39], s52 offen lds\n"
    "v_mfma_f32_16x16x32_bf16 v[20:23], a[164:167], v[176:179], v[20:23]\n"
    "v_exp_f32 v169, v169\n"
    "v_mfma_f32_16x16x32_bf16 v[20:23], a[228:231], v[180:183], v[20:23]\n"
    "s_waitcnt vmcnt(7)\n"
    "ds_read_b128 v[252:255], v228 offset:22528\n"
    "v_cvt_pk_bf16_f32 v186, v164, v165\n"
    "v_mfma_f32_16x16x32_bf16 v[24:27], a[232:235], v[180:183], v[24:27]\n"
    "v_exp_f32 v170, v170\n"
    "v_mfma_f32_16x16x32_bf16 v[24:27], a[168:171], v[176:179], v[24:27]\n"
    "v_cvt_pk_bf16_f32 v187, v166, v167\n"
    "v_cvt_pk_bf16_f32 v188, v168, v169\n"
    "v_mfma_f32_16x16x32_bf16 v[28:31], a[172:175], v[176:179], v[28:31]\n"
    "v_exp_f32 v171, v171\n"
    "v_mfma_f32_16x16x32_bf16 v[28:31], a[236:239], v[180:183], v[28:31]\n"
    "v_cvt_pk_bf16_f32 v189, v170, v171\n"
    "v_mfma_f32_16x16x32_bf16 v[160:163], a[80:83], a[32:35], -4.0\n"
    "v_exp_f32 v172, v172\n"
    "v_mfma_f32_16x16x32_bf16 v[160:163], a[84:87], a[36:39], v[160:163]\n"
    "v_mfma_f32_16x16x32_bf16 v[160:163], a[88:91], a[40:43], v[160:163]\n"
    "s_add_u32 m0, s57, 0\n"
    "v_exp_f32 v173, v173\n"
    "v_mfma_f32_16x16x32_bf16 v[160:163], a[92:95], a[44:47], v[160:163]\n"
    "buffer_load_dwordx4 v229, s[36:39], s52 offen offset:1024 lds\n"
    "v_mfma_f32_16x16x32_bf16 v[164:167], a[108:111], a[44:47], -4.0\n"
    "v_exp_f32 v174, v174\n"
    "v_mfma_f32_16x16x32_bf16 v[164:167], a[104:107], a[40:43], v[164:167]\n"
    "s_waitcnt lgkmcnt(0)\n"
    "v_cvt_pk_bf16_f32 v190, v172, v173\n"
    "v_lshlrev_b32 v238, 16, v252\n"
    "v_mfma_f32_16x16x32_bf16 v[164:167], a[100:103], a[36:39], v[164:167]\n"
    "v_exp_f32 v175, v175\n"
    "v_mfma_f32_16x16x32_bf16 v[164:167], a[96:99], a[32:35], v[164:167]\n"
    "v_cvt_pk_bf16_f32 v191, v174, v175\n"
    "v_and_b32 v252, s80, v252\n"
    "v_mfma_f32_16x16x32_bf16 v[168:171], a[112:115], a[32:35], -4.0\n"
    "v_lshlrev_b32 v239, 16, v253\n"
    "v_and_b32 v253, s80, v253\n"
    "v_mfma_f32_16x16x32_bf16 v[168:171], a[116:119], a[36:39], v[168:171]\n"
    "v_exp_f32 v160, v160\n"
    "v_mfma_f32_16x16x32_bf16 v[168:171], a[120:123], a[40:43], v[168:171]\n"
    "v_lshlrev_b32 v240, 16, v254\n"
    "v_and_b32 v254, s80, v254\n"
    "v_mfma_f32_16x16x32_bf16 v[168:171], a[124:127], a[44:47], v[168:171]\n"
    "s_add_u32 m0, s57, 32\n"
    "v_exp_f32 v161, v161\n"
    "v_mfma_f32_16x16x32_bf16 v[172:175], a[140:143], a[44:47], -4.0\n"
    "buffer_load_dwordx4 v229, s[36:39], s52 offen offset:2048 lds\n"
    "v_mfma_f32_16x16x32_bf16 v[172:175], a[136:139], a[40:43], v[172:175]\n"
    "v_exp_f32 v162, v162\n"
    "v_mfma_f32_16x16x32_bf16 v[172:175], a[132:135], a[36:39], v[172:175]\n"
    "v_cvt_pk_bf16_f32 v176, v160, v161\n"
    "v_lshlrev_b32 v241, 16, v255\n"
    "v_mfma_f32_16x16x32_bf16 v[172:175], a[128:131], a[32:35], v[172:175]\n"
    "v_exp_f32 v163, v163\n"
    "v_smfmac_f32_16x16x64_bf16 v[196:199], a[240:243], v[184:191], v237\n"
    "ds_read_b64_tr_b16 a[176:177], v216 offset:41536\n"
    "v_cvt_pk_bf16_f32 v177, v162, v163\n"
    "v_mfma_f32_16x16x32_bf16 v[32:35], a[208:211], v[188:191], v[32:35]\n"
    "v_exp_f32 v164, v164\n"
    "v_mfma_f32_16x16x32_bf16 v[32:35], a[144:147], v[184:187], v[32:35]\n"
    "ds_write_b128 v228, v[248:251] offset:10240\n"
    "v_and_b32 v255, s80, v255\n"
    "v_pk_mul_f32 v[238:239], v[238:239], s[76:77] op_sel_hi:[1,0]\n"
    "v_pk_mul_f32 v[252:253], v[252:253], s[76:77] op_sel_hi:[1,0]\n"
    "v_pk_mul_f32 v[240:241], v[240:241], s[76:77] op_sel_hi:[1,0]\n"
    "v_pk_mul_f32 v[254:255], v[254:255], s[76:77] op_sel_hi:[1,0]\n"
    "v_mfma_f32_16x16x32_bf16 v[36:39], a[148:151], v[184:187], v[36:39]\n"
    "s_add_u32 m0, s57, 32\n"
    "v_exp_f32 v165, v165\n"
    "v_mfma_f32_16x16x32_bf16 v[36:39], a[212:215], v[188:191], v[36:39]\n"
    "buffer_load_dwordx4 v229, s[36:39], s52 offen offset:3072 lds\n"
    "v_mfma_f32_16x16x32_bf16 v[40:43], a[216:219], v[188:191], v[40:43]\n"
    "v_exp_f32 v166, v166\n"
    "v_mfma_f32_16x16x32_bf16 v[40:43], a[152:155], v[184:187], v[40:43]\n"
    "v_cvt_pk_bf16_f32 v252, v238, v252\n"
    "v_cvt_pk_bf16_f32 v253, v239, v253\n"
    "v_mfma_f32_16x16x32_bf16 v[44:47], a[156:159], v[184:187], v[44:47]\n"
    "s_add_u32 m0, s63, 20480\n"
    "v_exp_f32 v167, v167\n"
    "v_mfma_f32_16x16x32_bf16 v[44:47], a[220:223], v[188:191], v[44:47]\n"
    "buffer_load_dwordx4 v226, s[44:47], s55 offen offset:2048 lds\n"
    "v_mfma_f32_16x16x32_bf16 v[48:51], a[224:227], v[188:191], v[48:51]\n"
    "v_exp_f32 v168, v168\n"
    "v_mfma_f32_16x16x32_bf16 v[48:51], a[160:163], v[184:187], v[48:51]\n"
    "v_cvt_pk_bf16_f32 v178, v164, v165\n"
    "v_cvt_pk_bf16_f32 v179, v166, v167\n"
    "v_mfma_f32_16x16x32_bf16 v[52:55], a[164:167], v[184:187], v[52:55]\n"
    "v_exp_f32 v169, v169\n"
    "v_mfma_f32_16x16x32_bf16 v[52:55], a[228:231], v[188:191], v[52:55]\n"
    "ds_read_b64_tr_b16 a[178:179], v216 offset:45664\n"
    "ds_read_b64_tr_b16 a[180:181], v216 offset:41544\n"
    "v_mfma_f32_16x16x32_bf16 v[56:59], a[232:235], v[188:191], v[56:59]\n"
    "s_add_u32 m0, s58, 16512\n"
    "v_exp_f32 v170, v170\n"
    "v_mfma_f32_16x16x32_bf16 v[56:59], a[168:171], v[184:187], v[56:59]\n"
    "buffer_load_dwordx4 v229, s[40:43], s54 offen lds\n"
    "v_mfma_f32_16x16x32_bf16 v[60:63], a[172:175], v[184:187], v[60:63]\n"
    "v_exp_f32 v171, v171\n"
    "v_mfma_f32_16x16x32_bf16 v[60:63], a[236:239], v[188:191], v[60:63]\n"
    "v_cvt_pk_bf16_f32 v180, v168, v169\n"
    "v_cvt_pk_bf16_f32 v181, v170, v171\n"
    "v_mfma_f32_16x16x32_bf16 v[160:163], a[80:83], a[48:51], -4.0\n"
    "v_exp_f32 v172, v172\n"
    "v_mfma_f32_16x16x32_bf16 v[160:163], a[84:87], a[52:55], v[160:163]\n"
    "ds_read_b64_tr_b16 a[182:183], v216 offset:45672\n"
    "ds_read_b64_tr_b16 a[184:185], v217 offset:41536\n"
    "v_mfma_f32_16x16x32_bf16 v[160:163], a[88:91], a[56:59], v[160:163]\n"
    "v_exp_f32 v173, v173\n"
    "v_mfma_f32_16x16x32_bf16 v[160:163], a[92:95], a[60:63], v[160:163]\n"
    "ds_read_b128 a[248:251], v225 offset:13312\n"
    "v_cvt_pk_bf16_f32 v182, v172, v173\n"
    "v_mfma_f32_16x16x32_bf16 v[164:167], a[108:111], a[60:63], -4.0\n"
    "s_add_u32 m0, s58, 16520\n"
    "v_exp_f32 v174, v174\n"
    "v_mfma_f32_16x16x32_bf16 v[164:167], a[104:107], a[56:59], v[164:167]\n"
    "buffer_load_dwordx4 v229, s[40:43], s54 offen offset:1024 lds\n"
    "v_mfma_f32_16x16x32_bf16 v[164:167], a[100:103], a[52:55], v[164:167]\n"
    "v_exp_f32 v175, v175\n"
    "v_mfma_f32_16x16x32_bf16 v[164:167], a[96:99], a[48:51], v[164:167]\n"
    "ds_read_b64_tr_b16 a[186:187], v217 offset:45664\n"
    "v_cvt_pk_bf16_f32 v183, v174, v175\n"
    "v_mfma_f32_16x16x32_bf16 v[168:171], a[112:115], a[48:51], -4.0\n"
    "s_waitcnt vmcnt(10)\n"
    "ds_read_b64_tr_b16 a[188:189], v217 offset:41544\n"
    "ds_read_b128 v[244:247], v228 offset:20480\n"
    "v_mfma_f32_16x16x32_bf16 v[168:171], a[116:119], a[52:55], v[168:171]\n"
    "v_exp_f32 v160, v160\n"
    "v_mfma_f32_16x16x32_bf16 v[168:171], a[120:123], a[56:59], v[168:171]\n"
    "ds_read_b64_tr_b16 a[190:191], v217 offset:45672\n"
    "ds_read_b64_tr_b16 a[192:193], v218 offset:41536\n"
    "v_mfma_f32_16x16x32_bf16 v[168:171], a[124:127], a[60:63], v[168:171]\n"
    "v_exp_f32 v161, v161\n"
    "v_mfma_f32_16x16x32_bf16 v[172:175], a[140:143], a[60:63], -4.0\n"
    "ds_read_b64_tr_b16 a[194:195], v218 offset:45664\n"
    "ds_read_b64_tr_b16 a[196:197], v218 offset:41544\n"
    "v_mfma_f32_16x16x32_bf16 v[172:175], a[136:139], a[56:59], v[172:175]\n"
    "s_waitcnt lgkmcnt(7)\n"
    "buffer_store_dwordx4 a[248:251], v228, s[48:51], s56 offen offset:1024 nt\n"
    "v_exp_f32 v162, v162\n"
    "v_mfma_f32_16x16x32_bf16 v[172:175], a[132:135], a[52:55], v[172:175]\n"
    "ds_read_b64_tr_b16 a[198:199], v218 offset:45672\n"
    "ds_read_b64_tr_b16 a[200:201], v219 offset:41536\n"
    "v_mfma_f32_16x16x32_bf16 v[172:175], a[128:131], a[48:51], v[172:175]\n"
    "v_exp_f32 v163, v163\n"
    "v_smfmac_f32_16x16x64_bf16 v[200:203], a[240:243], v[176:183], v237\n"
    "ds_read_b64_tr_b16 a[202:203], v219 offset:45664\n"
    "v_cvt_pk_bf16_f32 v254, v240, v254\n"
    "v_mfma_f32_16x16x32_bf16 v[64:67], a[208:211], v[180:183], v[64:67]\n"
    "v_exp_f32 v164, v164\n"
    "v_mfma_f32_16x16x32_bf16 v[64:67], a[144:147], v[176:179], v[64:67]\n"
    "ds_read_b64_tr_b16 a[204:205], v219 offset:41544\n"
    "v_cvt_pk_bf16_f32 v255, v241, v255\n"
    "v_mfma_f32_16x16x32_bf16 v[68:71], a[148:151], v[176:179], v[68:71]\n"
    "v_exp_f32 v165, v165\n"
    "v_mfma_f32_16x16x32_bf16 v[68:71], a[212:215], v[180:183], v[68:71]\n"
    "ds_read_b64_tr_b16 a[206:207], v219 offset:45672\n"
    "v_cvt_pk_bf16_f32 v184, v160, v161\n"
    "v_mfma_f32_16x16x32_bf16 v[72:75], a[216:219], v[180:183], v[72:75]\n"
    "v_exp_f32 v166, v166\n"
    "v_mfma_f32_16x16x32_bf16 v[72:75], a[152:155], v[176:179], v[72:75]\n"
    "v_cvt_pk_bf16_f32 v185, v162, v163\n"
    "v_cvt_pk_bf16_f32 v186, v164, v165\n"
    "v_mfma_f32_16x16x32_bf16 v[76:79], a[156:159], v[176:179], v[76:79]\n"
    "v_exp_f32 v167, v167\n"
    "v_mfma_f32_16x16x32_bf16 v[76:79], a[220:223], v[180:183], v[76:79]\n"
    "s_waitcnt lgkmcnt(2)\n"
    "v_cvt_pk_bf16_f32 v187, v166, v167\n"
    "v_lshlrev_b32 v238, 16, v244\n"
    "v_mfma_f32_16x16x32_bf16 v[80:83], a[224:227], v[180:183], v[80:83]\n"
    "v_exp_f32 v168, v168\n"
    "v_mfma_f32_16x16x32_bf16 v[80:83], a[160:163], v[176:179], v[80:83]\n"
    "v_and_b32 v244, s80, v244\n"
    "v_lshlrev_b32 v239, 16, v245\n"
    "v_mfma_f32_16x16x32_bf16 v[84:87], a[164:167], v[176:179], v[84:87]\n"
    "v_exp_f32 v169, v169\n"
    "v_mfma_f32_16x16x32_bf16 v[84:87], a[228:231], v[180:183], v[84:87]\n"
    "v_cvt_pk_bf16_f32 v188, v168, v169\n"
    "v_and_b32 v245, s80, v245\n"
    "v_mfma_f32_16x16x32_bf16 v[88:91], a[232:235], v[180:183], v[88:91]\n"
    "v_exp_f32 v170, v170\n"
    "v_mfma_f32_16x16x32_bf16 v[88:91], a[168:171], v[176:179], v[88:91]\n"
    "v_lshlrev_b32 v240, 16, v246\n"
    "v_and_b32 v246, s80, v246\n"
    "v_mfma_f32_16x16x32_bf16 v[92:95], a[172:175], v[176:179], v[92:95]\n"
    "v_exp_f32 v171, v171\n"
    "v_mfma_f32_16x16x32_bf16 v[92:95], a[236:239], v[180:183], v[92:95]\n"
    "v_cvt_pk_bf16_f32 v189, v170, v171\n"
    "v_lshlrev_b32 v241, 16, v247\n"
    "v_mfma_f32_16x16x32_bf16 v[160:163], a[80:83], a[64:67], -4.0\n"
    "v_exp_f32 v172, v172\n"
    "v_mfma_f32_16x16x32_bf16 v[160:163], a[84:87], a[68:71], v[160:163]\n"
    "ds_write_b128 v228, v[252:255] offset:11264\n"
    "v_and_b32 v247, s80, v247\n"
    "v_pk_mul_f32 v[238:239], v[238:239], s[76:77] op_sel_hi:[1,0]\n"
    "v_pk_mul_f32 v[244:245], v[244:245], s[76:77] op_sel_hi:[1,0]\n"
    "v_pk_mul_f32 v[240:241], v[240:241], s[76:77] op_sel_hi:[1,0]\n"
    "v_pk_mul_f32 v[246:247], v[246:247], s[76:77] op_sel_hi:[1,0]\n"
    "v_mfma_f32_16x16x32_bf16 v[160:163], a[88:91], a[72:75], v[160:163]\n"
    "v_exp_f32 v173, v173\n"
    "v_mfma_f32_16x16x32_bf16 v[160:163], a[92:95], a[76:79], v[160:163]\n"
    "v_cvt_pk_bf16_f32 v190, v172, v173\n"
    "v_cvt_pk_bf16_f32 v244, v238, v244\n"
    "v_mfma_f32_16x16x32_bf16 v[164:167], a[108:111], a[76:79], -4.0\n"
    "v_exp_f32 v174, v174\n"
    "v_mfma_f32_16x16x32_bf16 v[164:167], a[104:107], a[72:75], v[164:167]\n"
    "v_cvt_pk_bf16_f32 v245, v239, v245\n"
    "v_cvt_pk_bf16_f32 v246, v240, v246\n"
    "v_mfma_f32_16x16x32_bf16 v[164:167], a[100:103], a[68:71], v[164:167]\n"
    "s_add_u32 m0, s63, 17408\n"
    "v_exp_f32 v175, v175\n"
    "v_mfma_f32_16x16x32_bf16 v[164:167], a[96:99], a[64:67], v[164:167]\n"
    "s_waitcnt lgkmcnt(1)\n"
    "buffer_load_dwordx4 v227, s[44:47], s55 offen offset:3072 lds\n"
    "v_mfma_f32_16x16x32_bf16 v[168:171], a[112:115], a[64:67], -4.0\n"
    "s_add_u32 s55, s55, 4096\n"
    "s_barrier\n"
    "v_mfma_f32_16x16x32_bf16 v[168:171], a[116:119], a[68:71], v[168:171]\n"
    "v_exp_f32 v160, v160\n"
    "v_mfma_f32_16x16x32_bf16 v[168:171], a[120:123], a[72:75], v[168:171]\n"
    "v_cvt_pk_bf16_f32 v191, v174, v175\n"
    "v_cvt_pk_bf16_f32 v247, v241, v247\n"
    "v_mfma_f32_16x16x32_bf16 v[168:171], a[124:127], a[76:79], v[168:171]\n"
    "v_exp_f32 v161, v161\n"
    "v_mfma_f32_16x16x32_bf16 v[172:175], a[140:143], a[76:79], -4.0\n"
    "ds_read_b128 a[80:83], v212 offset:16640\n"
    "ds_read_b128 a[84:87], v213 offset:16640\n"
    "v_mfma_f32_16x16x32_bf16 v[172:175], a[136:139], a[72:75], v[172:175]\n"
    "v_exp_f32 v162, v162\n"
    "v_mfma_f32_16x16x32_bf16 v[172:175], a[132:135], a[68:71], v[172:175]\n"
    "ds_read_b128 a[88:91], v214 offset:16640\n"
    "ds_read_b128 a[92:95], v215 offset:16640\n"
    "v_mfma_f32_16x16x32_bf16 v[172:175], a[128:131], a[64:67], v[172:175]\n"
    "v_exp_f32 v163, v163\n"
    "v_smfmac_f32_16x16x64_bf16 v[204:207], a[240:243], v[184:191], v237\n"
    "ds_read_b128 a[108:111], v215 offset:20800\n"
    "ds_read_b128 a[104:107], v214 offset:20800\n"
    "v_mfma_f32_16x16x32_bf16 v[96:99], a[208:211], v[188:191], v[96:99]\n"
    "v_exp_f32 v164, v164\n"
    "v_mfma_f32_16x16x32_bf16 v[96:99], a[144:147], v[184:187], v[96:99]\n"
    "ds_read_b128 a[100:103], v213 offset:20800\n"
    "ds_read_b128 a[96:99], v212 offset:20800\n"
    "v_mfma_f32_16x16x32_bf16 v[100:103], a[148:151], v[184:187], v[100:103]\n"
    "v_exp_f32 v165, v165\n"
    "v_mfma_f32_16x16x32_bf16 v[100:103], a[212:215], v[188:191], v[100:103]\n"
    "v_cvt_pk_bf16_f32 v176, v160, v161\n"
    "v_cvt_pk_bf16_f32 v177, v162, v163\n"
    "v_mfma_f32_16x16x32_bf16 v[104:107], a[216:219], v[188:191], v[104:107]\n"
    "s_add_u32 m0, s58, 8256\n"
    "v_exp_f32 v166, v166\n"
    "v_mfma_f32_16x16x32_bf16 v[104:107], a[152:155], v[184:187], v[104:107]\n"
    "buffer_load_dwordx4 v229, s[40:43], s53 offen lds\n"
    "v_mfma_f32_16x16x32_bf16 v[108:111], a[156:159], v[184:187], v[108:111]\n"
    "v_exp_f32 v167, v167\n"
    "v_mfma_f32_16x16x32_bf16 v[108:111], a[220:223], v[188:191], v[108:111]\n"
    "s_waitcnt vmcnt(11)\n"
    "ds_read_b128 v[248:251], v228 offset:21504\n"
    "ds_read_b128 a[112:115], v212 offset:24960\n"
    "v_mfma_f32_16x16x32_bf16 v[112:115], a[224:227], v[188:191], v[112:115]\n"
    "s_add_u32 m0, s58, 8264\n"
    "v_exp_f32 v168, v168\n"
    "v_mfma_f32_16x16x32_bf16 v[112:115], a[160:163], v[184:187], v[112:115]\n"
    "buffer_load_dwordx4 v229, s[40:43], s53 offen offset:1024 lds\n"
    "v_mfma_f32_16x16x32_bf16 v[116:119], a[164:167], v[184:187], v[116:119]\n"
    "v_exp_f32 v169, v169\n"
    "v_mfma_f32_16x16x32_bf16 v[116:119], a[228:231], v[188:191], v[116:119]\n"
    "ds_read_b128 a[116:119], v213 offset:24960\n"
    "ds_read_b128 a[120:123], v214 offset:24960\n"
    "v_mfma_f32_16x16x32_bf16 v[120:123], a[232:235], v[188:191], v[120:123]\n"
    "v_exp_f32 v170, v170\n"
    "v_mfma_f32_16x16x32_bf16 v[120:123], a[168:171], v[184:187], v[120:123]\n"
    "ds_read_b128 a[252:255], v226 offset:14336\n"
    "ds_read_b128 a[124:127], v215 offset:24960\n"
    "v_mfma_f32_16x16x32_bf16 v[124:127], a[172:175], v[184:187], v[124:127]\n"
    "v_exp_f32 v171, v171\n"
    "v_mfma_f32_16x16x32_bf16 v[124:127], a[236:239], v[188:191], v[124:127]\n"
    "s_waitcnt lgkmcnt(4)\n"
    "v_cvt_pk_bf16_f32 v178, v164, v165\n"
    "v_cvt_pk_bf16_f32 v179, v166, v167\n"
    "v_mfma_f32_16x16x32_bf16 v[160:163], a[80:83], a[0:3], -4.0\n"
    "v_exp_f32 v172, v172\n"
    "v_mfma_f32_16x16x32_bf16 v[160:163], a[84:87], a[4:7], v[160:163]\n"
    "ds_read_b128 a[140:143], v215 offset:29120\n"
    "ds_read_b128 a[136:139], v214 offset:29120\n"
    "v_mfma_f32_16x16x32_bf16 v[160:163], a[88:91], a[8:11], v[160:163]\n"
    "v_exp_f32 v173, v173\n"
    "v_mfma_f32_16x16x32_bf16 v[160:163], a[92:95], a[12:15], v[160:163]\n"
    "ds_read_b128 a[132:135], v213 offset:29120\n"
    "ds_read_b128 a[128:131], v212 offset:29120\n"
    "v_mfma_f32_16x16x32_bf16 v[164:167], a[108:111], a[12:15], -4.0\n"
    "v_exp_f32 v174, v174\n"
    "v_mfma_f32_16x16x32_bf16 v[164:167], a[104:107], a[8:11], v[164:167]\n"
    "v_cvt_pk_bf16_f32 v180, v168, v169\n"
    "v_cvt_pk_bf16_f32 v181, v170, v171\n"
    "v_mfma_f32_16x16x32_bf16 v[164:167], a[100:103], a[4:7], v[164:167]\n"
    "s_waitcnt lgkmcnt(4)\n"
    "buffer_store_dwordx4 a[252:255], v228, s[48:51], s56 offen offset:2048 nt\n"
    "v_exp_f32 v175, v175\n"
    "v_mfma_f32_16x16x32_bf16 v[164:167], a[96:99], a[0:3], v[164:167]\n"
    "v_cvt_pk_bf16_f32 v182, v172, v173\n"
    "v_cvt_pk_bf16_f32 v183, v174, v175\n"
    "v_mfma_f32_16x16x32_bf16 v[168:171], a[112:115], a[0:3], -4.0\n"
    "v_lshlrev_b32 v238, 16, v248\n"
    "v_and_b32 v248, s80, v248\n"
    "v_mfma_f32_16x16x32_bf16 v[168:171], a[116:119], a[4:7], v[168:171]\n"
    "v_exp_f32 v160, v160\n"
    "v_mfma_f32_16x16x32_bf16 v[168:171], a[120:123], a[8:11], v[168:171]\n"
    "v_lshlrev_b32 v239, 16, v249\n"
    "v_and_b32 v249, s80, v249\n"
    "v_mfma_f32_16x16x32_bf16 v[168:171], a[124:127], a[12:15], v[168:171]\n"
    "s_waitcnt lgkmcnt(0)\n"
    "v_exp_f32 v161, v161\n"
    "v_mfma_f32_16x16x32_bf16 v[172:175], a[140:143], a[12:15], -4.0\n"
    "v_cvt_pk_bf16_f32 v184, v160, v161\n"
    "v_lshlrev_b32 v240, 16, v250\n"
    "v_mfma_f32_16x16x32_bf16 v[172:175], a[136:139], a[8:11], v[172:175]\n"
    "v_exp_f32 v162, v162\n"
    "v_mfma_f32_16x16x32_bf16 v[172:175], a[132:135], a[4:7], v[172:175]\n"
    "v_and_b32 v250, s80, v250\n"
    "v_lshlrev_b32 v241, 16, v251\n"
    "v_mfma_f32_16x16x32_bf16 v[172:175], a[128:131], a[0:3], v[172:175]\n"
    "v_exp_f32 v163, v163\n"
    "v_smfmac_f32_16x16x64_bf16 v[208:211], a[240:243], v[176:183], v237\n"
    "v_cvt_pk_bf16_f32 v185, v162, v163\n"
    "v_mfma_f32_16x16x32_bf16 v[128:131], a[208:211], v[180:183], v[128:131]\n"
    "v_exp_f32 v164, v164\n"
    "v_mfma_f32_16x16x32_bf16 v[128:131], a[144:147], v[176:179], v[128:131]\n"
    "ds_read_b64_tr_b16 a[208:209], v216 offset:58048\n"
    "v_and_b32 v251, s80, v251\n"
    "v_mfma_f32_16x16x32_bf16 v[132:135], a[148:151], v[176:179], v[132:135]\n"
    "v_exp_f32 v165, v165\n"
    "v_mfma_f32_16x16x32_bf16 v[132:135], a[212:215], v[180:183], v[132:135]\n"
    "ds_write_b128 v228, v[244:247] offset:12288\n"
    "v_cvt_pk_bf16_f32 v186, v164, v165\n"
    "v_pk_mul_f32 v[238:239], v[238:239], s[76:77] op_sel_hi:[1,0]\n"
    "v_pk_mul_f32 v[248:249], v[248:249], s[76:77] op_sel_hi:[1,0]\n"
    "v_pk_mul_f32 v[240:241], v[240:241], s[76:77] op_sel_hi:[1,0]\n"
    "v_pk_mul_f32 v[250:251], v[250:251], s[76:77] op_sel_hi:[1,0]\n"
    "v_mfma_f32_16x16x32_bf16 v[136:139], a[216:219], v[180:183], v[136:139]\n"
    "v_exp_f32 v166, v166\n"
    "v_mfma_f32_16x16x32_bf16 v[136:139], a[152:155], v[176:179], v[136:139]\n"
    "ds_read_b64_tr_b16 a[210:211], v216 offset:62176\n"
    "ds_read_b64_tr_b16 a[212:213], v216 offset:58056\n"
    "v_mfma_f32_16x16x32_bf16 v[140:143], a[156:159], v[176:179], v[140:143]\n"
    "s_add_u32 m0, s63, 21504\n"
    "v_exp_f32 v167, v167\n"
    "v_mfma_f32_16x16x32_bf16 v[140:143], a[220:223], v[180:183], v[140:143]\n"
    "buffer_load_dwordx4 v224, s[44:47], s55 offen lds\n"
    "v_mfma_f32_16x16x32_bf16 v[144:147], a[224:227], v[180:183], v[144:147]\n"
    "v_exp_f32 v168, v168\n"
    "v_mfma_f32_16x16x32_bf16 v[144:147], a[160:163], v[176:179], v[144:147]\n"
    "ds_read_b64_tr_b16 a[214:215], v216 offset:62184\n"
    "v_cvt_pk_bf16_f32 v187, v166, v167\n"
    "v_mfma_f32_16x16x32_bf16 v[148:151], a[164:167], v[176:179], v[148:151]\n"
    "v_exp_f32 v169, v169\n"
    "v_mfma_f32_16x16x32_bf16 v[148:151], a[228:231], v[180:183], v[148:151]\n"
    "ds_read_b64_tr_b16 a[216:217], v217 offset:58048\n"
    "ds_read_b64_tr_b16 a[218:219], v217 offset:62176\n"
    "v_mfma_f32_16x16x32_bf16 v[152:155], a[232:235], v[180:183], v[152:155]\n"
    "v_exp_f32 v170, v170\n"
    "v_mfma_f32_16x16x32_bf16 v[152:155], a[168:171], v[176:179], v[152:155]\n"
    "ds_read_b64_tr_b16 a[220:221], v217 offset:58056\n"
    "ds_read_b64_tr_b16 a[222:223], v217 offset:62184\n"
    "v_mfma_f32_16x16x32_bf16 v[156:159], a[172:175], v[176:179], v[156:159]\n"
    "v_exp_f32 v171, v171\n"
    "v_mfma_f32_16x16x32_bf16 v[156:159], a[236:239], v[180:183], v[156:159]\n"
    "v_cvt_pk_bf16_f32 v188, v168, v169\n"
    "v_cvt_pk_bf16_f32 v189, v170, v171\n"
    "v_mfma_f32_16x16x32_bf16 v[160:163], a[80:83], a[16:19], -4.0\n"
    "v_exp_f32 v172, v172\n"
    "v_mfma_f32_16x16x32_bf16 v[160:163], a[84:87], a[20:23], v[160:163]\n"
    "ds_read_b64_tr_b16 a[224:225], v218 offset:58048\n"
    "ds_read_b64_tr_b16 a[226:227], v218 offset:62176\n"
    "v_mfma_f32_16x16x32_bf16 v[160:163], a[88:91], a[24:27], v[160:163]\n"
    "v_exp_f32 v173, v173\n"
    "v_mfma_f32_16x16x32_bf16 v[160:163], a[92:95], a[28:31], v[160:163]\n"
    "ds_read_b64_tr_b16 a[228:229], v218 offset:58056\n"
    "ds_read_b64_tr_b16 a[230:231], v218 offset:62184\n"
    "v_mfma_f32_16x16x32_bf16 v[164:167], a[108:111], a[28:31], -4.0\n"
    "v_exp_f32 v174, v174\n"
    "v_mfma_f32_16x16x32_bf16 v[164:167], a[104:107], a[24:27], v[164:167]\n"
    "ds_read_b64_tr_b16 a[232:233], v219 offset:58048\n"
    "v_cvt_pk_bf16_f32 v190, v172, v173\n"
    "v_mfma_f32_16x16x32_bf16 v[164:167], a[100:103], a[20:23], v[164:167]\n"
    "v_exp_f32 v175, v175\n"
    "v_mfma_f32_16x16x32_bf16 v[164:167], a[96:99], a[16:19], v[164:167]\n"
    "ds_read_b64_tr_b16 a[234:235], v219 offset:62176\n"
    "v_cvt_pk_bf16_f32 v191, v174, v175\n"
    "v_mfma_f32_16x16x32_bf16 v[168:171], a[112:115], a[16:19], -4.0\n"
    "s_add_u32 s52, s52, 16384\n"
    "ds_read_b64_tr_b16 a[236:237], v219 offset:58056\n"
    "ds_read_b64_tr_b16 a[238:239], v219 offset:62184\n"
    "v_mfma_f32_16x16x32_bf16 v[168:171], a[116:119], a[20:23], v[168:171]\n"
    "s_waitcnt vmcnt(8)\n"
    "v_exp_f32 v160, v160\n"
    "v_mfma_f32_16x16x32_bf16 v[168:171], a[120:123], a[24:27], v[168:171]\n"
    "s_add_u32 s54, s54, 16384\n"
    "ds_read_b128 v[252:255], v228 offset:22528\n"
    "v_cvt_pk_bf16_f32 v248, v238, v248\n"
    "v_mfma_f32_16x16x32_bf16 v[168:171], a[124:127], a[28:31], v[168:171]\n"
    "v_exp_f32 v161, v161\n"
    "v_mfma_f32_16x16x32_bf16 v[172:175], a[140:143], a[28:31], -4.0\n"
    "s_add_u32 s53, s62, s60\n"
    "v_cvt_pk_bf16_f32 v249, v239, v249\n"
    "v_cvt_pk_bf16_f32 v250, v240, v250\n"
    "v_mfma_f32_16x16x32_bf16 v[172:175], a[136:139], a[24:27], v[172:175]\n"
    "v_exp_f32 v162, v162\n"
    "v_mfma_f32_16x16x32_bf16 v[172:175], a[132:135], a[20:23], v[172:175]\n"
    "ds_read_b128 a[244:247], v227 offset:15360\n"
    "v_cvt_pk_bf16_f32 v251, v241, v251\n"
    "v_mfma_f32_16x16x32_bf16 v[172:175], a[128:131], a[16:19], v[172:175]\n"
    "v_exp_f32 v163, v163\n"
    "v_smfmac_f32_16x16x64_bf16 v[192:195], a[240:243], v[184:191], v237\n"
    "s_waitcnt lgkmcnt(2)\n"
    "v_cvt_pk_bf16_f32 v176, v160, v161\n"
    "v_mfma_f32_16x16x32_bf16 v[0:3], a[208:211], v[188:191], v[0:3]\n"
    "v_exp_f32 v164, v164\n"
    "v_mfma_f32_16x16x32_bf16 v[0:3], a[176:179], v[184:187], v[0:3]\n"
    "v_cvt_pk_bf16_f32 v177, v162, v163\n"
    "v_mfma_f32_16x16x32_bf16 v[4:7], a[180:183], v[184:187], v[4:7]\n"
    "v_exp_f32 v165, v165\n"
    "v_mfma_f32_16x16x32_bf16 v[4:7], a[212:215], v[188:191], v[4:7]\n"
    "s_waitcnt lgkmcnt(1)\n"
    "v_cvt_pk_bf16_f32 v178, v164, v165\n"
    "v_lshlrev_b32 v238, 16, v252\n"
    "v_mfma_f32_16x16x32_bf16 v[8:11], a[216:219], v[188:191], v[8:11]\n"
    "v_exp_f32 v166, v166\n"
    "v_mfma_f32_16x16x32_bf16 v[8:11], a[184:187], v[184:187], v[8:11]\n"
    "v_and_b32 v252, s80, v252\n"
    "v_lshlrev_b32 v239, 16, v253\n"
    "v_mfma_f32_16x16x32_bf16 v[12:15], a[188:191], v[184:187], v[12:15]\n"
    "s_waitcnt lgkmcnt(0)\n"
    "buffer_store_dwordx4 a[244:247], v228, s[48:51], s56 offen offset:3072 nt\n"
    "v_exp_f32 v167, v167\n"
    "v_mfma_f32_16x16x32_bf16 v[12:15], a[220:223], v[188:191], v[12:15]\n"
    "s_barrier\n"
    "v_mfma_f32_16x16x32_bf16 v[16:19], a[224:227], v[188:191], v[16:19]\n"
    "s_add_u32 m0, s57, 16640\n"
    "v_exp_f32 v168, v168\n"
    "v_mfma_f32_16x16x32_bf16 v[16:19], a[192:195], v[184:187], v[16:19]\n"
    "buffer_load_dwordx4 v229, s[36:39], s52 offen lds\n"
    "v_mfma_f32_16x16x32_bf16 v[20:23], a[196:199], v[184:187], v[20:23]\n"
    "v_exp_f32 v169, v169\n"
    "v_mfma_f32_16x16x32_bf16 v[20:23], a[228:231], v[188:191], v[20:23]\n"
    "v_cvt_pk_bf16_f32 v179, v166, v167\n"
    "v_cvt_pk_bf16_f32 v180, v168, v169\n"
    "v_mfma_f32_16x16x32_bf16 v[24:27], a[232:235], v[188:191], v[24:27]\n"
    "v_exp_f32 v170, v170\n"
    "v_mfma_f32_16x16x32_bf16 v[24:27], a[200:203], v[184:187], v[24:27]\n"
    "v_and_b32 v253, s80, v253\n"
    "v_lshlrev_b32 v240, 16, v254\n"
    "v_mfma_f32_16x16x32_bf16 v[28:31], a[204:207], v[184:187], v[28:31]\n"
    "v_exp_f32 v171, v171\n"
    "v_mfma_f32_16x16x32_bf16 v[28:31], a[236:239], v[188:191], v[28:31]\n"
    "v_cvt_pk_bf16_f32 v181, v170, v171\n"
    "v_and_b32 v254, s80, v254\n"
    "v_mfma_f32_16x16x32_bf16 v[160:163], a[80:83], a[32:35], -4.0\n"
    "v_exp_f32 v172, v172\n"
    "v_mfma_f32_16x16x32_bf16 v[160:163], a[84:87], a[36:39], v[160:163]\n"
    "ds_write_b128 v228, v[248:251] offset:13312\n"
    "v_lshlrev_b32 v241, 16, v255\n"
    "v_and_b32 v255, s80, v255\n"
    "v_pk_mul_f32 v[238:239], v[238:239], s[76:77] op_sel_hi:[1,0]\n"
    "v_pk_mul_f32 v[252:253], v[252:253], s[76:77] op_sel_hi:[1,0]\n"
    "v_pk_mul_f32 v[240:241], v[240:241], s[76:77] op_sel_hi:[1,0]\n"
    "v_pk_mul_f32 v[254:255], v[254:255], s[76:77] op_sel_hi:[1,0]\n"
    "v_mfma_f32_16x16x32_bf16 v[160:163], a[88:91], a[40:43], v[160:163]\n"
    "s_add_u32 m0, s57, 16640\n"
    "v_exp_f32 v173, v173\n"
    "v_mfma_f32_16x16x32_bf16 v[160:163], a[92:95], a[44:47], v[160:163]\n"
    "buffer_load_dwordx4 v229, s[36:39], s52 offen offset:1024 lds\n"
    "v_mfma_f32_16x16x32_bf16 v[164:167], a[108:111], a[44:47], -4.0\n"
    "v_exp_f32 v174, v174\n"
    "v_mfma_f32_16x16x32_bf16 v[164:167], a[104:107], a[40:43], v[164:167]\n"
    "v_cvt_pk_bf16_f32 v182, v172, v173\n"
    "v_cvt_pk_bf16_f32 v252, v238, v252\n"
    "v_mfma_f32_16x16x32_bf16 v[164:167], a[100:103], a[36:39], v[164:167]\n"
    "s_add_u32 m0, s63, 21504\n"
    "v_exp_f32 v175, v175\n"
    "v_mfma_f32_16x16x32_bf16 v[164:167], a[96:99], a[32:35], v[164:167]\n"
    "buffer_load_dwordx4 v225, s[44:47], s55 offen offset:1024 lds\n"
    "v_mfma_f32_16x16x32_bf16 v[168:171], a[112:115], a[32:35], -4.0\n"
    "v_cvt_pk_bf16_f32 v183, v174, v175\n"
    "v_cvt_pk_bf16_f32 v253, v239, v253\n"
    "v_mfma_f32_16x16x32_bf16 v[168:171], a[116:119], a[36:39], v[168:171]\n"
    "v_exp_f32 v160, v160\n"
    "v_mfma_f32_16x16x32_bf16 v[168:171], a[120:123], a[40:43], v[168:171]\n"
    "v_cvt_pk_bf16_f32 v254, v240, v254\n"
    "v_cvt_pk_bf16_f32 v255, v241, v255\n"
    "v_mfma_f32_16x16x32_bf16 v[168:171], a[124:127], a[44:47], v[168:171]\n"
    "s_add_u32 m0, s57, 16672\n"
    "v_exp_f32 v161, v161\n"
    "v_mfma_f32_16x16x32_bf16 v[172:175], a[140:143], a[44:47], -4.0\n"
    "buffer_load_dwordx4 v229, s[36:39], s52 offen offset:2048 lds\n"
    "v_mfma_f32_16x16x32_bf16 v[172:175], a[136:139], a[40:43], v[172:175]\n"
    "v_exp_f32 v162, v162\n"
    "v_mfma_f32_16x16x32_bf16 v[172:175], a[132:135], a[36:39], v[172:175]\n"
    "ds_read_b64_tr_b16 a[144:145], v216 offset:33280\n"
    "v_cvt_pk_bf16_f32 v184, v160, v161\n"
    "v_mfma_f32_16x16x32_bf16 v[172:175], a[128:131], a[32:35], v[172:175]\n"
    "v_exp_f32 v163, v163\n"
    "v_smfmac_f32_16x16x64_bf16 v[196:199], a[240:243], v[176:183], v237\n"
    "ds_read_b64_tr_b16 a[146:147], v216 offset:37408\n"
    "v_cvt_pk_bf16_f32 v185, v162, v163\n"
    "v_mfma_f32_16x16x32_bf16 v[32:35], a[208:211], v[180:183], v[32:35]\n"
    "v_exp_f32 v164, v164\n"
    "v_mfma_f32_16x16x32_bf16 v[32:35], a[176:179], v[176:179], v[32:35]\n"
    "ds_read_b64_tr_b16 a[148:149], v216 offset:33288\n"
    "ds_read_b64_tr_b16 a[150:151], v216 offset:37416\n"
    "v_mfma_f32_16x16x32_bf16 v[36:39], a[180:183], v[176:179], v[36:39]\n"
    "s_add_u32 m0, s57, 16672\n"
    "v_exp_f32 v165, v165\n"
    "v_mfma_f32_16x16x32_bf16 v[36:39], a[212:215], v[180:183], v[36:39]\n"
    "buffer_load_dwordx4 v229, s[36:39], s52 offen offset:3072 lds\n"
    "v_mfma_f32_16x16x32_bf16 v[40:43], a[216:219], v[180:183], v[40:43]\n"
    "v_exp_f32 v166, v166\n"
    "v_mfma_f32_16x16x32_bf16 v[40:43], a[184:187], v[176:179], v[40:43]\n"
    "ds_read_b64_tr_b16 a[152:153], v217 offset:33280\n"
    "v_cvt_pk_bf16_f32 v186, v164, v165\n"
    "v_mfma_f32_16x16x32_bf16 v[44:47], a[188:191], v[176:179], v[44:47]\n"
    "v_exp_f32 v167, v167\n"
    "v_mfma_f32_16x16x32_bf16 v[44:47], a[220:223], v[180:183], v[44:47]\n"
    "s_waitcnt vmcnt(10)\n"
    "ds_read_b128 v[244:247], v228 offset:20480\n"
    "v_cvt_pk_bf16_f32 v187, v166, v167\n"
    "v_mfma_f32_16x16x32_bf16 v[48:51], a[224:227], v[180:183], v[48:51]\n"
    "v_exp_f32 v168, v168\n"
    "v_mfma_f32_16x16x32_bf16 v[48:51], a[192:195], v[176:179], v[48:51]\n"
    "ds_read_b64_tr_b16 a[154:155], v217 offset:37408\n"
    "ds_read_b64_tr_b16 a[156:157], v217 offset:33288\n"
    "v_mfma_f32_16x16x32_bf16 v[52:55], a[196:199], v[176:179], v[52:55]\n"
    "v_exp_f32 v169, v169\n"
    "v_mfma_f32_16x16x32_bf16 v[52:55], a[228:231], v[180:183], v[52:55]\n"
    "ds_read_b64_tr_b16 a[158:159], v217 offset:37416\n"
    "v_cvt_pk_bf16_f32 v188, v168, v169\n"
    "v_mfma_f32_16x16x32_bf16 v[56:59], a[232:235], v[180:183], v[56:59]\n"
    "s_add_u32 m0, s58, 24768\n"
    "v_exp_f32 v170, v170\n"
    "v_mfma_f32_16x16x32_bf16 v[56:59], a[200:203], v[176:179], v[56:59]\n"
    "buffer_load_dwordx4 v229, s[40:43], s54 offen lds\n"
    "v_mfma_f32_16x16x32_bf16 v[60:63], a[204:207], v[176:179], v[60:63]\n"
    "v_exp_f32 v171, v171\n"
    "v_mfma_f32_16x16x32_bf16 v[60:63], a[236:239], v[180:183], v[60:63]\n"
    "ds_read_b128 a[248:251], v224 offset:16384\n"
    "v_cvt_pk_bf16_f32 v189, v170, v171\n"
    "v_mfma_f32_16x16x32_bf16 v[160:163], a[80:83], a[48:51], -4.0\n"
    "v_exp_f32 v172, v172\n"
    "v_mfma_f32_16x16x32_bf16 v[160:163], a[84:87], a[52:55], v[160:163]\n"
    "ds_read_b64_tr_b16 a[160:161], v218 offset:33280\n"
    "ds_read_b64_tr_b16 a[162:163], v218 offset:37408\n"
    "v_mfma_f32_16x16x32_bf16 v[160:163], a[88:91], a[56:59], v[160:163]\n"
    "v_exp_f32 v173, v173\n"
    "v_mfma_f32_16x16x32_bf16 v[160:163], a[92:95], a[60:63], v[160:163]\n"
    "ds_read_b64_tr_b16 a[164:165], v218 offset:33288\n"
    "v_cvt_pk_bf16_f32 v190, v172, v173\n"
    "v_mfma_f32_16x16x32_bf16 v[164:167], a[108:111], a[60:63], -4.0\n"
    "s_add_u32 m0, s58, 24776\n"
    "v_exp_f32 v174, v174\n"
    "v_mfma_f32_16x16x32_bf16 v[164:167], a[104:107], a[56:59], v[164:167]\n"
    "buffer_load_dwordx4 v229, s[40:43], s54 offen offset:1024 lds\n"
    "v_mfma_f32_16x16x32_bf16 v[164:167], a[100:103], a[52:55], v[164:167]\n"
    "v_exp_f32 v175, v175\n"
    "v_mfma_f32_16x16x32_bf16 v[164:167], a[96:99], a[48:51], v[164:167]\n"
    "s_waitcnt lgkmcnt(3)\n"
    "ds_read_b64_tr_b16 a[166:167], v218 offset:37416\n"
    "v_cvt_pk_bf16_f32 v191, v174, v175\n"
    "v_mfma_f32_16x16x32_bf16 v[168:171], a[112:115], a[48:51], -4.0\n"
    "s_add_u32 s56, s64, 12288\n"
    "ds_read_b64_tr_b16 a[168:169], v219 offset:33280\n"
    "v_lshlrev_b32 v238, 16, v244\n"
    "v_mfma_f32_16x16x32_bf16 v[168:171], a[116:119], a[52:55], v[168:171]\n"
    "buffer_store_dwordx4 a[248:251], v228, s[48:51], s56 offen nt\n"
    "v_exp_f32 v160, v160\n"
    "v_mfma_f32_16x16x32_bf16 v[168:171], a[120:123], a[56:59], v[168:171]\n"
    "v_and_b32 v244, s80, v244\n"
    "v_lshlrev_b32 v239, 16, v245\n"
    "v_mfma_f32_16x16x32_bf16 v[168:171], a[124:127], a[60:63], v[168:171]\n"
    "v_exp_f32 v161, v161\n"
    "v_mfma_f32_16x16x32_bf16 v[172:175], a[140:143], a[60:63], -4.0\n"
    "ds_read_b64_tr_b16 a[170:171], v219 offset:37408\n"
    "v_cvt_pk_bf16_f32 v176, v160, v161\n"
    "v_mfma_f32_16x16x32_bf16 v[172:175], a[136:139], a[56:59], v[172:175]\n"
    "v_exp_f32 v162, v162\n"
    "v_mfma_f32_16x16x32_bf16 v[172:175], a[132:135], a[52:55], v[172:175]\n"
    "ds_read_b64_tr_b16 a[172:173], v219 offset:33288\n"
    "v_and_b32 v245, s80, v245\n"
    "v_mfma_f32_16x16x32_bf16 v[172:175], a[128:131], a[48:51], v[172:175]\n"
    "v_exp_f32 v163, v163\n"
    "v_smfmac_f32_16x16x64_bf16 v[200:203], a[240:243], v[184:191], v237\n"
    "v_cvt_pk_bf16_f32 v177, v162, v163\n"
    "v_mfma_f32_16x16x32_bf16 v[64:67], a[208:211], v[188:191], v[64:67]\n"
    "v_exp_f32 v164, v164\n"
    "v_mfma_f32_16x16x32_bf16 v[64:67], a[176:179], v[184:187], v[64:67]\n"
    "ds_read_b64_tr_b16 a[174:175], v219 offset:37416\n"
    "v_lshlrev_b32 v240, 16, v246\n"
    "v_mfma_f32_16x16x32_bf16 v[68:71], a[180:183], v[184:187], v[68:71]\n"
    "v_exp_f32 v165, v165\n"
    "v_mfma_f32_16x16x32_bf16 v[68:71], a[212:215], v[188:191], v[68:71]\n"
    "v_and_b32 v246, s80, v246\n"
    "v_lshlrev_b32 v241, 16, v247\n"
    "v_mfma_f32_16x16x32_bf16 v[72:75], a[216:219], v[188:191], v[72:75]\n"
    "v_exp_f32 v166, v166\n"
    "v_mfma_f32_16x16x32_bf16 v[72:75], a[184:187], v[184:187], v[72:75]\n"
    "ds_write_b128 v228, v[252:255] offset:14336\n"
    "v_and_b32 v247, s80, v247\n"
    "v_cvt_pk_bf16_f32 v178, v164, v165\n"
    "v_pk_mul_f32 v[238:239], v[238:239], s[76:77] op_sel_hi:[1,0]\n"
    "v_pk_mul_f32 v[244:245], v[244:245], s[76:77] op_sel_hi:[1,0]\n"
    "v_pk_mul_f32 v[240:241], v[240:241], s[76:77] op_sel_hi:[1,0]\n"
    "v_pk_mul_f32 v[246:247], v[246:247], s[76:77] op_sel_hi:[1,0]\n"
    "v_mfma_f32_16x16x32_bf16 v[76:79], a[188:191], v[184:187], v[76:79]\n"
    "s_add_u32 m0, s63, 18432\n"
    "v_exp_f32 v167, v167\n"
    "v_mfma_f32_16x16x32_bf16 v[76:79], a[220:223], v[188:191], v[76:79]\n"
    "buffer_load_dwordx4 v226, s[44:47], s55 offen offset:2048 lds\n"
    "v_mfma_f32_16x16x32_bf16 v[80:83], a[224:227], v[188:191], v[80:83]\n"
    "v_exp_f32 v168, v168\n"
    "v_mfma_f32_16x16x32_bf16 v[80:83], a[192:195], v[184:187], v[80:83]\n"
    "v_cvt_pk_bf16_f32 v179, v166, v167\n"
    "v_cvt_pk_bf16_f32 v244, v238, v244\n"
    "v_mfma_f32_16x16x32_bf16 v[84:87], a[196:199], v[184:187], v[84:87]\n"
    "v_exp_f32 v169, v169\n"
    "v_mfma_f32_16x16x32_bf16 v[84:87], a[228:231], v[188:191], v[84:87]\n"
    "v_cvt_pk_bf16_f32 v180, v168, v169\n"
    "v_cvt_pk_bf16_f32 v245, v239, v245\n"
    "v_mfma_f32_16x16x32_bf16 v[88:91], a[232:235], v[188:191], v[88:91]\n"
    "v_exp_f32 v170, v170\n"
    "v_mfma_f32_16x16x32_bf16 v[88:91], a[200:203], v[184:187], v[88:91]\n"
    "v_cvt_pk_bf16_f32 v246, v240, v246\n"
    "v_cvt_pk_bf16_f32 v247, v241, v247\n"
    "v_mfma_f32_16x16x32_bf16 v[92:95], a[204:207], v[184:187], v[92:95]\n"
    "v_exp_f32 v171, v171\n"
    "v_mfma_f32_16x16x32_bf16 v[92:95], a[236:239], v[188:191], v[92:95]\n"
    "v_cvt_pk_bf16_f32 v181, v170, v171\n"
    "v_mfma_f32_16x16x32_bf16 v[160:163], a[80:83], a[64:67], -4.0\n"
    "v_exp_f32 v172, v172\n"
    "v_mfma_f32_16x16x32_bf16 v[160:163], a[84:87], a[68:71], v[160:163]\n"
    "v_mfma_f32_16x16x32_bf16 v[160:163], a[88:91], a[72:75], v[160:163]\n"
    "v_exp_f32 v173, v173\n"
    "v_mfma_f32_16x16x32_bf16 v[160:163], a[92:95], a[76:79], v[160:163]\n"
    "v_cvt_pk_bf16_f32 v182, v172, v173\n"
    "v_mfma_f32_16x16x32_bf16 v[164:167], a[108:111], a[76:79], -4.0\n"
    "v_exp_f32 v174, v174\n"
    "v_mfma_f32_16x16x32_bf16 v[164:167], a[104:107], a[72:75], v[164:167]\n"
    "v_mfma_f32_16x16x32_bf16 v[164:167], a[100:103], a[68:71], v[164:167]\n"
    "v_exp_f32 v175, v175\n"
    "v_mfma_f32_16x16x32_bf16 v[164:167], a[96:99], a[64:67], v[164:167]\n"
    "s_waitcnt vmcnt(10) lgkmcnt(0)\n"
    "ds_read_b128 v[248:251], v228 offset:21504\n"
    "v_cvt_pk_bf16_f32 v183, v174, v175\n"
    "v_mfma_f32_16x16x32_bf16 v[168:171], a[112:115], a[64:67], -4.0\n"
    "s_barrier\n"
    "v_mfma_f32_16x16x32_bf16 v[168:171], a[116:119], a[68:71], v[168:171]\n"
    "v_exp_f32 v160, v160\n"
    "v_mfma_f32_16x16x32_bf16 v[168:171], a[120:123], a[72:75], v[168:171]\n"
    "ds_read_b128 a[80:83], v212 offset:0\n"
    "ds_read_b128 a[84:87], v213 offset:0\n"
    "v_mfma_f32_16x16x32_bf16 v[168:171], a[124:127], a[76:79], v[168:171]\n"
    "v_exp_f32 v161, v161\n"
    "v_mfma_f32_16x16x32_bf16 v[172:175], a[140:143], a[76:79], -4.0\n"
    "ds_read_b128 a[88:91], v214 offset:0\n"
    "ds_read_b128 a[92:95], v215 offset:0\n"
    "v_mfma_f32_16x16x32_bf16 v[172:175], a[136:139], a[72:75], v[172:175]\n"
    "v_exp_f32 v162, v162\n"
    "v_mfma_f32_16x16x32_bf16 v[172:175], a[132:135], a[68:71], v[172:175]\n"
    "ds_read_b128 a[108:111], v215 offset:4160\n"
    "ds_read_b128 a[104:107], v214 offset:4160\n"
    "v_mfma_f32_16x16x32_bf16 v[172:175], a[128:131], a[64:67], v[172:175]\n"
    "v_exp_f32 v163, v163\n"
    "v_smfmac_f32_16x16x64_bf16 v[204:207], a[240:243], v[176:183], v237\n"
    "ds_read_b128 a[100:103], v213 offset:4160\n"
    "ds_read_b128 a[96:99], v212 offset:4160\n"
    "v_mfma_f32_16x16x32_bf16 v[96:99], a[208:211], v[180:183], v[96:99]\n"
    "v_exp_f32 v164, v164\n"
    "v_mfma_f32_16x16x32_bf16 v[96:99], a[176:179], v[176:179], v[96:99]\n"
    "v_cvt_pk_bf16_f32 v184, v160, v161\n"
    "v_cvt_pk_bf16_f32 v185, v162, v163\n"
    "v_mfma_f32_16x16x32_bf16 v[100:103], a[180:183], v[176:179], v[100:103]\n"
    "v_exp_f32 v165, v165\n"
    "v_mfma_f32_16x16x32_bf16 v[100:103], a[212:215], v[180:183], v[100:103]\n"
    "ds_read_b128 a[112:115], v212 offset:8320\n"
    "ds_read_b128 a[116:119], v213 offset:8320\n"
    "v_mfma_f32_16x16x32_bf16 v[104:107], a[216:219], v[180:183], v[104:107]\n"
    "s_add_u32 m0, s58, 0\n"
    "v_exp_f32 v166, v166\n"
    "v_mfma_f32_16x16x32_bf16 v[104:107], a[184:187], v[176:179], v[104:107]\n"
    "buffer_load_dwordx4 v229, s[40:43], s53 offen lds\n"
    "v_mfma_f32_16x16x32_bf16 v[108:111], a[188:191], v[176:179], v[108:111]\n"
    "s_add_u32 m0, s58, 8\n"
    "v_exp_f32 v167, v167\n"
    "v_mfma_f32_16x16x32_bf16 v[108:111], a[220:223], v[180:183], v[108:111]\n"
    "buffer_load_dwordx4 v229, s[40:43], s53 offen offset:1024 lds\n"
    "v_mfma_f32_16x16x32_bf16 v[112:115], a[224:227], v[180:183], v[112:115]\n"
    "v_exp_f32 v168, v168\n"
    "v_mfma_f32_16x16x32_bf16 v[112:115], a[192:195], v[176:179], v[112:115]\n"
    "ds_read_b128 a[120:123], v214 offset:8320\n"
    "ds_read_b128 a[124:127], v215 offset:8320\n"
    "v_mfma_f32_16x16x32_bf16 v[116:119], a[196:199], v[176:179], v[116:119]\n"
    "v_exp_f32 v169, v169\n"
    "v_mfma_f32_16x16x32_bf16 v[116:119], a[228:231], v[180:183], v[116:119]\n"
    "v_cvt_pk_bf16_f32 v186, v164, v165\n"
    "v_cvt_pk_bf16_f32 v187, v166, v167\n"
    "v_mfma_f32_16x16x32_bf16 v[120:123], a[232:235], v[180:183], v[120:123]\n"
    "v_exp_f32 v170, v170\n"
    "v_mfma_f32_16x16x32_bf16 v[120:123], a[200:203], v[176:179], v[120:123]\n"
    "ds_read_b128 a[140:143], v215 offset:12480\n"
    "ds_read_b128 a[136:139], v214 offset:12480\n"
    "v_mfma_f32_16x16x32_bf16 v[124:127], a[204:207], v[176:179], v[124:127]\n"
    "v_exp_f32 v171, v171\n"
    "v_mfma_f32_16x16x32_bf16 v[124:127], a[236:239], v[180:183], v[124:127]\n"
    "s_waitcnt lgkmcnt(4)\n"
    "ds_read_b128 a[132:135], v213 offset:12480\n"
    "ds_read_b128 a[128:131], v212 offset:12480\n"
    "v_mfma_f32_16x16x32_bf16 v[160:163], a[80:83], a[0:3], -4.0\n"
    "v_exp_f32 v172, v172\n"
    "v_mfma_f32_16x16x32_bf16 v[160:163], a[84:87], a[4:7], v[160:163]\n"
    "ds_read_b128 a[252:255], v225 offset:17408\n"
    "v_cvt_pk_bf16_f32 v188, v168, v169\n"
    "v_mfma_f32_16x16x32_bf16 v[160:163], a[88:91], a[8:11], v[160:163]\n"
    "v_exp_f32 v173, v173\n"
    "v_mfma_f32_16x16x32_bf16 v[160:163], a[92:95], a[12:15], v[160:163]\n"
    "v_cvt_pk_bf16_f32 v189, v170, v171\n"
    "v_cvt_pk_bf16_f32 v190, v172, v173\n"
    "v_mfma_f32_16x16x32_bf16 v[164:167], a[108:111], a[12:15], -4.0\n"
    "v_exp_f32 v174, v174\n"
    "v_mfma_f32_16x16x32_bf16 v[164:167], a[104:107], a[8:11], v[164:167]\n"
    "v_lshlrev_b32 v238, 16, v248\n"
    "v_and_b32 v248, s80, v248\n"
    "v_mfma_f32_16x16x32_bf16 v[164:167], a[100:103], a[4:7], v[164:167]\n"
    "s_add_u32 m0, s63, 18432\n"
    "v_exp_f32 v175, v175\n"
    "v_mfma_f32_16x16x32_bf16 v[164:167], a[96:99], a[0:3], v[164:167]\n"
    "buffer_load_dwordx4 v227, s[44:47], s55 offen offset:3072 lds\n"
    "v_mfma_f32_16x16x32_bf16 v[168:171], a[112:115], a[0:3], -4.0\n"
    "s_add_u32 s55, s55, 4096\n"
    "v_cvt_pk_bf16_f32 v191, v174, v175\n"
    "v_lshlrev_b32 v239, 16, v249\n"
    "v_mfma_f32_16x16x32_bf16 v[168:171], a[116:119], a[4:7], v[168:171]\n"
    "s_waitcnt lgkmcnt(0)\n"
    "v_exp_f32 v160, v160\n"
    "v_mfma_f32_16x16x32_bf16 v[168:171], a[120:123], a[8:11], v[168:171]\n"
    "v_and_b32 v249, s80, v249\n"
    "v_lshlrev_b32 v240, 16, v250\n"
    "v_mfma_f32_16x16x32_bf16 v[168:171], a[124:127], a[12:15], v[168:171]\n"
    "buffer_store_dwordx4 a[252:255], v228, s[48:51], s56 offen offset:1024 nt\n"
    "v_exp_f32 v161, v161\n"
    "v_mfma_f32_16x16x32_bf16 v[172:175], a[140:143], a[12:15], -4.0\n"
    "v_and_b32 v250, s80, v250\n"
    "v_lshlrev_b32 v241, 16, v251\n"
    "v_mfma_f32_16x16x32_bf16 v[172:175], a[136:139], a[8:11], v[172:175]\n"
    "v_exp_f32 v162, v162\n"
    "v_mfma_f32_16x16x32_bf16 v[172:175], a[132:135], a[4:7], v[172:175]\n"
    "ds_write_b128 v228, v[244:247] offset:15360\n"
    "v_and_b32 v251, s80, v251\n"
    "v_cvt_pk_bf16_f32 v176, v160, v161\n"
    "v_pk_mul_f32 v[238:239], v[238:239], s[76:77] op_sel_hi:[1,0]\n"
    "v_pk_mul_f32 v[248:249], v[248:249], s[76:77] op_sel_hi:[1,0]\n"
    "v_pk_mul_f32 v[240:241], v[240:241], s[76:77] op_sel_hi:[1,0]\n"
    "v_pk_mul_f32 v[250:251], v[250:251], s[76:77] op_sel_hi:[1,0]\n"
    "v_mfma_f32_16x16x32_bf16 v[172:175], a[128:131], a[0:3], v[172:175]\n"
    "v_exp_f32 v163, v163\n"
    "v_smfmac_f32_16x16x64_bf16 v[208:211], a[240:243], v[184:191], v237\n"
    "v_cvt_pk_bf16_f32 v248, v238, v248\n"
    "v_mfma_f32_16x16x32_bf16 v[128:131], a[208:211], v[188:191], v[128:131]\n"
    "v_exp_f32 v164, v164\n"
    "v_mfma_f32_16x16x32_bf16 v[128:131], a[176:179], v[184:187], v[128:131]\n"
    "v_cvt_pk_bf16_f32 v249, v239, v249\n"
    "v_cvt_pk_bf16_f32 v250, v240, v250\n"
    "v_mfma_f32_16x16x32_bf16 v[132:135], a[180:183], v[184:187], v[132:135]\n"
    "v_exp_f32 v165, v165\n"
    "v_mfma_f32_16x16x32_bf16 v[132:135], a[212:215], v[188:191], v[132:135]\n"
    "ds_read_b64_tr_b16 a[208:209], v216 offset:49792\n"
    "v_cvt_pk_bf16_f32 v177, v162, v163\n"
    "v_mfma_f32_16x16x32_bf16 v[136:139], a[216:219], v[188:191], v[136:139]\n"
    "v_exp_f32 v166, v166\n"
    "v_mfma_f32_16x16x32_bf16 v[136:139], a[184:187], v[184:187], v[136:139]\n"
    "ds_read_b64_tr_b16 a[210:211], v216 offset:53920\n"
    "ds_read_b64_tr_b16 a[212:213], v216 offset:49800\n"
    "v_mfma_f32_16x16x32_bf16 v[140:143], a[188:191], v[184:187], v[140:143]\n"
    "v_exp_f32 v167, v167\n"
    "v_mfma_f32_16x16x32_bf16 v[140:143], a[220:223], v[188:191], v[140:143]\n"
    "s_waitcnt vmcnt(10)\n"
    "ds_read_b128 v[252:255], v228 offset:22528\n"
    "ds_read_b64_tr_b16 a[214:215], v216 offset:53928\n"
    "v_mfma_f32_16x16x32_bf16 v[144:147], a[224:227], v[188:191], v[144:147]\n"
    "v_exp_f32 v168, v168\n"
    "v_mfma_f32_16x16x32_bf16 v[144:147], a[192:195], v[184:187], v[144:147]\n"
    "v_cvt_pk_bf16_f32 v178, v164, v165\n"
    "v_cvt_pk_bf16_f32 v179, v166, v167\n"
    "v_mfma_f32_16x16x32_bf16 v[148:151], a[196:199], v[184:187], v[148:151]\n"
    "v_exp_f32 v169, v169\n"
    "v_mfma_f32_16x16x32_bf16 v[148:151], a[228:231], v[188:191], v[148:151]\n"
    "ds_read_b64_tr_b16 a[216:217], v217 offset:49792\n"
    "ds_read_b64_tr_b16 a[218:219], v217 offset:53920\n"
    "v_mfma_f32_16x16x32_bf16 v[152:155], a[232:235], v[188:191], v[152:155]\n"
    "v_exp_f32 v170, v170\n"
    "v_mfma_f32_16x16x32_bf16 v[152:155], a[200:203], v[184:187], v[152:155]\n"
    "ds_read_b64_tr_b16 a[220:221], v217 offset:49800\n"
    "ds_read_b64_tr_b16 a[222:223], v217 offset:53928\n"
    "v_mfma_f32_16x16x32_bf16 v[156:159], a[204:207], v[184:187], v[156:159]\n"
    "v_exp_f32 v171, v171\n"
    "v_mfma_f32_16x16x32_bf16 v[156:159], a[236:239], v[188:191], v[156:159]\n"
    "v_cvt_pk_bf16_f32 v180, v168, v169\n"
    "v_cvt_pk_bf16_f32 v181, v170, v171\n"
    "v_mfma_f32_16x16x32_bf16 v[160:163], a[80:83], a[16:19], -4.0\n"
    "v_exp_f32 v172, v172\n"
    "v_mfma_f32_16x16x32_bf16 v[160:163], a[84:87], a[20:23], v[160:163]\n"
    "ds_read_b64_tr_b16 a[224:225], v218 offset:49792\n"
    "ds_read_b64_tr_b16 a[226:227], v218 offset:53920\n"
    "v_mfma_f32_16x16x32_bf16 v[160:163], a[88:91], a[24:27], v[160:163]\n"
    "v_exp_f32 v173, v173\n"
    "v_mfma_f32_16x16x32_bf16 v[160:163], a[92:95], a[28:31], v[160:163]\n"
    "ds_read_b64_tr_b16 a[228:229], v218 offset:49800\n"
    "ds_read_b64_tr_b16 a[230:231], v218 offset:53928\n"
    "v_mfma_f32_16x16x32_bf16 v[164:167], a[108:111], a[28:31], -4.0\n"
    "v_exp_f32 v174, v174\n"
    "v_mfma_f32_16x16x32_bf16 v[164:167], a[104:107], a[24:27], v[164:167]\n"
    "ds_read_b64_tr_b16 a[232:233], v219 offset:49792\n"
    "v_cvt_pk_bf16_f32 v182, v172, v173\n"
    "v_mfma_f32_16x16x32_bf16 v[164:167], a[100:103], a[20:23], v[164:167]\n"
    "v_exp_f32 v175, v175\n"
    "v_mfma_f32_16x16x32_bf16 v[164:167], a[96:99], a[16:19], v[164:167]\n"
    "ds_read_b64_tr_b16 a[234:235], v219 offset:53920\n"
    "v_cvt_pk_bf16_f32 v183, v174, v175\n"
    "v_mfma_f32_16x16x32_bf16 v[168:171], a[112:115], a[16:19], -4.0\n"
    "s_add_u32 s52, s62, s59\n"
    "ds_read_b64_tr_b16 a[236:237], v219 offset:49800\n"
    "ds_read_b64_tr_b16 a[238:239], v219 offset:53928\n"
    "v_mfma_f32_16x16x32_bf16 v[168:171], a[116:119], a[20:23], v[168:171]\n"
    "v_exp_f32 v160, v160\n"
    "v_mfma_f32_16x16x32_bf16 v[168:171], a[120:123], a[24:27], v[168:171]\n"
    "s_add_u32 s54, s62, s61\n"
    "ds_read_b128 a[244:247], v226 offset:18432\n"
    "v_cvt_pk_bf16_f32 v251, v241, v251\n"
    "v_mfma_f32_16x16x32_bf16 v[168:171], a[124:127], a[28:31], v[168:171]\n"
    "s_waitcnt lgkmcnt(5)\n"
    "v_exp_f32 v161, v161\n"
    "v_mfma_f32_16x16x32_bf16 v[172:175], a[140:143], a[28:31], -4.0\n"
    "s_add_u32 s53, s53, 16384\n"
    "v_cvt_pk_bf16_f32 v184, v160, v161\n"
    "v_lshlrev_b32 v238, 16, v252\n"
    "v_mfma_f32_16x16x32_bf16 v[172:175], a[136:139], a[24:27], v[172:175]\n"
    "v_exp_f32 v162, v162\n"
    "v_mfma_f32_16x16x32_bf16 v[172:175], a[132:135], a[20:23], v[172:175]\n"
    "v_and_b32 v252, s80, v252\n"
    "v_lshlrev_b32 v239, 16, v253\n"
    "v_mfma_f32_16x16x32_bf16 v[172:175], a[128:131], a[16:19], v[172:175]\n"
    "v_exp_f32 v163, v163\n"
    "v_smfmac_f32_16x16x64_bf16 v[192:195], a[240:243], v[176:183], v237\n"
    "v_cvt_pk_bf16_f32 v185, v162, v163\n"
    "v_mfma_f32_16x16x32_bf16 v[0:3], a[208:211], v[180:183], v[0:3]\n"
    "v_exp_f32 v164, v164\n"
    "v_mfma_f32_16x16x32_bf16 v[0:3], a[144:147], v[176:179], v[0:3]\n"
    "v_and_b32 v253, s80, v253\n"
    "v_lshlrev_b32 v240, 16, v254\n"
    "v_mfma_f32_16x16x32_bf16 v[4:7], a[148:151], v[176:179], v[4:7]\n"
    "s_waitcnt lgkmcnt(0)\n"
    "buffer_store_dwordx4 a[244:247], v228, s[48:51], s56 offen offset:2048 nt\n"
    "v_exp_f32 v165, v165\n"
    "v_mfma_f32_16x16x32_bf16 v[4:7], a[212:215], v[180:183], v[4:7]\n"
    "v_and_b32 v254, s80, v254\n"
    "v_lshlrev_b32 v241, 16, v255\n"
    "v_mfma_f32_16x16x32_bf16 v[8:11], a[216:219], v[180:183], v[8:11]\n"
    "v_exp_f32 v166, v166\n"
    "v_mfma_f32_16x16x32_bf16 v[8:11], a[152:155], v[176:179], v[8:11]\n"
    "ds_write_b128 v228, v[248:251] offset:16384\n"
    "v_and_b32 v255, s80, v255\n"
    "v_cvt_pk_bf16_f32 v186, v164, v165\n"
    "v_pk_mul_f32 v[238:239], v[238:239], s[76:77] op_sel_hi:[1,0]\n"
    "v_pk_mul_f32 v[252:253], v[252:253], s[76:77] op_sel_hi:[1,0]\n"
    "v_pk_mul_f32 v[240:241], v[240:241], s[76:77] op_sel_hi:[1,0]\n"
    "v_pk_mul_f32 v[254:255], v[254:255], s[76:77] op_sel_hi:[1,0]\n"
    "v_mfma_f32_16x16x32_bf16 v[12:15], a[156:159], v[176:179], v[12:15]\n"
    "v_exp_f32 v167, v167\n"
    "v_mfma_f32_16x16x32_bf16 v[12:15], a[220:223], v[180:183], v[12:15]\n"
    "s_barrier\n"
    "v_mfma_f32_16x16x32_bf16 v[16:19], a[224:227], v[180:183], v[16:19]\n"
    "s_add_u32 m0, s57, 0\n"
    "v_exp_f32 v168, v168\n"
    "v_mfma_f32_16x16x32_bf16 v[16:19], a[160:163], v[176:179], v[16:19]\n"
    "buffer_load_dwordx4 v229, s[36:39], s52 offen lds\n"
    "v_mfma_f32_16x16x32_bf16 v[20:23], a[164:167], v[176:179], v[20:23]\n"
    "v_exp_f32 v169, v169\n"
    "v_mfma_f32_16x16x32_bf16 v[20:23], a[228:231], v[180:183], v[20:23]\n"
    "v_cvt_pk_bf16_f32 v187, v166, v167\n"
    "v_cvt_pk_bf16_f32 v188, v168, v169\n"
    "v_mfma_f32_16x16x32_bf16 v[24:27], a[232:235], v[180:183], v[24:27]\n"
    "v_exp_f32 v170, v170\n"
    "v_mfma_f32_16x16x32_bf16 v[24:27], a[168:171], v[176:179], v[24:27]\n"
    "v_cvt_pk_bf16_f32 v252, v238, v252\n"
    "v_cvt_pk_bf16_f32 v253, v239, v253\n"
    "v_mfma_f32_16x16x32_bf16 v[28:31], a[172:175], v[176:179], v[28:31]\n"
    "v_exp_f32 v171, v171\n"
    "v_mfma_f32_16x16x32_bf16 v[28:31], a[236:239], v[180:183], v[28:31]\n"
    "v_cvt_pk_bf16_f32 v189, v170, v171\n"
    "v_cvt_pk_bf16_f32 v254, v240, v254\n"
    "v_mfma_f32_16x16x32_bf16 v[160:163], a[80:83], a[32:35], -4.0\n"
    "v_exp_f32 v172, v172\n"
    "v_mfma_f32_16x16x32_bf16 v[160:163], a[84:87], a[36:39], v[160:163]\n"
    "v_cvt_pk_bf16_f32 v255, v241, v255\n"
    "v_mfma_f32_16x16x32_bf16 v[160:163], a[88:91], a[40:43], v[160:163]\n"
    "s_add_u32 m0, s57, 0\n"
    "v_exp_f32 v173, v173\n"
    "v_mfma_f32_16x16x32_bf16 v[160:163], a[92:95], a[44:47], v[160:163]\n"
    "buffer_load_dwordx4 v229, s[36:39], s52 offen offset:1024 lds\n"
    "v_mfma_f32_16x16x32_bf16 v[164:167], a[108:111], a[44:47], -4.0\n"
    "v_exp_f32 v174, v174\n"
    "v_mfma_f32_16x16x32_bf16 v[164:167], a[104:107], a[40:43], v[164:167]\n"
    "ds_read_b64_tr_b16 a[176:177], v216 offset:41536\n"
    "v_cvt_pk_bf16_f32 v190, v172, v173\n"
    "v_mfma_f32_16x16x32_bf16 v[164:167], a[100:103], a[36:39], v[164:167]\n"
    "v_exp_f32 v175, v175\n"
    "v_mfma_f32_16x16x32_bf16 v[164:167], a[96:99], a[32:35], v[164:167]\n"
    "s_waitcnt vmcnt(7)\n"
    "ds_read_b128 v[244:247], v228 offset:20480\n"
    "v_cvt_pk_bf16_f32 v191, v174, v175\n"
    "v_mfma_f32_16x16x32_bf16 v[168:171], a[112:115], a[32:35], -4.0\n"
    "ds_read_b64_tr_b16 a[178:179], v216 offset:45664\n"
    "v_mfma_f32_16x16x32_bf16 v[168:171], a[116:119], a[36:39], v[168:171]\n"
    "v_exp_f32 v160, v160\n"
    "v_mfma_f32_16x16x32_bf16 v[168:171], a[120:123], a[40:43], v[168:171]\n"
    "v_mfma_f32_16x16x32_bf16 v[168:171], a[124:127], a[44:47], v[168:171]\n"
    "s_add_u32 m0, s57, 32\n"
    "v_exp_f32 v161, v161\n"
    "v_mfma_f32_16x16x32_bf16 v[172:175], a[140:143], a[44:47], -4.0\n"
    "buffer_load_dwordx4 v229, s[36:39], s52 offen offset:2048 lds\n"
    "v_mfma_f32_16x16x32_bf16 v[172:175], a[136:139], a[40:43], v[172:175]\n"
    "v_exp_f32 v162, v162\n"
    "v_mfma_f32_16x16x32_bf16 v[172:175], a[132:135], a[36:39], v[172:175]\n"
    "ds_read_b64_tr_b16 a[180:181], v216 offset:41544\n"
    "v_cvt_pk_bf16_f32 v176, v160, v161\n"
    "v_mfma_f32_16x16x32_bf16 v[172:175], a[128:131], a[32:35], v[172:175]\n"
    "v_exp_f32 v163, v163\n"
    "v_smfmac_f32_16x16x64_bf16 v[196:199], a[240:243], v[184:191], v237\n"
    "ds_read_b64_tr_b16 a[182:183], v216 offset:45672\n"
    "v_cvt_pk_bf16_f32 v177, v162, v163\n"
    "v_mfma_f32_16x16x32_bf16 v[32:35], a[208:211], v[188:191], v[32:35]\n"
    "v_exp_f32 v164, v164\n"
    "v_mfma_f32_16x16x32_bf16 v[32:35], a[144:147], v[184:187], v[32:35]\n"
    "s_waitcnt lgkmcnt(2)\n"
    "ds_read_b64_tr_b16 a[184:185], v217 offset:41536\n"
    "v_lshlrev_b32 v238, 16, v244\n"
    "v_mfma_f32_16x16x32_bf16 v[36:39], a[148:151], v[184:187], v[36:39]\n"
    "s_add_u32 m0, s57, 32\n"
    "v_exp_f32 v165, v165\n"
    "v_mfma_f32_16x16x32_bf16 v[36:39], a[212:215], v[188:191], v[36:39]\n"
    "buffer_load_dwordx4 v229, s[36:39], s52 offen offset:3072 lds\n"
    "v_mfma_f32_16x16x32_bf16 v[40:43], a[216:219], v[188:191], v[40:43]\n"
    "v_exp_f32 v166, v166\n"
    "v_mfma_f32_16x16x32_bf16 v[40:43], a[152:155], v[184:187], v[40:43]\n"
    "ds_read_b64_tr_b16 a[186:187], v217 offset:45664\n"
    "v_cvt_pk_bf16_f32 v178, v164, v165\n"
    "v_mfma_f32_16x16x32_bf16 v[44:47], a[156:159], v[184:187], v[44:47]\n"
    "v_exp_f32 v167, v167\n"
    "v_mfma_f32_16x16x32_bf16 v[44:47], a[220:223], v[188:191], v[44:47]\n"
    "ds_read_b64_tr_b16 a[188:189], v217 offset:41544\n"
    "v_cvt_pk_bf16_f32 v179, v166, v167\n"
    "v_mfma_f32_16x16x32_bf16 v[48:51], a[224:227], v[188:191], v[48:51]\n"
    "v_exp_f32 v168, v168\n"
    "v_mfma_f32_16x16x32_bf16 v[48:51], a[160:163], v[184:187], v[48:51]\n"
    "ds_read_b64_tr_b16 a[190:191], v217 offset:45672\n"
    "v_and_b32 v244, s80, v244\n"
    "v_mfma_f32_16x16x32_bf16 v[52:55], a[164:167], v[184:187], v[52:55]\n"
    "v_exp_f32 v169, v169\n"
    "v_mfma_f32_16x16x32_bf16 v[52:55], a[228:231], v[188:191], v[52:55]\n"
    "v_cvt_pk_bf16_f32 v180, v168, v169\n"
    "v_lshlrev_b32 v239, 16, v245\n"
    "v_mfma_f32_16x16x32_bf16 v[56:59], a[232:235], v[188:191], v[56:59]\n"
    "s_add_u32 m0, s58, 16512\n"
    "v_exp_f32 v170, v170\n"
    "v_mfma_f32_16x16x32_bf16 v[56:59], a[168:171], v[184:187], v[56:59]\n"
    "buffer_load_dwordx4 v229, s[40:43], s54 offen lds\n"
    "v_mfma_f32_16x16x32_bf16 v[60:63], a[172:175], v[184:187], v[60:63]\n"
    "v_exp_f32 v171, v171\n"
    "v_mfma_f32_16x16x32_bf16 v[60:63], a[236:239], v[188:191], v[60:63]\n"
    "v_cvt_pk_bf16_f32 v181, v170, v171\n"
    "v_and_b32 v245, s80, v245\n"
    "v_mfma_f32_16x16x32_bf16 v[160:163], a[80:83], a[48:51], -4.0\n"
    "v_exp_f32 v172, v172\n"
    "v_mfma_f32_16x16x32_bf16 v[160:163], a[84:87], a[52:55], v[160:163]\n"
    "v_lshlrev_b32 v240, 16, v246\n"
    "v_and_b32 v246, s80, v246\n"
    "v_mfma_f32_16x16x32_bf16 v[160:163], a[88:91], a[56:59], v[160:163]\n"
    "v_exp_f32 v173, v173\n"
    "v_mfma_f32_16x16x32_bf16 v[160:163], a[92:95], a[60:63], v[160:163]\n"
    "v_cvt_pk_bf16_f32 v182, v172, v173\n"
    "v_lshlrev_b32 v241, 16, v247\n"
    "v_mfma_f32_16x16x32_bf16 v[164:167], a[108:111], a[60:63], -4.0\n"
    "s_add_u32 m0, s58, 16520\n"
    "v_exp_f32 v174, v174\n"
    "v_mfma_f32_16x16x32_bf16 v[164:167], a[104:107], a[56:59], v[164:167]\n"
    "buffer_load_dwordx4 v229, s[40:43], s54 offen offset:1024 lds\n"
    "v_mfma_f32_16x16x32_bf16 v[164:167], a[100:103], a[52:55], v[164:167]\n"
    "v_exp_f32 v175, v175\n"
    "v_mfma_f32_16x16x32_bf16 v[164:167], a[96:99], a[48:51], v[164:167]\n"
    "ds_read_b64_tr_b16 a[192:193], v218 offset:41536\n"
    "v_cvt_pk_bf16_f32 v183, v174, v175\n"
    "v_mfma_f32_16x16x32_bf16 v[168:171], a[112:115], a[48:51], -4.0\n"
    "ds_read_b64_tr_b16 a[194:195], v218 offset:45664\n"
    "v_and_b32 v247, s80, v247\n"
    "v_mfma_f32_16x16x32_bf16 v[168:171], a[116:119], a[52:55], v[168:171]\n"
    "v_exp_f32 v160, v160\n"
    "v_mfma_f32_16x16x32_bf16 v[168:171], a[120:123], a[56:59], v[168:171]\n"
    "ds_read_b64_tr_b16 a[196:197], v218 offset:41544\n"
    "ds_read_b128 a[248:251], v227 offset:19456\n"
    "v_mfma_f32_16x16x32_bf16 v[168:171], a[124:127], a[60:63], v[168:171]\n"
    "v_exp_f32 v161, v161\n"
    "v_mfma_f32_16x16x32_bf16 v[172:175], a[140:143], a[60:63], -4.0\n"
    "ds_read_b64_tr_b16 a[198:199], v218 offset:45672\n"
    "ds_read_b64_tr_b16 a[200:201], v219 offset:41536\n"
    "v_mfma_f32_16x16x32_bf16 v[172:175], a[136:139], a[56:59], v[172:175]\n"
    "v_exp_f32 v162, v162\n"
    "v_mfma_f32_16x16x32_bf16 v[172:175], a[132:135], a[52:55], v[172:175]\n"
    "ds_write_b128 v228, v[252:255] offset:17408\n"
    "v_cvt_pk_bf16_f32 v184, v160, v161\n"
    "v_pk_mul_f32 v[238:239], v[238:239], s[76:77] op_sel_hi:[1,0]\n"
    "v_pk_mul_f32 v[244:245], v[244:245], s[76:77] op_sel_hi:[1,0]\n"
    "v_pk_mul_f32 v[240:241], v[240:241], s[76:77] op_sel_hi:[1,0]\n"
    "v_pk_mul_f32 v[246:247], v[246:247], s[76:77] op_sel_hi:[1,0]\n"
    "v_mfma_f32_16x16x32_bf16 v[172:175], a[128:131], a[48:51], v[172:175]\n"
    "v_exp_f32 v163, v163\n"
    "v_smfmac_f32_16x16x64_bf16 v[200:203], a[240:243], v[176:183], v237\n"
    "ds_read_b64_tr_b16 a[202:203], v219 offset:45664\n"
    "v_cvt_pk_bf16_f32 v244, v238, v244\n"
    "v_mfma_f32_16x16x32_bf16 v[64:67], a[208:211], v[180:183], v[64:67]\n"
    "v_exp_f32 v164, v164\n"
    "v_mfma_f32_16x16x32_bf16 v[64:67], a[144:147], v[176:179], v[64:67]\n"
    "ds_read_b64_tr_b16 a[204:205], v219 offset:41544\n"
    "v_cvt_pk_bf16_f32 v245, v239, v245\n"
    "v_mfma_f32_16x16x32_bf16 v[68:71], a[148:151], v[176:179], v[68:71]\n"
    "s_waitcnt lgkmcnt(5)\n"
    "buffer_store_dwordx4 a[248:251], v228, s[48:51], s56 offen offset:3072 nt\n"
    "v_exp_f32 v165, v165\n"
    "v_mfma_f32_16x16x32_bf16 v[68:71], a[212:215], v[180:183], v[68:71]\n"
    "ds_read_b64_tr_b16 a[206:207], v219 offset:45672\n"
    "v_cvt_pk_bf16_f32 v185, v162, v163\n"
    "v_mfma_f32_16x16x32_bf16 v[72:75], a[216:219], v[180:183], v[72:75]\n"
    "v_exp_f32 v166, v166\n"
    "v_mfma_f32_16x16x32_bf16 v[72:75], a[152:155], v[176:179], v[72:75]\n"
    "v_cvt_pk_bf16_f32 v246, v240, v246\n"
    "v_cvt_pk_bf16_f32 v247, v241, v247\n"
    "v_mfma_f32_16x16x32_bf16 v[76:79], a[156:159], v[176:179], v[76:79]\n"
    "v_exp_f32 v167, v167\n"
    "v_mfma_f32_16x16x32_bf16 v[76:79], a[220:223], v[180:183], v[76:79]\n"
    "s_waitcnt vmcnt(9)\n"
    "ds_read_b128 v[248:251], v228 offset:21504\n"
    "v_cvt_pk_bf16_f32 v186, v164, v165\n"
    "v_mfma_f32_16x16x32_bf16 v[80:83], a[224:227], v[180:183], v[80:83]\n"
    "v_exp_f32 v168, v168\n"
    "v_mfma_f32_16x16x32_bf16 v[80:83], a[160:163], v[176:179], v[80:83]\n"
    "v_cvt_pk_bf16_f32 v187, v166, v167\n"
    "v_mfma_f32_16x16x32_bf16 v[84:87], a[164:167], v[176:179], v[84:87]\n"
    "v_exp_f32 v169, v169\n"
    "v_mfma_f32_16x16x32_bf16 v[84:87], a[228:231], v[180:183], v[84:87]\n"
    "v_cvt_pk_bf16_f32 v188, v168, v169\n"
    "v_mfma_f32_16x16x32_bf16 v[88:91], a[232:235], v[180:183], v[88:91]\n"
    "v_exp_f32 v170, v170\n"
    "v_mfma_f32_16x16x32_bf16 v[88:91], a[168:171], v[176:179], v[88:91]\n"
    "v_mfma_f32_16x16x32_bf16 v[92:95], a[172:175], v[176:179], v[92:95]\n"
    "v_exp_f32 v171, v171\n"
    "v_mfma_f32_16x16x32_bf16 v[92:95], a[236:239], v[180:183], v[92:95]\n"
    "v_cvt_pk_bf16_f32 v189, v170, v171\n"
    "v_mfma_f32_16x16x32_bf16 v[160:163], a[80:83], a[64:67], -4.0\n"
    "v_exp_f32 v172, v172\n"
    "v_mfma_f32_16x16x32_bf16 v[160:163], a[84:87], a[68:71], v[160:163]\n"
    "s_waitcnt lgkmcnt(0)\n"
    "v_lshlrev_b32 v238, 16, v248\n"
    "v_and_b32 v248, s80, v248\n"
    "v_mfma_f32_16x16x32_bf16 v[160:163], a[88:91], a[72:75], v[160:163]\n"
    "v_exp_f32 v173, v173\n"
    "v_mfma_f32_16x16x32_bf16 v[160:163], a[92:95], a[76:79], v[160:163]\n"
    "v_cvt_pk_bf16_f32 v190, v172, v173\n"
    "v_lshlrev_b32 v239, 16, v249\n"
    "v_mfma_f32_16x16x32_bf16 v[164:167], a[108:111], a[76:79], -4.0\n"
    "v_exp_f32 v174, v174\n"
    "v_mfma_f32_16x16x32_bf16 v[164:167], a[104:107], a[72:75], v[164:167]\n"
    "v_and_b32 v249, s80, v249\n"
    "v_lshlrev_b32 v240, 16, v250\n"
    "v_mfma_f32_16x16x32_bf16 v[164:167], a[100:103], a[68:71], v[164:167]\n"
    "v_exp_f32 v175, v175\n"
    "v_mfma_f32_16x16x32_bf16 v[164:167], a[96:99], a[64:67], v[164:167]\n"
    "v_cvt_pk_bf16_f32 v191, v174, v175\n"
    "v_and_b32 v250, s80, v250\n"
    "v_mfma_f32_16x16x32_bf16 v[168:171], a[112:115], a[64:67], -4.0\n"
    "s_barrier\n"
    "v_mfma_f32_16x16x32_bf16 v[168:171], a[116:119], a[68:71], v[168:171]\n"
    "v_exp_f32 v160, v160\n"
    "v_mfma_f32_16x16x32_bf16 v[168:171], a[120:123], a[72:75], v[168:171]\n"
    "ds_read_b128 a[80:83], v212 offset:16640\n"
    "ds_read_b128 a[84:87], v213 offset:16640\n"
    "v_mfma_f32_16x16x32_bf16 v[168:171], a[124:127], a[76:79], v[168:171]\n"
    "v_exp_f32 v161, v161\n"
    "v_mfma_f32_16x16x32_bf16 v[172:175], a[140:143], a[76:79], -4.0\n"
    "ds_read_b128 a[88:91], v214 offset:16640\n"
    "ds_read_b128 a[92:95], v215 offset:16640\n"
    "v_mfma_f32_16x16x32_bf16 v[172:175], a[136:139], a[72:75], v[172:175]\n"
    "v_exp_f32 v162, v162\n"
    "v_mfma_f32_16x16x32_bf16 v[172:175], a[132:135], a[68:71], v[172:175]\n"
    "ds_read_b128 a[108:111], v215 offset:20800\n"
    "ds_read_b128 a[104:107], v214 offset:20800\n"
    "v_mfma_f32_16x16x32_bf16 v[172:175], a[128:131], a[64:67], v[172:175]\n"
    "v_exp_f32 v163, v163\n"
    "v_smfmac_f32_16x16x64_bf16 v[204:207], a[240:243], v[184:191], v237\n"
    "ds_read_b128 a[100:103], v213 offset:20800\n"
    "ds_read_b128 a[96:99], v212 offset:20800\n"
    "v_mfma_f32_16x16x32_bf16 v[96:99], a[208:211], v[188:191], v[96:99]\n"
    "v_exp_f32 v164, v164\n"
    "v_mfma_f32_16x16x32_bf16 v[96:99], a[144:147], v[184:187], v[96:99]\n"
    "v_cvt_pk_bf16_f32 v176, v160, v161\n"
    "v_cvt_pk_bf16_f32 v177, v162, v163\n"
    "v_mfma_f32_16x16x32_bf16 v[100:103], a[148:151], v[184:187], v[100:103]\n"
    "v_exp_f32 v165, v165\n"
    "v_mfma_f32_16x16x32_bf16 v[100:103], a[212:215], v[188:191], v[100:103]\n"
    "ds_read_b128 a[112:115], v212 offset:24960\n"
    "v_lshlrev_b32 v241, 16, v251\n"
    "v_mfma_f32_16x16x32_bf16 v[104:107], a[216:219], v[188:191], v[104:107]\n"
    "s_add_u32 m0, s58, 8256\n"
    "v_exp_f32 v166, v166\n"
    "v_mfma_f32_16x16x32_bf16 v[104:107], a[152:155], v[184:187], v[104:107]\n"
    "buffer_load_dwordx4 v229, s[40:43], s53 offen lds\n"
    "v_mfma_f32_16x16x32_bf16 v[108:111], a[156:159], v[184:187], v[108:111]\n"
    "s_add_u32 m0, s58, 8264\n"
    "v_exp_f32 v167, v167\n"
    "v_mfma_f32_16x16x32_bf16 v[108:111], a[220:223], v[188:191], v[108:111]\n"
    "buffer_load_dwordx4 v229, s[40:43], s53 offen offset:1024 lds\n"
    "v_mfma_f32_16x16x32_bf16 v[112:115], a[224:227], v[188:191], v[112:115]\n"
    "v_exp_f32 v168, v168\n"
    "v_mfma_f32_16x16x32_bf16 v[112:115], a[160:163], v[184:187], v[112:115]\n"
    "ds_read_b128 a[116:119], v213 offset:24960\n"
    "ds_read_b128 a[120:123], v214 offset:24960\n"
    "v_mfma_f32_16x16x32_bf16 v[116:119], a[164:167], v[184:187], v[116:119]\n"
    "v_exp_f32 v169, v169\n"
    "v_mfma_f32_16x16x32_bf16 v[116:119], a[228:231], v[188:191], v[116:119]\n"
    "ds_read_b128 a[124:127], v215 offset:24960\n"
    "v_cvt_pk_bf16_f32 v178, v164, v165\n"
    "v_mfma_f32_16x16x32_bf16 v[120:123], a[232:235], v[188:191], v[120:123]\n"
    "v_exp_f32 v170, v170\n"
    "v_mfma_f32_16x16x32_bf16 v[120:123], a[168:171], v[184:187], v[120:123]\n"
    "ds_read_b128 a[140:143], v215 offset:29120\n"
    "v_cvt_pk_bf16_f32 v179, v166, v167\n"
    "v_mfma_f32_16x16x32_bf16 v[124:127], a[172:175], v[184:187], v[124:127]\n"
    "v_exp_f32 v171, v171\n"
    "v_mfma_f32_16x16x32_bf16 v[124:127], a[236:239], v[188:191], v[124:127]\n"
    "s_waitcnt lgkmcnt(4)\n"
    "ds_read_b128 a[136:139], v214 offset:29120\n"
    "ds_read_b128 a[132:135], v213 offset:29120\n"
    "v_mfma_f32_16x16x32_bf16 v[160:163], a[80:83], a[0:3], -4.0\n"
    "v_exp_f32 v172, v172\n"
    "v_mfma_f32_16x16x32_bf16 v[160:163], a[84:87], a[4:7], v[160:163]\n"
    "ds_read_b128 a[128:131], v212 offset:29120\n"
    "v_cvt_pk_bf16_f32 v180, v168, v169\n"
    "v_mfma_f32_16x16x32_bf16 v[160:163], a[88:91], a[8:11], v[160:163]\n"
    "s_mov_b32 s64, s70\n"
    "v_exp_f32 v173, v173\n"
    "v_mfma_f32_16x16x32_bf16 v[160:163], a[92:95], a[12:15], v[160:163]\n"
    "v_cvt_pk_bf16_f32 v181, v170, v171\n"
    "v_cvt_pk_bf16_f32 v182, v172, v173\n"
    "v_mfma_f32_16x16x32_bf16 v[164:167], a[108:111], a[12:15], -4.0\n"
    "s_mov_b32 s50, s81\n"
    "v_exp_f32 v174, v174\n"
    "v_mfma_f32_16x16x32_bf16 v[164:167], a[104:107], a[8:11], v[164:167]\n"
    "ds_write_b128 v228, v[244:247] offset:18432\n"
    "v_and_b32 v251, s80, v251\n"
    "v_pk_mul_f32 v[238:239], v[238:239], s[76:77] op_sel_hi:[1,0]\n"
    "v_pk_mul_f32 v[248:249], v[248:249], s[76:77] op_sel_hi:[1,0]\n"
    "v_pk_mul_f32 v[240:241], v[240:241], s[76:77] op_sel_hi:[1,0]\n"
    "v_pk_mul_f32 v[250:251], v[250:251], s[76:77] op_sel_hi:[1,0]\n"
    "v_mfma_f32_16x16x32_bf16 v[164:167], a[100:103], a[4:7], v[164:167]\n"
    "v_exp_f32 v175, v175\n"
    "v_mfma_f32_16x16x32_bf16 v[164:167], a[96:99], a[0:3], v[164:167]\n"
    "v_cvt_pk_bf16_f32 v183, v174, v175\n"
    "v_cvt_pk_bf16_f32 v248, v238, v248\n"
    "v_mfma_f32_16x16x32_bf16 v[168:171], a[112:115], a[0:3], -4.0\n"
    "s_waitcnt lgkmcnt(2)\n"
    "v_cvt_pk_bf16_f32 v249, v239, v249\n"
    "v_cvt_pk_bf16_f32 v250, v240, v250\n"
    "v_mfma_f32_16x16x32_bf16 v[168:171], a[116:119], a[4:7], v[168:171]\n"
    "v_exp_f32 v160, v160\n"
    "v_mfma_f32_16x16x32_bf16 v[168:171], a[120:123], a[8:11], v[168:171]\n"
    "v_cvt_pk_bf16_f32 v251, v241, v251\n"
    "v_mfma_f32_16x16x32_bf16 v[168:171], a[124:127], a[12:15], v[168:171]\n"
    "ds_write_b128 v228, v[248:251] offset:19456\n"
    "v_exp_f32 v161, v161\n"
    "v_mfma_f32_16x16x32_bf16 v[172:175], a[140:143], a[12:15], -4.0\n"
    "v_cvt_pk_bf16_f32 v184, v160, v161\n"
    "v_mfma_f32_16x16x32_bf16 v[172:175], a[136:139], a[8:11], v[172:175]\n"
    "v_exp_f32 v162, v162\n"
    "v_mfma_f32_16x16x32_bf16 v[172:175], a[132:135], a[4:7], v[172:175]\n"
    "s_waitcnt lgkmcnt(1)\n"
    "v_mfma_f32_16x16x32_bf16 v[172:175], a[128:131], a[0:3], v[172:175]\n"
    "v_exp_f32 v163, v163\n"
    "v_smfmac_f32_16x16x64_bf16 v[208:211], a[240:243], v[176:183], v237\n"
    "ds_read_b128 a[0:3], v220 offset:0\n"
    "v_cvt_pk_bf16_f32 v185, v162, v163\n"
    "v_mfma_f32_16x16x32_bf16 v[128:131], a[208:211], v[180:183], v[128:131]\n"
    "v_exp_f32 v164, v164\n"
    "v_mfma_f32_16x16x32_bf16 v[128:131], a[144:147], v[176:179], v[128:131]\n"
    "ds_read_b64_tr_b16 a[208:209], v216 offset:58048\n"
    "ds_read_b64_tr_b16 a[210:211], v216 offset:62176\n"
    "v_mfma_f32_16x16x32_bf16 v[132:135], a[148:151], v[176:179], v[132:135]\n"
    "v_exp_f32 v165, v165\n"
    "v_mfma_f32_16x16x32_bf16 v[132:135], a[212:215], v[180:183], v[132:135]\n"
    "ds_read_b64_tr_b16 a[212:213], v216 offset:58056\n"
    "ds_read_b64_tr_b16 a[214:215], v216 offset:62184\n"
    "v_mfma_f32_16x16x32_bf16 v[136:139], a[216:219], v[180:183], v[136:139]\n"
    "v_exp_f32 v166, v166\n"
    "v_mfma_f32_16x16x32_bf16 v[136:139], a[152:155], v[176:179], v[136:139]\n"
    "ds_read_b64_tr_b16 a[216:217], v217 offset:58048\n"
    "v_cvt_pk_bf16_f32 v186, v164, v165\n"
    "v_mfma_f32_16x16x32_bf16 v[140:143], a[156:159], v[176:179], v[140:143]\n"
    "v_exp_f32 v167, v167\n"
    "v_mfma_f32_16x16x32_bf16 v[140:143], a[220:223], v[180:183], v[140:143]\n"
    "ds_read_b64_tr_b16 a[218:219], v217 offset:62176\n"
    "v_cvt_pk_bf16_f32 v187, v166, v167\n"
    "v_mfma_f32_16x16x32_bf16 v[144:147], a[224:227], v[180:183], v[144:147]\n"
    "v_exp_f32 v168, v168\n"
    "v_mfma_f32_16x16x32_bf16 v[144:147], a[160:163], v[176:179], v[144:147]\n"
    "ds_read_b64_tr_b16 a[220:221], v217 offset:58056\n"
    "ds_read_b64_tr_b16 a[222:223], v217 offset:62184\n"
    "v_mfma_f32_16x16x32_bf16 v[148:151], a[164:167], v[176:179], v[148:151]\n"
    "v_exp_f32 v169, v169\n"
    "v_mfma_f32_16x16x32_bf16 v[148:151], a[228:231], v[180:183], v[148:151]\n"
    "ds_read_b64_tr_b16 a[224:225], v218 offset:58048\n"
    "v_cvt_pk_bf16_f32 v188, v168, v169\n"
    "v_mfma_f32_16x16x32_bf16 v[152:155], a[232:235], v[180:183], v[152:155]\n"
    "v_exp_f32 v170, v170\n"
    "v_mfma_f32_16x16x32_bf16 v[152:155], a[168:171], v[176:179], v[152:155]\n"
    "ds_read_b64_tr_b16 a[226:227], v218 offset:62176\n"
    "ds_read_b64_tr_b16 a[228:229], v218 offset:58056\n"
    "v_mfma_f32_16x16x32_bf16 v[156:159], a[172:175], v[176:179], v[156:159]\n"
    "v_exp_f32 v171, v171\n"
    "v_mfma_f32_16x16x32_bf16 v[156:159], a[236:239], v[180:183], v[156:159]\n"
    "ds_read_b64_tr_b16 a[230:231], v218 offset:62184\n"
    "v_cvt_pk_bf16_f32 v189, v170, v171\n"
    "v_mfma_f32_16x16x32_bf16 v[160:163], a[80:83], a[16:19], -4.0\n"
    "v_exp_f32 v172, v172\n"
    "v_mfma_f32_16x16x32_bf16 v[160:163], a[84:87], a[20:23], v[160:163]\n"
    "ds_read_b64_tr_b16 a[232:233], v219 offset:58048\n"
    "ds_read_b64_tr_b16 a[234:235], v219 offset:62176\n"
    "v_mfma_f32_16x16x32_bf16 v[160:163], a[88:91], a[24:27], v[160:163]\n"
    "v_exp_f32 v173, v173\n"
    "v_mfma_f32_16x16x32_bf16 v[160:163], a[92:95], a[28:31], v[160:163]\n"
    "ds_read_b64_tr_b16 a[236:237], v219 offset:58056\n"
    "v_cvt_pk_bf16_f32 v190, v172, v173\n"
    "v_mfma_f32_16x16x32_bf16 v[164:167], a[108:111], a[28:31], -4.0\n"
    "v_exp_f32 v174, v174\n"
    "v_mfma_f32_16x16x32_bf16 v[164:167], a[104:107], a[24:27], v[164:167]\n"
    "ds_read_b64_tr_b16 a[238:239], v219 offset:62184\n"
    "ds_read_b128 a[4:7], v221 offset:0\n"
    "v_mfma_f32_16x16x32_bf16 v[164:167], a[100:103], a[20:23], v[164:167]\n"
    "v_exp_f32 v175, v175\n"
    "v_mfma_f32_16x16x32_bf16 v[164:167], a[96:99], a[16:19], v[164:167]\n"
    "ds_read_b128 a[8:11], v222 offset:0\n"
    "v_cvt_pk_bf16_f32 v191, v174, v175\n"
    "v_mfma_f32_16x16x32_bf16 v[168:171], a[112:115], a[16:19], -4.0\n"
    "s_add_u32 s52, s52, 16384\n"
    "ds_read_b128 a[12:15], v223 offset:0\n"
    "v_mfma_f32_16x16x32_bf16 v[168:171], a[116:119], a[20:23], v[168:171]\n"
    "v_exp_f32 v160, v160\n"
    "v_mfma_f32_16x16x32_bf16 v[168:171], a[120:123], a[24:27], v[168:171]\n"
    "s_add_u32 s54, s54, 16384\n"
    "v_mfma_f32_16x16x32_bf16 v[168:171], a[124:127], a[28:31], v[168:171]\n"
    "v_exp_f32 v161, v161\n"
    "v_mfma_f32_16x16x32_bf16 v[172:175], a[140:143], a[28:31], -4.0\n"
    "s_add_u32 s53, s53, 16384\n"
    "v_cvt_pk_bf16_f32 v176, v160, v161\n"
    "v_mfma_f32_16x16x32_bf16 v[172:175], a[136:139], a[24:27], v[172:175]\n"
    "v_exp_f32 v162, v162\n"
    "v_mfma_f32_16x16x32_bf16 v[172:175], a[132:135], a[20:23], v[172:175]\n"
    "v_mfma_f32_16x16x32_bf16 v[172:175], a[128:131], a[16:19], v[172:175]\n"
    "v_exp_f32 v163, v163\n"
    "v_smfmac_f32_16x16x64_bf16 v[192:195], a[240:243], v[184:191], v237\n"
    "s_waitcnt lgkmcnt(0)\n"
    "ds_read_b128 a[16:19], v220 offset:4096\n"
    "v_cvt_pk_bf16_f32 v177, v162, v163\n"
    "v_mfma_f32_16x16x32_bf16 v[0:3], a[208:211], v[188:191], v[0:3]\n"
    "v_exp_f32 v164, v164\n"
    "v_mfma_f32_16x16x32_bf16 v[0:3], a[176:179], v[184:187], v[0:3]\n"
    "ds_read_b128 a[20:23], v221 offset:4096\n"
    "ds_read_b128 a[24:27], v222 offset:4096\n"
    "v_mfma_f32_16x16x32_bf16 v[4:7], a[180:183], v[184:187], v[4:7]\n"
    "v_exp_f32 v165, v165\n"
    "v_mfma_f32_16x16x32_bf16 v[4:7], a[212:215], v[188:191], v[4:7]\n"
    "ds_read_b128 a[28:31], v223 offset:4096\n"
    "v_cvt_pk_bf16_f32 v178, v164, v165\n"
    "v_mfma_f32_16x16x32_bf16 v[8:11], a[216:219], v[188:191], v[8:11]\n"
    "v_exp_f32 v166, v166\n"
    "v_mfma_f32_16x16x32_bf16 v[8:11], a[184:187], v[184:187], v[8:11]\n"
    "v_add_f32 v231, v192, v193\n"
    "v_mfma_f32_16x16x32_bf16 v[12:15], a[188:191], v[184:187], v[12:15]\n"
    "v_exp_f32 v167, v167\n"
    "v_mfma_f32_16x16x32_bf16 v[12:15], a[220:223], v[188:191], v[12:15]\n"
    "s_barrier\n"
    "v_mfma_f32_16x16x32_bf16 v[16:19], a[224:227], v[188:191], v[16:19]\n"
    "s_add_u32 m0, s57, 16640\n"
    "v_exp_f32 v168, v168\n"
    "v_mfma_f32_16x16x32_bf16 v[16:19], a[192:195], v[184:187], v[16:19]\n"
    "buffer_load_dwordx4 v229, s[36:39], s52 offen lds\n"
    "v_mfma_f32_16x16x32_bf16 v[20:23], a[196:199], v[184:187], v[20:23]\n"
    "v_exp_f32 v169, v169\n"
    "v_mfma_f32_16x16x32_bf16 v[20:23], a[228:231], v[188:191], v[20:23]\n"
    "v_cvt_pk_bf16_f32 v179, v166, v167\n"
    "v_cvt_pk_bf16_f32 v180, v168, v169\n"
    "v_mfma_f32_16x16x32_bf16 v[24:27], a[232:235], v[188:191], v[24:27]\n"
    "v_exp_f32 v170, v170\n"
    "v_mfma_f32_16x16x32_bf16 v[24:27], a[200:203], v[184:187], v[24:27]\n"
    "v_rcp_f32 v230, v231\n"
    "v_mfma_f32_16x16x32_bf16 v[28:31], a[204:207], v[184:187], v[28:31]\n"
    "v_exp_f32 v171, v171\n"
    "v_mfma_f32_16x16x32_bf16 v[28:31], a[236:239], v[188:191], v[28:31]\n"
    "v_cvt_pk_bf16_f32 v181, v170, v171\n"
    "v_subrev_u32 v234, s75, v231\n"
    "v_mfma_f32_16x16x32_bf16 v[160:163], a[80:83], a[32:35], -4.0\n"
    "v_exp_f32 v172, v172\n"
    "v_mfma_f32_16x16x32_bf16 v[160:163], a[84:87], a[36:39], v[160:163]\n"
    "v_cmp_lt_u32 vcc, s77, v234\n"
    "v_mov_b32 v192, 0\n"
    "v_mfma_f32_16x16x32_bf16 v[160:163], a[88:91], a[40:43], v[160:163]\n"
    "s_add_u32 m0, s57, 16640\n"
    "v_exp_f32 v173, v173\n"
    "v_mfma_f32_16x16x32_bf16 v[160:163], a[92:95], a[44:47], v[160:163]\n"
    "buffer_load_dwordx4 v229, s[36:39], s52 offen offset:1024 lds\n"
    "v_mfma_f32_16x16x32_bf16 v[164:167], a[108:111], a[44:47], -4.0\n"
    "s_cmp_lg_u64 vcc, 0\n"
    "s_addc_u32 s78, s78, s78\n"
    "v_exp_f32 v174, v174\n"
    "v_mfma_f32_16x16x32_bf16 v[164:167], a[104:107], a[40:43], v[164:167]\n"
    "v_cvt_pk_bf16_f32 v182, v172, v173\n"
    "v_mov_b32 v193, 0\n"
    "v_pk_mul_f32 v[0:1], v[0:1], v[230:231] op_sel_hi:[1,0]\n"
    "v_pk_mul_f32 v[2:3], v[2:3], v[230:231] op_sel_hi:[1,0]\n"
    "v_pk_mul_f32 v[4:5], v[4:5], v[230:231] op_sel_hi:[1,0]\n"
    "v_pk_mul_f32 v[6:7], v[6:7], v[230:231] op_sel_hi:[1,0]\n"
    "v_pk_mul_f32 v[8:9], v[8:9], v[230:231] op_sel_hi:[1,0]\n"
    "v_pk_mul_f32 v[10:11], v[10:11], v[230:231] op_sel_hi:[1,0]\n"
    "v_pk_mul_f32 v[12:13], v[12:13], v[230:231] op_sel_hi:[1,0]\n"
    "v_pk_mul_f32 v[14:15], v[14:15], v[230:231] op_sel_hi:[1,0]\n"
    "v_pk_mul_f32 v[16:17], v[16:17], v[230:231] op_sel_hi:[1,0]\n"
    "v_pk_mul_f32 v[18:19], v[18:19], v[230:231] op_sel_hi:[1,0]\n"
    "v_pk_mul_f32 v[20:21], v[20:21], v[230:231] op_sel_hi:[1,0]\n"
    "v_pk_mul_f32 v[22:23], v[22:23], v[230:231] op_sel_hi:[1,0]\n"
    "v_pk_mul_f32 v[24:25], v[24:25], v[230:231] op_sel_hi:[1,0]\n"
    "v_pk_mul_f32 v[26:27], v[26:27], v[230:231] op_sel_hi:[1,0]\n"
    "v_pk_mul_f32 v[28:29], v[28:29], v[230:231] op_sel_hi:[1,0]\n"
    "v_pk_mul_f32 v[30:31], v[30:31], v[230:231] op_sel_hi:[1,0]\n"
    "v_mfma_f32_16x16x32_bf16 v[164:167], a[100:103], a[36:39], v[164:167]\n"
    "v_exp_f32 v175, v175\n"
    "v_mfma_f32_16x16x32_bf16 v[164:167], a[96:99], a[32:35], v[164:167]\n"
    "ds_read_b64_tr_b16 a[144:145], v216 offset:33280\n"
    "v_cvt_pk_bf16_f32 v183, v174, v175\n"
    "v_mfma_f32_16x16x32_bf16 v[168:171], a[112:115], a[32:35], -4.0\n"
    "ds_read_b64_tr_b16 a[146:147], v216 offset:37408\n"
    "v_cvt_pk_bf16_f32 v0, v0, v1\n"
    "v_mfma_f32_16x16x32_bf16 v[168:171], a[116:119], a[36:39], v[168:171]\n"
    "v_exp_f32 v160, v160\n"
    "v_mfma_f32_16x16x32_bf16 v[168:171], a[120:123], a[40:43], v[168:171]\n"
    "v_cvt_pk_bf16_f32 v1, v2, v3\n"
    "v_cvt_pk_bf16_f32 v2, v4, v5\n"
    "v_mfma_f32_16x16x32_bf16 v[168:171], a[124:127], a[44:47], v[168:171]\n"
    "s_add_u32 m0, s57, 16672\n"
    "v_exp_f32 v161, v161\n"
    "v_mfma_f32_16x16x32_bf16 v[172:175], a[140:143], a[44:47], -4.0\n"
    "buffer_load_dwordx4 v229, s[36:39], s52 offen offset:2048 lds\n"
    "v_mfma_f32_16x16x32_bf16 v[172:175], a[136:139], a[40:43], v[172:175]\n"
    "v_exp_f32 v162, v162\n"
    "v_mfma_f32_16x16x32_bf16 v[172:175], a[132:135], a[36:39], v[172:175]\n"
    "ds_read_b64_tr_b16 a[148:149], v216 offset:33288\n"
    "v_cvt_pk_bf16_f32 v184, v160, v161\n"
    "v_mfma_f32_16x16x32_bf16 v[172:175], a[128:131], a[32:35], v[172:175]\n"
    "v_exp_f32 v163, v163\n"
    "v_smfmac_f32_16x16x64_bf16 v[196:199], a[240:243], v[176:183], v237\n"
    "ds_read_b64_tr_b16 a[150:151], v216 offset:37416\n"
    "v_cvt_pk_bf16_f32 v185, v162, v163\n"
    "v_mfma_f32_16x16x32_bf16 v[32:35], a[208:211], v[180:183], v[32:35]\n"
    "v_exp_f32 v164, v164\n"
    "v_mfma_f32_16x16x32_bf16 v[32:35], a[176:179], v[176:179], v[32:35]\n"
    "ds_read_b64_tr_b16 a[152:153], v217 offset:33280\n"
    "v_cvt_pk_bf16_f32 v3, v6, v7\n"
    "v_mfma_f32_16x16x32_bf16 v[36:39], a[180:183], v[176:179], v[36:39]\n"
    "s_add_u32 m0, s57, 16672\n"
    "v_exp_f32 v165, v165\n"
    "v_mfma_f32_16x16x32_bf16 v[36:39], a[212:215], v[180:183], v[36:39]\n"
    "buffer_load_dwordx4 v229, s[36:39], s52 offen offset:3072 lds\n"
    "v_mfma_f32_16x16x32_bf16 v[40:43], a[216:219], v[180:183], v[40:43]\n"
    "v_exp_f32 v166, v166\n"
    "v_mfma_f32_16x16x32_bf16 v[40:43], a[184:187], v[176:179], v[40:43]\n"
    "v_cvt_pk_bf16_f32 v186, v164, v165\n"
    "v_add_f32 v233, v196, v197\n"
    "v_mfma_f32_16x16x32_bf16 v[44:47], a[188:191], v[176:179], v[44:47]\n"
    "v_exp_f32 v167, v167\n"
    "v_mfma_f32_16x16x32_bf16 v[44:47], a[220:223], v[180:183], v[44:47]\n"
    "ds_read_b64_tr_b16 a[154:155], v217 offset:37408\n"
    "v_cvt_pk_bf16_f32 v187, v166, v167\n"
    "v_mfma_f32_16x16x32_bf16 v[48:51], a[224:227], v[180:183], v[48:51]\n"
    "v_exp_f32 v168, v168\n"
    "v_mfma_f32_16x16x32_bf16 v[48:51], a[192:195], v[176:179], v[48:51]\n"
    "v_rcp_f32 v232, v233\n"
    "v_mfma_f32_16x16x32_bf16 v[52:55], a[196:199], v[176:179], v[52:55]\n"
    "v_exp_f32 v169, v169\n"
    "v_mfma_f32_16x16x32_bf16 v[52:55], a[228:231], v[180:183], v[52:55]\n"
    "v_cvt_pk_bf16_f32 v188, v168, v169\n"
    "v_subrev_u32 v234, s75, v233\n"
    "v_mfma_f32_16x16x32_bf16 v[56:59], a[232:235], v[180:183], v[56:59]\n"
    "s_add_u32 m0, s58, 24768\n"
    "v_exp_f32 v170, v170\n"
    "v_mfma_f32_16x16x32_bf16 v[56:59], a[200:203], v[176:179], v[56:59]\n"
    "buffer_load_dwordx4 v229, s[40:43], s54 offen lds\n"
    "v_mfma_f32_16x16x32_bf16 v[60:63], a[204:207], v[176:179], v[60:63]\n"
    "v_exp_f32 v171, v171\n"
    "v_mfma_f32_16x16x32_bf16 v[60:63], a[236:239], v[180:183], v[60:63]\n"
    "v_cvt_pk_bf16_f32 v189, v170, v171\n"
    "v_cmp_lt_u32 vcc, s77, v234\n"
    "v_mfma_f32_16x16x32_bf16 v[160:163], a[80:83], a[48:51], -4.0\n"
    "v_exp_f32 v172, v172\n"
    "v_mfma_f32_16x16x32_bf16 v[160:163], a[84:87], a[52:55], v[160:163]\n"
    "ds_read_b64_tr_b16 a[156:157], v217 offset:33288\n"
    "ds_read_b64_tr_b16 a[158:159], v217 offset:37416\n"
    "v_mfma_f32_16x16x32_bf16 v[160:163], a[88:91], a[56:59], v[160:163]\n"
    "s_cmp_lg_u64 vcc, 0\n"
    "s_addc_u32 s78, s78, s78\n"
    "v_exp_f32 v173, v173\n"
    "v_mfma_f32_16x16x32_bf16 v[160:163], a[92:95], a[60:63], v[160:163]\n"
    "ds_read_b64_tr_b16 a[160:161], v218 offset:33280\n"
    "v_cvt_pk_bf16_f32 v190, v172, v173\n"
    "v_mfma_f32_16x16x32_bf16 v[164:167], a[108:111], a[60:63], -4.0\n"
    "s_add_u32 m0, s58, 24776\n"
    "v_exp_f32 v174, v174\n"
    "v_mfma_f32_16x16x32_bf16 v[164:167], a[104:107], a[56:59], v[164:167]\n"
    "buffer_load_dwordx4 v229, s[40:43], s54 offen offset:1024 lds\n"
    "v_mfma_f32_16x16x32_bf16 v[164:167], a[100:103], a[52:55], v[164:167]\n"
    "s_cmp_le_u32 s79, 1\n"
    "s_cselect_b32 s46, 0, s46\n"
    "v_exp_f32 v175, v175\n"
    "v_mfma_f32_16x16x32_bf16 v[164:167], a[96:99], a[48:51], v[164:167]\n"
    "ds_write_b128 v220, v[0:3] offset:4096\n"
    "v_cvt_pk_bf16_f32 v191, v174, v175\n"
    "v_cvt_pk_bf16_f32 v8, v8, v9\n"
    "v_pk_mul_f32 v[32:33], v[32:33], v[232:233] op_sel_hi:[1,0]\n"
    "v_pk_mul_f32 v[34:35], v[34:35], v[232:233] op_sel_hi:[1,0]\n"
    "v_pk_mul_f32 v[36:37], v[36:37], v[232:233] op_sel_hi:[1,0]\n"
    "v_pk_mul_f32 v[38:39], v[38:39], v[232:233] op_sel_hi:[1,0]\n"
    "v_pk_mul_f32 v[40:41], v[40:41], v[232:233] op_sel_hi:[1,0]\n"
    "v_pk_mul_f32 v[42:43], v[42:43], v[232:233] op_sel_hi:[1,0]\n"
    "v_pk_mul_f32 v[44:45], v[44:45], v[232:233] op_sel_hi:[1,0]\n"
    "v_pk_mul_f32 v[46:47], v[46:47], v[232:233] op_sel_hi:[1,0]\n"
    "v_pk_mul_f32 v[48:49], v[48:49], v[232:233] op_sel_hi:[1,0]\n"
    "v_pk_mul_f32 v[50:51], v[50:51], v[232:233] op_sel_hi:[1,0]\n"
    "v_pk_mul_f32 v[52:53], v[52:53], v[232:233] op_sel_hi:[1,0]\n"
    "v_pk_mul_f32 v[54:55], v[54:55], v[232:233] op_sel_hi:[1,0]\n"
    "v_pk_mul_f32 v[56:57], v[56:57], v[232:233] op_sel_hi:[1,0]\n"
    "v_pk_mul_f32 v[58:59], v[58:59], v[232:233] op_sel_hi:[1,0]\n"
    "v_pk_mul_f32 v[60:61], v[60:61], v[232:233] op_sel_hi:[1,0]\n"
    "v_pk_mul_f32 v[62:63], v[62:63], v[232:233] op_sel_hi:[1,0]\n"
    "v_mfma_f32_16x16x32_bf16 v[168:171], a[112:115], a[48:51], -4.0\n"
    "s_mov_b32 s55, s72\n"
    "ds_read_b64_tr_b16 a[162:163], v218 offset:37408\n"
    "ds_read_b64_tr_b16 a[164:165], v218 offset:33288\n"
    "v_mfma_f32_16x16x32_bf16 v[168:171], a[116:119], a[52:55], v[168:171]\n"
    "v_exp_f32 v160, v160\n"
    "v_mfma_f32_16x16x32_bf16 v[168:171], a[120:123], a[56:59], v[168:171]\n"
    "ds_read_b64_tr_b16 a[166:167], v218 offset:37416\n"
    "ds_read_b64_tr_b16 a[168:169], v219 offset:33280\n"
    "v_mfma_f32_16x16x32_bf16 v[168:171], a[124:127], a[60:63], v[168:171]\n"
    "v_exp_f32 v161, v161\n"
    "v_mfma_f32_16x16x32_bf16 v[172:175], a[140:143], a[60:63], -4.0\n"
    "ds_read_b64_tr_b16 a[170:171], v219 offset:37408\n"
    "v_cvt_pk_bf16_f32 v176, v160, v161\n"
    "v_mfma_f32_16x16x32_bf16 v[172:175], a[136:139], a[56:59], v[172:175]\n"
    "v_exp_f32 v162, v162\n"
    "v_mfma_f32_16x16x32_bf16 v[172:175], a[132:135], a[52:55], v[172:175]\n"
    "ds_read_b64_tr_b16 a[172:173], v219 offset:33288\n"
    "ds_read_b128 a[32:35], v220 offset:8192\n"
    "v_mfma_f32_16x16x32_bf16 v[172:175], a[128:131], a[48:51], v[172:175]\n"
    "v_exp_f32 v163, v163\n"
    "v_smfmac_f32_16x16x64_bf16 v[200:203], a[240:243], v[184:191], v237\n"
    "ds_read_b128 a[36:39], v221 offset:8192\n"
    "v_cvt_pk_bf16_f32 v177, v162, v163\n"
    "v_mfma_f32_16x16x32_bf16 v[64:67], a[208:211], v[188:191], v[64:67]\n"
    "v_exp_f32 v164, v164\n"
    "v_mfma_f32_16x16x32_bf16 v[64:67], a[176:179], v[184:187], v[64:67]\n"
    "ds_read_b64_tr_b16 a[174:175], v219 offset:37416\n"
    "ds_read_b128 a[40:43], v222 offset:8192\n"
    "v_mfma_f32_16x16x32_bf16 v[68:71], a[180:183], v[184:187], v[68:71]\n"
    "v_exp_f32 v165, v165\n"
    "v_mfma_f32_16x16x32_bf16 v[68:71], a[212:215], v[188:191], v[68:71]\n"
    "buffer_load_dword v235, v236, s[84:87], 0 offen\n"
    "v_mfma_f32_16x16x32_bf16 v[72:75], a[216:219], v[188:191], v[72:75]\n"
    "v_exp_f32 v166, v166\n"
    "v_mfma_f32_16x16x32_bf16 v[72:75], a[184:187], v[184:187], v[72:75]\n"
    "ds_read_b128 a[44:47], v223 offset:8192\n"
    "v_cvt_pk_bf16_f32 v178, v164, v165\n"
    "v_mfma_f32_16x16x32_bf16 v[76:79], a[188:191], v[184:187], v[76:79]\n"
    "v_exp_f32 v167, v167\n"
    "v_mfma_f32_16x16x32_bf16 v[76:79], a[220:223], v[188:191], v[76:79]\n"
    "v_cvt_pk_bf16_f32 v179, v166, v167\n"
    "v_add_f32 v231, v200, v201\n"
    "v_mfma_f32_16x16x32_bf16 v[80:83], a[224:227], v[188:191], v[80:83]\n"
    "v_exp_f32 v168, v168\n"
    "v_mfma_f32_16x16x32_bf16 v[80:83], a[192:195], v[184:187], v[80:83]\n"
    "v_rcp_f32 v230, v231\n"
    "v_mfma_f32_16x16x32_bf16 v[84:87], a[196:199], v[184:187], v[84:87]\n"
    "v_exp_f32 v169, v169\n"
    "v_mfma_f32_16x16x32_bf16 v[84:87], a[228:231], v[188:191], v[84:87]\n"
    "v_cvt_pk_bf16_f32 v180, v168, v169\n"
    "v_subrev_u32 v234, s75, v231\n"
    "v_mfma_f32_16x16x32_bf16 v[88:91], a[232:235], v[188:191], v[88:91]\n"
    "v_exp_f32 v170, v170\n"
    "v_mfma_f32_16x16x32_bf16 v[88:91], a[200:203], v[184:187], v[88:91]\n"
    "v_cmp_lt_u32 vcc, s77, v234\n"
    "v_cvt_pk_bf16_f32 v32, v32, v33\n"
    "v_mfma_f32_16x16x32_bf16 v[92:95], a[204:207], v[184:187], v[92:95]\n"
    "v_exp_f32 v171, v171\n"
    "v_mfma_f32_16x16x32_bf16 v[92:95], a[236:239], v[188:191], v[92:95]\n"
    "v_cvt_pk_bf16_f32 v181, v170, v171\n"
    "v_cvt_pk_bf16_f32 v33, v34, v35\n"
    "v_mfma_f32_16x16x32_bf16 v[160:163], a[80:83], a[64:67], -4.0\n"
    "s_cmp_lg_u64 vcc, 0\n"
    "s_addc_u32 s78, s78, s78\n"
    "v_exp_f32 v172, v172\n"
    "v_mfma_f32_16x16x32_bf16 v[160:163], a[84:87], a[68:71], v[160:163]\n"
    "v_cvt_pk_bf16_f32 v34, v36, v37\n"
    "v_cvt_pk_bf16_f32 v35, v38, v39\n"
    "v_mfma_f32_16x16x32_bf16 v[160:163], a[88:91], a[72:75], v[160:163]\n"
    "v_exp_f32 v173, v173\n"
    "v_mfma_f32_16x16x32_bf16 v[160:163], a[92:95], a[76:79], v[160:163]\n"
    "ds_read_b128 a[48:51], v220 offset:12288\n"
    "v_cvt_pk_bf16_f32 v182, v172, v173\n"
    "v_mfma_f32_16x16x32_bf16 v[164:167], a[108:111], a[76:79], -4.0\n"
    "v_exp_f32 v174, v174\n"
    "v_mfma_f32_16x16x32_bf16 v[164:167], a[104:107], a[72:75], v[164:167]\n"
    "ds_write_b128 v220, v[32:35] offset:8192\n"
    "v_cvt_pk_bf16_f32 v9, v10, v11\n"
    "v_cvt_pk_bf16_f32 v10, v12, v13\n"
    "v_pk_mul_f32 v[64:65], v[64:65], v[230:231] op_sel_hi:[1,0]\n"
    "v_pk_mul_f32 v[66:67], v[66:67], v[230:231] op_sel_hi:[1,0]\n"
    "v_pk_mul_f32 v[68:69], v[68:69], v[230:231] op_sel_hi:[1,0]\n"
    "v_pk_mul_f32 v[70:71], v[70:71], v[230:231] op_sel_hi:[1,0]\n"
    "v_pk_mul_f32 v[72:73], v[72:73], v[230:231] op_sel_hi:[1,0]\n"
    "v_pk_mul_f32 v[74:75], v[74:75], v[230:231] op_sel_hi:[1,0]\n"
    "v_pk_mul_f32 v[76:77], v[76:77], v[230:231] op_sel_hi:[1,0]\n"
    "v_pk_mul_f32 v[78:79], v[78:79], v[230:231] op_sel_hi:[1,0]\n"
    "v_pk_mul_f32 v[80:81], v[80:81], v[230:231] op_sel_hi:[1,0]\n"
    "v_pk_mul_f32 v[82:83], v[82:83], v[230:231] op_sel_hi:[1,0]\n"
    "v_pk_mul_f32 v[84:85], v[84:85], v[230:231] op_sel_hi:[1,0]\n"
    "v_pk_mul_f32 v[86:87], v[86:87], v[230:231] op_sel_hi:[1,0]\n"
    "v_pk_mul_f32 v[88:89], v[88:89], v[230:231] op_sel_hi:[1,0]\n"
    "v_pk_mul_f32 v[90:91], v[90:91], v[230:231] op_sel_hi:[1,0]\n"
    "v_pk_mul_f32 v[92:93], v[92:93], v[230:231] op_sel_hi:[1,0]\n"
    "v_pk_mul_f32 v[94:95], v[94:95], v[230:231] op_sel_hi:[1,0]\n"
    "v_mfma_f32_16x16x32_bf16 v[164:167], a[100:103], a[68:71], v[164:167]\n"
    "v_exp_f32 v175, v175\n"
    "v_mfma_f32_16x16x32_bf16 v[164:167], a[96:99], a[64:67], v[164:167]\n"
    "s_waitcnt vmcnt(10) lgkmcnt(2)\n"
    "ds_read_b128 a[52:55], v221 offset:12288\n"
    "v_cvt_pk_bf16_f32 v183, v174, v175\n"
    "v_mfma_f32_16x16x32_bf16 v[168:171], a[112:115], a[64:67], -4.0\n"
    "s_barrier\n"
    "v_mfma_f32_16x16x32_bf16 v[168:171], a[116:119], a[68:71], v[168:171]\n"
    "v_exp_f32 v160, v160\n"
    "v_mfma_f32_16x16x32_bf16 v[168:171], a[120:123], a[72:75], v[168:171]\n"
    "ds_read_b128 a[80:83], v212 offset:0\n"
    "ds_read_b128 a[84:87], v213 offset:0\n"
    "v_mfma_f32_16x16x32_bf16 v[168:171], a[124:127], a[76:79], v[168:171]\n"
    "v_exp_f32 v161, v161\n"
    "v_mfma_f32_16x16x32_bf16 v[172:175], a[140:143], a[76:79], -4.0\n"
    "ds_read_b128 a[88:91], v214 offset:0\n"
    "ds_read_b128 a[92:95], v215 offset:0\n"
    "v_mfma_f32_16x16x32_bf16 v[172:175], a[136:139], a[72:75], v[172:175]\n"
    "v_exp_f32 v162, v162\n"
    "v_mfma_f32_16x16x32_bf16 v[172:175], a[132:135], a[68:71], v[172:175]\n"
    "ds_read_b128 a[108:111], v215 offset:4160\n"
    "ds_read_b128 a[104:107], v214 offset:4160\n"
    "v_mfma_f32_16x16x32_bf16 v[172:175], a[128:131], a[64:67], v[172:175]\n"
    "v_exp_f32 v163, v163\n"
    "v_smfmac_f32_16x16x64_bf16 v[204:207], a[240:243], v[176:183], v237\n"
    "ds_read_b128 a[100:103], v213 offset:4160\n"
    "ds_read_b128 a[96:99], v212 offset:4160\n"
    "v_mfma_f32_16x16x32_bf16 v[96:99], a[208:211], v[180:183], v[96:99]\n"
    "v_exp_f32 v164, v164\n"
    "v_mfma_f32_16x16x32_bf16 v[96:99], a[176:179], v[176:179], v[96:99]\n"
    "v_cvt_pk_bf16_f32 v184, v160, v161\n"
    "v_cvt_pk_bf16_f32 v185, v162, v163\n"
    "v_mfma_f32_16x16x32_bf16 v[100:103], a[180:183], v[176:179], v[100:103]\n"
    "v_exp_f32 v165, v165\n"
    "v_mfma_f32_16x16x32_bf16 v[100:103], a[212:215], v[180:183], v[100:103]\n"
    "ds_read_b128 a[112:115], v212 offset:8320\n"
    "ds_read_b128 a[116:119], v213 offset:8320\n"
    "v_mfma_f32_16x16x32_bf16 v[104:107], a[216:219], v[180:183], v[104:107]\n"
    "s_add_u32 m0, s58, 0\n"
    "v_exp_f32 v166, v166\n"
    "v_mfma_f32_16x16x32_bf16 v[104:107], a[184:187], v[176:179], v[104:107]\n"
    "buffer_load_dwordx4 v229, s[40:43], s53 offen lds\n"
    "v_mfma_f32_16x16x32_bf16 v[108:111], a[188:191], v[176:179], v[108:111]\n"
    "s_add_u32 m0, s58, 8\n"
    "v_exp_f32 v167, v167\n"
    "v_mfma_f32_16x16x32_bf16 v[108:111], a[220:223], v[180:183], v[108:111]\n"
    "buffer_load_dwordx4 v229, s[40:43], s53 offen offset:1024 lds\n"
    "v_mfma_f32_16x16x32_bf16 v[112:115], a[224:227], v[180:183], v[112:115]\n"
    "v_exp_f32 v168, v168\n"
    "v_mfma_f32_16x16x32_bf16 v[112:115], a[192:195], v[176:179], v[112:115]\n"
    "ds_read_b128 a[56:59], v222 offset:12288\n"
    "ds_read_b128 a[60:63], v223 offset:12288\n"
    "v_mfma_f32_16x16x32_bf16 v[116:119], a[196:199], v[176:179], v[116:119]\n"
    "v_exp_f32 v169, v169\n"
    "v_mfma_f32_16x16x32_bf16 v[116:119], a[228:231], v[180:183], v[116:119]\n"
    "ds_read_b128 a[120:123], v214 offset:8320\n"
    "ds_read_b128 a[124:127], v215 offset:8320\n"
    "v_mfma_f32_16x16x32_bf16 v[120:123], a[232:235], v[180:183], v[120:123]\n"
    "v_exp_f32 v170, v170\n"
    "v_mfma_f32_16x16x32_bf16 v[120:123], a[200:203], v[176:179], v[120:123]\n"
    "v_cvt_pk_bf16_f32 v186, v164, v165\n"
    "v_cvt_pk_bf16_f32 v187, v166, v167\n"
    "v_mfma_f32_16x16x32_bf16 v[124:127], a[204:207], v[176:179], v[124:127]\n"
    "v_exp_f32 v171, v171\n"
    "v_mfma_f32_16x16x32_bf16 v[124:127], a[236:239], v[180:183], v[124:127]\n"
    "s_waitcnt lgkmcnt(4)\n"
    "ds_read_b128 a[140:143], v215 offset:12480\n"
    "ds_read_b128 a[136:139], v214 offset:12480\n"
    "v_mfma_f32_16x16x32_bf16 v[160:163], a[80:83], a[0:3], -4.0\n"
    "v_exp_f32 v172, v172\n"
    "v_mfma_f32_16x16x32_bf16 v[160:163], a[84:87], a[4:7], v[160:163]\n"
    "ds_read_b128 a[132:135], v213 offset:12480\n"
    "ds_read_b128 a[128:131], v212 offset:12480\n"
    "v_mfma_f32_16x16x32_bf16 v[160:163], a[88:91], a[8:11], v[160:163]\n"
    "v_exp_f32 v173, v173\n"
    "v_mfma_f32_16x16x32_bf16 v[160:163], a[92:95], a[12:15], v[160:163]\n"
    "v_cvt_pk_bf16_f32 v188, v168, v169\n"
    "v_cvt_pk_bf16_f32 v189, v170, v171\n"
    "v_mfma_f32_16x16x32_bf16 v[164:167], a[108:111], a[12:15], -4.0\n"
    "v_exp_f32 v174, v174\n"
    "v_mfma_f32_16x16x32_bf16 v[164:167], a[104:107], a[8:11], v[164:167]\n"
    "v_add_f32 v233, v204, v205\n"
    "v_cvt_pk_bf16_f32 v190, v172, v173\n"
    "v_mfma_f32_16x16x32_bf16 v[164:167], a[100:103], a[4:7], v[164:167]\n"
    "v_exp_f32 v175, v175\n"
    "v_mfma_f32_16x16x32_bf16 v[164:167], a[96:99], a[0:3], v[164:167]\n"
    "v_rcp_f32 v232, v233\n"
    "v_mfma_f32_16x16x32_bf16 v[168:171], a[112:115], a[0:3], -4.0\n"
    "v_subrev_u32 v234, s75, v233\n"
    "v_cmp_lt_u32 vcc, s77, v234\n"
    "v_mfma_f32_16x16x32_bf16 v[168:171], a[116:119], a[4:7], v[168:171]\n"
    "s_waitcnt lgkmcnt(0)\n"
    "v_exp_f32 v160, v160\n"
    "v_mfma_f32_16x16x32_bf16 v[168:171], a[120:123], a[8:11], v[168:171]\n"
    "v_cvt_pk_bf16_f32 v191, v174, v175\n"
    "v_cvt_pk_bf16_f32 v64, v64, v65\n"
    "v_mfma_f32_16x16x32_bf16 v[168:171], a[124:127], a[12:15], v[168:171]\n"
    "s_cmp_lg_u64 vcc, 0\n"
    "s_addc_u32 s78, s78, s78\n"
    "v_exp_f32 v161, v161\n"
    "v_mfma_f32_16x16x32_bf16 v[172:175], a[140:143], a[12:15], -4.0\n"
    "v_cvt_pk_bf16_f32 v65, v66, v67\n"
    "v_cvt_pk_bf16_f32 v66, v68, v69\n"
    "v_mfma_f32_16x16x32_bf16 v[172:175], a[136:139], a[8:11], v[172:175]\n"
    "v_exp_f32 v162, v162\n"
    "v_cvt_pk_bf16_f32 v67, v70, v71\n"
    "v_mfma_f32_16x16x32_bf16 v[172:175], a[132:135], a[4:7], v[172:175]\n"
    "ds_write_b128 v220, v[64:67] offset:12288\n"
    "v_cvt_pk_bf16_f32 v11, v14, v15\n"
    "v_cvt_pk_bf16_f32 v16, v16, v17\n"
    "v_pk_mul_f32 v[96:97], v[96:97], v[232:233] op_sel_hi:[1,0]\n"
    "v_pk_mul_f32 v[98:99], v[98:99], v[232:233] op_sel_hi:[1,0]\n"
    "v_pk_mul_f32 v[100:101], v[100:101], v[232:233] op_sel_hi:[1,0]\n"
    "v_pk_mul_f32 v[102:103], v[102:103], v[232:233] op_sel_hi:[1,0]\n"
    "v_pk_mul_f32 v[104:105], v[104:105], v[232:233] op_sel_hi:[1,0]\n"
    "v_pk_mul_f32 v[106:107], v[106:107], v[232:233] op_sel_hi:[1,0]\n"
    "v_pk_mul_f32 v[108:109], v[108:109], v[232:233] op_sel_hi:[1,0]\n"
    "v_pk_mul_f32 v[110:111], v[110:111], v[232:233] op_sel_hi:[1,0]\n"
    "v_pk_mul_f32 v[112:113], v[112:113], v[232:233] op_sel_hi:[1,0]\n"
    "v_pk_mul_f32 v[114:115], v[114:115], v[232:233] op_sel_hi:[1,0]\n"
    "v_pk_mul_f32 v[116:117], v[116:117], v[232:233] op_sel_hi:[1,0]\n"
    "v_pk_mul_f32 v[118:119], v[118:119], v[232:233] op_sel_hi:[1,0]\n"
    "v_pk_mul_f32 v[120:121], v[120:121], v[232:233] op_sel_hi:[1,0]\n"
    "v_pk_mul_f32 v[122:123], v[122:123], v[232:233] op_sel_hi:[1,0]\n"
    "v_pk_mul_f32 v[124:125], v[124:125], v[232:233] op_sel_hi:[1,0]\n"
    "v_pk_mul_f32 v[126:127], v[126:127], v[232:233] op_sel_hi:[1,0]\n"
    "v_mfma_f32_16x16x32_bf16 v[172:175], a[128:131], a[0:3], v[172:175]\n"
    "v_exp_f32 v163, v163\n"
    "v_smfmac_f32_16x16x64_bf16 v[208:211], a[240:243], v[184:191], v237\n"
    "ds_read_b128 a[64:67], v220 offset:16384\n"
    "v_cvt_pk_bf16_f32 v17, v18, v19\n"
    "v_mfma_f32_16x16x32_bf16 v[128:131], a[208:211], v[188:191], v[128:131]\n"
    "v_exp_f32 v164, v164\n"
    "v_mfma_f32_16x16x32_bf16 v[128:131], a[176:179], v[184:187], v[128:131]\n"
    "v_cvt_pk_bf16_f32 v18, v20, v21\n"
    "v_cvt_pk_bf16_f32 v19, v22, v23\n"
    "v_mfma_f32_16x16x32_bf16 v[132:135], a[180:183], v[184:187], v[132:135]\n"
    "ds_write_b128 v221, v[8:11] offset:4096\n"
    "v_exp_f32 v165, v165\n"
    "v_mfma_f32_16x16x32_bf16 v[132:135], a[212:215], v[188:191], v[132:135]\n"
    "v_cvt_pk_bf16_f32 v24, v24, v25\n"
    "v_cvt_pk_bf16_f32 v25, v26, v27\n"
    "v_mfma_f32_16x16x32_bf16 v[136:139], a[216:219], v[188:191], v[136:139]\n"
    "v_exp_f32 v166, v166\n"
    "v_mfma_f32_16x16x32_bf16 v[136:139], a[184:187], v[184:187], v[136:139]\n"
    "v_cvt_pk_bf16_f32 v26, v28, v29\n"
    "v_cvt_pk_bf16_f32 v27, v30, v31\n"
    "v_mfma_f32_16x16x32_bf16 v[140:143], a[188:191], v[184:187], v[140:143]\n"
    "ds_write_b128 v222, v[16:19] offset:4096\n"
    "v_exp_f32 v167, v167\n"
    "v_mfma_f32_16x16x32_bf16 v[140:143], a[220:223], v[188:191], v[140:143]\n"
    "v_cvt_pk_bf16_f32 v176, v160, v161\n"
    "v_cvt_pk_bf16_f32 v177, v162, v163\n"
    "v_mfma_f32_16x16x32_bf16 v[144:147], a[224:227], v[188:191], v[144:147]\n"
    "v_exp_f32 v168, v168\n"
    "v_mfma_f32_16x16x32_bf16 v[144:147], a[192:195], v[184:187], v[144:147]\n"
    "ds_read_b64_tr_b16 a[208:209], v216 offset:49792\n"
    "ds_read_b128 a[68:71], v221 offset:16384\n"
    "v_mfma_f32_16x16x32_bf16 v[148:151], a[196:199], v[184:187], v[148:151]\n"
    "ds_write_b128 v223, v[24:27] offset:4096\n"
    "v_exp_f32 v169, v169\n"
    "v_mfma_f32_16x16x32_bf16 v[148:151], a[228:231], v[188:191], v[148:151]\n"
    "ds_read_b128 a[72:75], v222 offset:16384\n"
    "ds_read_b128 a[76:79], v223 offset:16384\n"
    "v_mfma_f32_16x16x32_bf16 v[152:155], a[232:235], v[188:191], v[152:155]\n"
    "v_exp_f32 v170, v170\n"
    "v_mfma_f32_16x16x32_bf16 v[152:155], a[200:203], v[184:187], v[152:155]\n"
    "ds_read_b64_tr_b16 a[210:211], v216 offset:53920\n"
    "ds_read_b128 a[244:247], v224 offset:4096\n"
    "v_mfma_f32_16x16x32_bf16 v[156:159], a[204:207], v[184:187], v[156:159]\n"
    "v_exp_f32 v171, v171\n"
    "v_mfma_f32_16x16x32_bf16 v[156:159], a[236:239], v[188:191], v[156:159]\n"
    "ds_read_b64_tr_b16 a[212:213], v216 offset:49800\n"
    "ds_read_b64_tr_b16 a[214:215], v216 offset:53928\n"
    "v_mfma_f32_16x16x32_bf16 v[160:163], a[80:83], a[16:19], -4.0\n"
    "v_exp_f32 v172, v172\n"
    "v_mfma_f32_16x16x32_bf16 v[160:163], a[84:87], a[20:23], v[160:163]\n"
    "v_cvt_pk_bf16_f32 v178, v164, v165\n"
    "v_cvt_pk_bf16_f32 v179, v166, v167\n"
    "v_mfma_f32_16x16x32_bf16 v[160:163], a[88:91], a[24:27], v[160:163]\n"
    "v_exp_f32 v173, v173\n"
    "v_mfma_f32_16x16x32_bf16 v[160:163], a[92:95], a[28:31], v[160:163]\n"
    "ds_read_b64_tr_b16 a[216:217], v217 offset:49792\n"
    "ds_read_b64_tr_b16 a[218:219], v217 offset:53920\n"
    "v_mfma_f32_16x16x32_bf16 v[164:167], a[108:111], a[28:31], -4.0\n"
    "v_exp_f32 v174, v174\n"
    "v_mfma_f32_16x16x32_bf16 v[164:167], a[104:107], a[24:27], v[164:167]\n"
    "s_add_u32 s56, s64, 0\n"
    "ds_read_b64_tr_b16 a[220:221], v217 offset:49800\n"
    "ds_read_b64_tr_b16 a[222:223], v217 offset:53928\n"
    "v_mfma_f32_16x16x32_bf16 v[164:167], a[100:103], a[20:23], v[164:167]\n"
    "s_waitcnt lgkmcnt(6)\n"
    "buffer_store_dwordx4 a[244:247], v228, s[48:51], s56 offen nt\n"
    "v_exp_f32 v175, v175\n"
    "v_mfma_f32_16x16x32_bf16 v[164:167], a[96:99], a[16:19], v[164:167]\n"
    "ds_read_b64_tr_b16 a[224:225], v218 offset:49792\n"
    "v_cvt_pk_bf16_f32 v180, v168, v169\n"
    "v_cvt_pk_bf16_f32 v181, v170, v171\n"
    "v_add_f32 v231, v208, v209\n"
    "v_rcp_f32 v230, v231\n"
    "v_mfma_f32_16x16x32_bf16 v[168:171], a[112:115], a[16:19], -4.0\n"
    "s_add_u32 s52, s52, 16384\n"
    "ds_read_b64_tr_b16 a[226:227], v218 offset:53920\n"
    "v_subrev_u32 v234, s75, v231\n"
    "v_cmp_lt_u32 vcc, s77, v234\n"
    "v_mfma_f32_16x16x32_bf16 v[168:171], a[116:119], a[20:23], v[168:171]\n"
    "ds_read_b64_tr_b16 a[228:229], v218 offset:49800\n"
    "v_exp_f32 v160, v160\n"
    "v_mfma_f32_16x16x32_bf16 v[168:171], a[120:123], a[24:27], v[168:171]\n"
    "s_add_u32 s54, s54, 16384\n"
    "ds_read_b64_tr_b16 a[230:231], v218 offset:53928\n"
    "v_cvt_pk_bf16_f32 v182, v172, v173\n"
    "v_mfma_f32_16x16x32_bf16 v[168:171], a[124:127], a[28:31], v[168:171]\n"
    "s_cmp_lg_u64 vcc, 0\n"
    "s_addc_u32 s78, s78, s78\n"
    "ds_read_b64_tr_b16 a[232:233], v219 offset:49792\n"
    "v_exp_f32 v161, v161\n"
    "v_cvt_pk_bf16_f32 v183, v174, v175\n"
    "v_mfma_f32_16x16x32_bf16 v[172:175], a[140:143], a[28:31], -4.0\n"
    "s_add_u32 s53, s53, 16384\n"
    "ds_read_b64_tr_b16 a[234:235], v219 offset:53920\n"
    "ds_read_b64_tr_b16 a[236:237], v219 offset:49800\n"
    "v_mfma_f32_16x16x32_bf16 v[172:175], a[136:139], a[24:27], v[172:175]\n"
    "v_exp_f32 v162, v162\n"
    "v_cvt_pk_bf16_f32 v96, v96, v97\n"
    "v_cvt_pk_bf16_f32 v97, v98, v99\n"
    "v_cvt_pk_bf16_f32 v98, v100, v101\n"
    "v_cvt_pk_bf16_f32 v99, v102, v103\n"
    "v_mfma_f32_16x16x32_bf16 v[172:175], a[132:135], a[20:23], v[172:175]\n"
    "ds_write_b128 v220, v[96:99] offset:16384\n"
    "ds_read_b64_tr_b16 a[238:239], v219 offset:53928\n"
    "v_cvt_pk_bf16_f32 v184, v160, v161\n"
    "v_pk_mul_f32 v[128:129], v[128:129], v[230:231] op_sel_hi:[1,0]\n"
    "v_pk_mul_f32 v[130:131], v[130:131], v[230:231] op_sel_hi:[1,0]\n"
    "v_pk_mul_f32 v[132:133], v[132:133], v[230:231] op_sel_hi:[1,0]\n"
    "v_pk_mul_f32 v[134:135], v[134:135], v[230:231] op_sel_hi:[1,0]\n"
    "v_pk_mul_f32 v[136:137], v[136:137], v[230:231] op_sel_hi:[1,0]\n"
    "v_pk_mul_f32 v[138:139], v[138:139], v[230:231] op_sel_hi:[1,0]\n"
    "v_pk_mul_f32 v[140:141], v[140:141], v[230:231] op_sel_hi:[1,0]\n"
    "v_pk_mul_f32 v[142:143], v[142:143], v[230:231] op_sel_hi:[1,0]\n"
    "v_pk_mul_f32 v[144:145], v[144:145], v[230:231] op_sel_hi:[1,0]\n"
    "v_pk_mul_f32 v[146:147], v[146:147], v[230:231] op_sel_hi:[1,0]\n"
    "v_pk_mul_f32 v[148:149], v[148:149], v[230:231] op_sel_hi:[1,0]\n"
    "v_pk_mul_f32 v[150:151], v[150:151], v[230:231] op_sel_hi:[1,0]\n"
    "v_pk_mul_f32 v[152:153], v[152:153], v[230:231] op_sel_hi:[1,0]\n"
    "v_pk_mul_f32 v[154:155], v[154:155], v[230:231] op_sel_hi:[1,0]\n"
    "v_pk_mul_f32 v[156:157], v[156:157], v[230:231] op_sel_hi:[1,0]\n"
    "v_pk_mul_f32 v[158:159], v[158:159], v[230:231] op_sel_hi:[1,0]\n"
    "v_mfma_f32_16x16x32_bf16 v[172:175], a[128:131], a[16:19], v[172:175]\n"
    "v_exp_f32 v163, v163\n"
    "v_smfmac_f32_16x16x64_bf16 v[192:195], a[240:243], v[176:183], v237\n"
    "v_cvt_pk_bf16_f32 v185, v162, v163\n"
    "v_mfma_f32_16x16x32_bf16 v[0:3], a[208:211], v[180:183], 0\n"
    "v_exp_f32 v164, v164\n"
    "v_mfma_f32_16x16x32_bf16 v[0:3], a[144:147], v[176:179], v[0:3]\n"
    "v_mov_b32 v196, 0\n"
    "v_mov_b32 v197, 0\n"
    "v_mfma_f32_16x16x32_bf16 v[4:7], a[148:151], v[176:179], 0\n"
    "s_waitcnt lgkmcnt(4)\n"
    "v_exp_f32 v165, v165\n"
    "v_mfma_f32_16x16x32_bf16 v[4:7], a[212:215], v[180:183], v[4:7]\n"
    "v_cvt_pk_bf16_f32 v186, v164, v165\n"
    "v_cvt_pk_bf16_f32 v40, v40, v41\n"
    "v_mfma_f32_16x16x32_bf16 v[8:11], a[216:219], v[180:183], 0\n"
    "v_exp_f32 v166, v166\n"
    "v_mfma_f32_16x16x32_bf16 v[8:11], a[152:155], v[176:179], v[8:11]\n"
    "v_cvt_pk_bf16_f32 v41, v42, v43\n"
    "v_cvt_pk_bf16_f32 v42, v44, v45\n"
    "v_mfma_f32_16x16x32_bf16 v[12:15], a[156:159], v[176:179], 0\n"
    "s_waitcnt vmcnt(10) lgkmcnt(0)\n"
    "v_exp_f32 v167, v167\n"
    "v_mfma_f32_16x16x32_bf16 v[12:15], a[220:223], v[180:183], v[12:15]\n"
    "s_barrier\n"
    "v_mfma_f32_16x16x32_bf16 v[16:19], a[224:227], v[180:183], 0\n"
    "s_add_u32 m0, s63, 20480\n"
    "v_exp_f32 v168, v168\n"
    "v_mfma_f32_16x16x32_bf16 v[16:19], a[160:163], v[176:179], v[16:19]\n"
    "buffer_load_dwordx4 v224, s[44:47], s55 offen lds\n"
    "v_mfma_f32_16x16x32_bf16 v[20:23], a[164:167], v[176:179], 0\n"
    "s_add_u32 m0, s57, 0\n"
    "v_exp_f32 v169, v169\n"
    "v_mfma_f32_16x16x32_bf16 v[20:23], a[228:231], v[180:183], v[20:23]\n"
    "buffer_load_dwordx4 v229, s[36:39], s52 offen lds\n"
    "v_mfma_f32_16x16x32_bf16 v[24:27], a[232:235], v[180:183], 0\n"
    "v_exp_f32 v170, v170\n"
    "v_mfma_f32_16x16x32_bf16 v[24:27], a[168:171], v[176:179], v[24:27]\n"
    "v_cvt_pk_bf16_f32 v187, v166, v167\n"
    "v_cvt_pk_bf16_f32 v188, v168, v169\n"
    "v_mfma_f32_16x16x32_bf16 v[28:31], a[172:175], v[176:179], 0\n"
    "v_exp_f32 v171, v171\n"
    "v_mfma_f32_16x16x32_bf16 v[28:31], a[236:239], v[180:183], v[28:31]\n"
    "v_cvt_pk_bf16_f32 v189, v170, v171\n"
    "v_cvt_pk_bf16_f32 v43, v46, v47\n"
    "v_mfma_f32_16x16x32_bf16 v[160:163], a[80:83], a[32:35], -4.0\n"
    "ds_write_b128 v221, v[40:43] offset:8192\n"
    "v_exp_f32 v172, v172\n"
    "v_mfma_f32_16x16x32_bf16 v[160:163], a[84:87], a[36:39], v[160:163]\n"
    "v_cvt_pk_bf16_f32 v48, v48, v49\n"
    "v_cvt_pk_bf16_f32 v49, v50, v51\n"
    "v_mfma_f32_16x16x32_bf16 v[160:163], a[88:91], a[40:43], v[160:163]\n"
    "s_add_u32 m0, s57, 0\n"
    "v_exp_f32 v173, v173\n"
    "v_mfma_f32_16x16x32_bf16 v[160:163], a[92:95], a[44:47], v[160:163]\n"
    "buffer_load_dwordx4 v229, s[36:39], s52 offen offset:1024 lds\n"
    "v_mfma_f32_16x16x32_bf16 v[164:167], a[108:111], a[44:47], -4.0\n"
    "v_exp_f32 v174, v174\n"
    "v_mfma_f32_16x16x32_bf16 v[164:167], a[104:107], a[40:43], v[164:167]\n"
    "v_cvt_pk_bf16_f32 v190, v172, v173\n"
    "v_cvt_pk_bf16_f32 v50, v52, v53\n"
    "v_mfma_f32_16x16x32_bf16 v[164:167], a[100:103], a[36:39], v[164:167]\n"
    "v_exp_f32 v175, v175\n"
    "v_mfma_f32_16x16x32_bf16 v[164:167], a[96:99], a[32:35], v[164:167]\n"
    "v_cvt_pk_bf16_f32 v191, v174, v175\n"
    "v_cvt_pk_bf16_f32 v51, v54, v55\n"
    "v_mfma_f32_16x16x32_bf16 v[168:171], a[112:115], a[32:35], -4.0\n"
    "ds_read_b128 a[248:251], v225 offset:5120\n"
    "v_cvt_pk_bf16_f32 v56, v56, v57\n"
    "v_mfma_f32_16x16x32_bf16 v[168:171], a[116:119], a[36:39], v[168:171]\n"
    "ds_write_b128 v222, v[48:51] offset:8192\n"
    "v_exp_f32 v160, v160\n"
    "v_mfma_f32_16x16x32_bf16 v[168:171], a[120:123], a[40:43], v[168:171]\n"
    "v_cvt_pk_bf16_f32 v57, v58, v59\n"
    "v_cvt_pk_bf16_f32 v58, v60, v61\n"
    "v_mfma_f32_16x16x32_bf16 v[168:171], a[124:127], a[44:47], v[168:171]\n"
    "s_add_u32 m0, s57, 32\n"
    "v_exp_f32 v161, v161\n"
    "v_mfma_f32_16x16x32_bf16 v[172:175], a[140:143], a[44:47], -4.0\n"
    "buffer_load_dwordx4 v229, s[36:39], s52 offen offset:2048 lds\n"
    "v_mfma_f32_16x16x32_bf16 v[172:175], a[136:139], a[40:43], v[172:175]\n"
    "v_exp_f32 v162, v162\n"
    "v_mfma_f32_16x16x32_bf16 v[172:175], a[132:135], a[36:39], v[172:175]\n"
    "v_cvt_pk_bf16_f32 v59, v62, v63\n"
    "v_cvt_pk_bf16_f32 v176, v160, v161\n"
    "v_mfma_f32_16x16x32_bf16 v[172:175], a[128:131], a[32:35], v[172:175]\n"
    "ds_write_b128 v223, v[56:59] offset:8192\n"
    "v_exp_f32 v163, v163\n"
    "v_smfmac_f32_16x16x64_bf16 v[196:199], a[240:243], v[184:191], v237\n"
    "ds_read_b64_tr_b16 a[176:177], v216 offset:41536\n"
    "v_cvt_pk_bf16_f32 v177, v162, v163\n"
    "v_mfma_f32_16x16x32_bf16 v[32:35], a[208:211], v[188:191], 0\n"
    "s_waitcnt lgkmcnt(2)\n"
    "buffer_store_dwordx4 a[248:251], v228, s[48:51], s56 offen offset:1024 nt\n"
    "v_exp_f32 v164, v164\n"
    "v_mfma_f32_16x16x32_bf16 v[32:35], a[144:147], v[184:187], v[32:35]\n"
    "ds_read_b64_tr_b16 a[178:179], v216 offset:45664\n"
    "ds_read_b64_tr_b16 a[180:181], v216 offset:41544\n"
    "v_mfma_f32_16x16x32_bf16 v[36:39], a[148:151], v[184:187], 0\n"
    "s_add_u32 m0, s57, 32\n"
    "v_exp_f32 v165, v165\n"
    "v_mfma_f32_16x16x32_bf16 v[36:39], a[212:215], v[188:191], v[36:39]\n"
    "buffer_load_dwordx4 v229, s[36:39], s52 offen offset:3072 lds\n"
    "v_mfma_f32_16x16x32_bf16 v[40:43], a[216:219], v[188:191], 0\n"
    "v_exp_f32 v166, v166\n"
    "v_mfma_f32_16x16x32_bf16 v[40:43], a[152:155], v[184:187], v[40:43]\n"
    "ds_read_b64_tr_b16 a[182:183], v216 offset:45672\n"
    "v_cvt_pk_bf16_f32 v178, v164, v165\n"
    "v_mfma_f32_16x16x32_bf16 v[44:47], a[156:159], v[184:187], 0\n"
    "v_exp_f32 v167, v167\n"
    "v_mfma_f32_16x16x32_bf16 v[44:47], a[220:223], v[188:191], v[44:47]\n"
    "ds_read_b64_tr_b16 a[184:185], v217 offset:41536\n"
    "v_cvt_pk_bf16_f32 v179, v166, v167\n"
    "v_mfma_f32_16x16x32_bf16 v[48:51], a[224:227], v[188:191], 0\n"
    "v_exp_f32 v168, v168\n"
    "v_mfma_f32_16x16x32_bf16 v[48:51], a[160:163], v[184:187], v[48:51]\n"
    "ds_read_b64_tr_b16 a[186:187], v217 offset:45664\n"
    "v_mov_b32 v200, 0\n"
    "v_mfma_f32_16x16x32_bf16 v[52:55], a[164:167], v[184:187], 0\n"
    "v_exp_f32 v169, v169\n"
    "v_mfma_f32_16x16x32_bf16 v[52:55], a[228:231], v[188:191], v[52:55]\n"
    "v_cvt_pk_bf16_f32 v180, v168, v169\n"
    "v_mov_b32 v201, 0\n"
    "v_mfma_f32_16x16x32_bf16 v[56:59], a[232:235], v[188:191], 0\n"
    "s_add_u32 m0, s58, 16512\n"
    "v_exp_f32 v170, v170\n"
    "v_mfma_f32_16x16x32_bf16 v[56:59], a[168:171], v[184:187], v[56:59]\n"
    "buffer_load_dwordx4 v229, s[40:43], s54 offen lds\n"
    "v_mfma_f32_16x16x32_bf16 v[60:63], a[172:175], v[184:187], 0\n"
    "v_exp_f32 v171, v171\n"
    "v_mfma_f32_16x16x32_bf16 v[60:63], a[236:239], v[188:191], v[60:63]\n"
    "ds_read_b64_tr_b16 a[188:189], v217 offset:41544\n"
    "v_cvt_pk_bf16_f32 v181, v170, v171\n"
    "v_mfma_f32_16x16x32_bf16 v[160:163], a[80:83], a[48:51], -4.0\n"
    "v_exp_f32 v172, v172\n"
    "v_mfma_f32_16x16x32_bf16 v[160:163], a[84:87], a[52:55], v[160:163]\n"
    "ds_read_b64_tr_b16 a[190:191], v217 offset:45672\n"
    "ds_read_b64_tr_b16 a[192:193], v218 offset:41536\n"
    "v_mfma_f32_16x16x32_bf16 v[160:163], a[88:91], a[56:59], v[160:163]\n"
    "v_exp_f32 v173, v173\n"
    "v_mfma_f32_16x16x32_bf16 v[160:163], a[92:95], a[60:63], v[160:163]\n"
    "v_cvt_pk_bf16_f32 v182, v172, v173\n"
    "v_cvt_pk_bf16_f32 v72, v72, v73\n"
    "v_mfma_f32_16x16x32_bf16 v[164:167], a[108:111], a[60:63], -4.0\n"
    "s_add_u32 m0, s58, 16520\n"
    "v_exp_f32 v174, v174\n"
    "v_mfma_f32_16x16x32_bf16 v[164:167], a[104:107], a[56:59], v[164:167]\n"
    "buffer_load_dwordx4 v229, s[40:43], s54 offen offset:1024 lds\n"
    "v_mfma_f32_16x16x32_bf16 v[164:167], a[100:103], a[52:55], v[164:167]\n"
    "s_add_u32 m0, s63, 20480\n"
    "v_exp_f32 v175, v175\n"
    "v_mfma_f32_16x16x32_bf16 v[164:167], a[96:99], a[48:51], v[164:167]\n"
    "buffer_load_dwordx4 v225, s[44:47], s55 offen offset:1024 lds\n"
    "v_mfma_f32_16x16x32_bf16 v[168:171], a[112:115], a[48:51], -4.0\n"
    "v_cvt_pk_bf16_f32 v183, v174, v175\n"
    "v_cvt_pk_bf16_f32 v73, v74, v75\n"
    "v_mfma_f32_16x16x32_bf16 v[168:171], a[116:119], a[52:55], v[168:171]\n"
    "v_exp_f32 v160, v160\n"
    "v_mfma_f32_16x16x32_bf16 v[168:171], a[120:123], a[56:59], v[168:171]\n"
    "v_cvt_pk_bf16_f32 v74, v76, v77\n"
    "v_cvt_pk_bf16_f32 v75, v78, v79\n"
    "v_mfma_f32_16x16x32_bf16 v[168:171], a[124:127], a[60:63], v[168:171]\n"
    "ds_write_b128 v221, v[72:75] offset:12288\n"
    "v_exp_f32 v161, v161\n"
    "v_mfma_f32_16x16x32_bf16 v[172:175], a[140:143], a[60:63], -4.0\n"
    "ds_read_b64_tr_b16 a[194:195], v218 offset:45664\n"
    "ds_read_b64_tr_b16 a[196:197], v218 offset:41544\n"
    "v_mfma_f32_16x16x32_bf16 v[172:175], a[136:139], a[56:59], v[172:175]\n"
    "v_exp_f32 v162, v162\n"
    "v_mfma_f32_16x16x32_bf16 v[172:175], a[132:135], a[52:55], v[172:175]\n"
    "v_cvt_pk_bf16_f32 v80, v80, v81\n"
    "v_cvt_pk_bf16_f32 v81, v82, v83\n"
    "v_mfma_f32_16x16x32_bf16 v[172:175], a[128:131], a[48:51], v[172:175]\n"
    "v_exp_f32 v163, v163\n"
    "v_smfmac_f32_16x16x64_bf16 v[200:203], a[240:243], v[176:183], v237\n"
    "ds_read_b64_tr_b16 a[198:199], v218 offset:45672\n"
    "v_cvt_pk_bf16_f32 v82, v84, v85\n"
    "v_mfma_f32_16x16x32_bf16 v[64:67], a[208:211], v[180:183], 0\n"
    "v_exp_f32 v164, v164\n"
    "v_mfma_f32_16x16x32_bf16 v[64:67], a[144:147], v[176:179], v[64:67]\n"
    "ds_read_b64_tr_b16 a[200:201], v219 offset:41536\n"
    "v_cvt_pk_bf16_f32 v83, v86, v87\n"
    "v_mfma_f32_16x16x32_bf16 v[68:71], a[148:151], v[176:179], 0\n"
    "ds_write_b128 v222, v[80:83] offset:12288\n"
    "v_exp_f32 v165, v165\n"
    "v_mfma_f32_16x16x32_bf16 v[68:71], a[212:215], v[180:183], v[68:71]\n"
    "v_cvt_pk_bf16_f32 v88, v88, v89\n"
    "v_cvt_pk_bf16_f32 v89, v90, v91\n"
    "v_mfma_f32_16x16x32_bf16 v[72:75], a[216:219], v[180:183], 0\n"
    "v_exp_f32 v166, v166\n"
    "v_mfma_f32_16x16x32_bf16 v[72:75], a[152:155], v[176:179], v[72:75]\n"
    "v_cvt_pk_bf16_f32 v90, v92, v93\n"
    "v_cvt_pk_bf16_f32 v91, v94, v95\n"
    "v_mfma_f32_16x16x32_bf16 v[76:79], a[156:159], v[176:179], 0\n"
    "ds_write_b128 v223, v[88:91] offset:12288\n"
    "v_exp_f32 v167, v167\n"
    "v_mfma_f32_16x16x32_bf16 v[76:79], a[220:223], v[180:183], v[76:79]\n"
    "ds_read_b64_tr_b16 a[202:203], v219 offset:45664\n"
    "ds_read_b64_tr_b16 a[204:205], v219 offset:41544\n"
    "v_mfma_f32_16x16x32_bf16 v[80:83], a[224:227], v[180:183], 0\n"
    "v_exp_f32 v168, v168\n"
    "v_mfma_f32_16x16x32_bf16 v[80:83], a[160:163], v[176:179], v[80:83]\n"
    "ds_read_b64_tr_b16 a[206:207], v219 offset:45672\n"
    "v_cvt_pk_bf16_f32 v184, v160, v161\n"
    "v_mfma_f32_16x16x32_bf16 v[84:87], a[164:167], v[176:179], 0\n"
    "v_exp_f32 v169, v169\n"
    "v_mfma_f32_16x16x32_bf16 v[84:87], a[228:231], v[180:183], v[84:87]\n"
    "v_cvt_pk_bf16_f32 v185, v162, v163\n"
    "v_cvt_pk_bf16_f32 v186, v164, v165\n"
    "v_mfma_f32_16x16x32_bf16 v[88:91], a[232:235], v[180:183], 0\n"
    "v_exp_f32 v170, v170\n"
    "v_mfma_f32_16x16x32_bf16 v[88:91], a[168:171], v[176:179], v[88:91]\n"
    "v_cvt_pk_bf16_f32 v187, v166, v167\n"
    "v_cvt_pk_bf16_f32 v188, v168, v169\n"
    "v_mfma_f32_16x16x32_bf16 v[92:95], a[172:175], v[176:179], 0\n"
    "v_exp_f32 v171, v171\n"
    "v_mfma_f32_16x16x32_bf16 v[92:95], a[236:239], v[180:183], v[92:95]\n"
    "v_cvt_pk_bf16_f32 v189, v170, v171\n"
    "v_mov_b32 v204, 0\n"
    "v_mfma_f32_16x16x32_bf16 v[160:163], a[80:83], a[64:67], -4.0\n"
    "v_exp_f32 v172, v172\n"
    "v_mfma_f32_16x16x32_bf16 v[160:163], a[84:87], a[68:71], v[160:163]\n"
    "ds_read_b128 a[252:255], v226 offset:6144\n"
    "v_mov_b32 v205, 0\n"
    "v_mfma_f32_16x16x32_bf16 v[160:163], a[88:91], a[72:75], v[160:163]\n"
    "v_exp_f32 v173, v173\n"
    "v_mfma_f32_16x16x32_bf16 v[160:163], a[92:95], a[76:79], v[160:163]\n"
    "v_cvt_pk_bf16_f32 v190, v172, v173\n"
    "v_cvt_pk_bf16_f32 v104, v104, v105\n"
    "v_mfma_f32_16x16x32_bf16 v[164:167], a[108:111], a[76:79], -4.0\n"
    "v_exp_f32 v174, v174\n"
    "v_mfma_f32_16x16x32_bf16 v[164:167], a[104:107], a[72:75], v[164:167]\n"
    "v_cvt_pk_bf16_f32 v105, v106, v107\n"
    "v_cvt_pk_bf16_f32 v106, v108, v109\n"
    "v_mfma_f32_16x16x32_bf16 v[164:167], a[100:103], a[68:71], v[164:167]\n"
    "v_exp_f32 v175, v175\n"
    "v_mfma_f32_16x16x32_bf16 v[164:167], a[96:99], a[64:67], v[164:167]\n"
    "s_waitcnt vmcnt(13) lgkmcnt(1)\n"
    "v_cvt_pk_bf16_f32 v191, v174, v175\n"
    "v_cvt_pk_bf16_f32 v107, v110, v111\n"
    "v_mfma_f32_16x16x32_bf16 v[168:171], a[112:115], a[64:67], -4.0\n"
    "s_barrier\n"
    "v_mfma_f32_16x16x32_bf16 v[168:171], a[116:119], a[68:71], v[168:171]\n"
    "ds_write_b128 v221, v[104:107] offset:16384\n"
    "v_exp_f32 v160, v160\n"
    "v_mfma_f32_16x16x32_bf16 v[168:171], a[120:123], a[72:75], v[168:171]\n"
    "v_cvt_pk_bf16_f32 v112, v112, v113\n"
    "v_cvt_pk_bf16_f32 v113, v114, v115\n"
    "v_mfma_f32_16x16x32_bf16 v[168:171], a[124:127], a[76:79], v[168:171]\n"
    "s_waitcnt lgkmcnt(1)\n"
    "buffer_store_dwordx4 a[252:255], v228, s[48:51], s56 offen offset:2048 nt\n"
    "v_exp_f32 v161, v161\n"
    "v_mfma_f32_16x16x32_bf16 v[172:175], a[140:143], a[76:79], -4.0\n"
    "v_cvt_pk_bf16_f32 v114, v116, v117\n"
    "v_cvt_pk_bf16_f32 v115, v118, v119\n"
    "v_mfma_f32_16x16x32_bf16 v[172:175], a[136:139], a[72:75], v[172:175]\n"
    "ds_write_b128 v222, v[112:115] offset:16384\n"
    "v_exp_f32 v162, v162\n"
    "v_mfma_f32_16x16x32_bf16 v[172:175], a[132:135], a[68:71], v[172:175]\n"
    "ds_read_b128 a[80:83], v212 offset:16640\n"
    "ds_read_b128 a[84:87], v213 offset:16640\n"
    "v_mfma_f32_16x16x32_bf16 v[172:175], a[128:131], a[64:67], v[172:175]\n"
    "v_exp_f32 v163, v163\n"
    "v_smfmac_f32_16x16x64_bf16 v[204:207], a[240:243], v[184:191], v237\n"
    "ds_read_b128 a[88:91], v214 offset:16640\n"
    "v_cvt_pk_bf16_f32 v120, v120, v121\n"
    "v_mfma_f32_16x16x32_bf16 v[96:99], a[208:211], v[188:191], 0\n"
    "v_exp_f32 v164, v164\n"
    "v_mfma_f32_16x16x32_bf16 v[96:99], a[144:147], v[184:187], v[96:99]\n"
    "v_cvt_pk_bf16_f32 v121, v122, v123\n"
    "v_cvt_pk_bf16_f32 v122, v124, v125\n"
    "v_mfma_f32_16x16x32_bf16 v[100:103], a[148:151], v[184:187], 0\n"
    "v_exp_f32 v165, v165\n"
    "v_mfma_f32_16x16x32_bf16 v[100:103], a[212:215], v[188:191], v[100:103]\n"
    "ds_read_b128 a[92:95], v215 offset:16640\n"
    "v_cvt_pk_bf16_f32 v123, v126, v127\n"
    "v_mfma_f32_16x16x32_bf16 v[104:107], a[216:219], v[188:191], 0\n"
    "ds_write_b128 v223, v[120:123] offset:16384\n"
    "v_exp_f32 v166, v166\n"
    "v_mfma_f32_16x16x32_bf16 v[104:107], a[152:155], v[184:187], v[104:107]\n"
    "ds_read_b128 a[108:111], v215 offset:20800\n"
    "ds_read_b128 a[104:107], v214 offset:20800\n"
    "v_mfma_f32_16x16x32_bf16 v[108:111], a[156:159], v[184:187], 0\n"
    "s_add_u32 m0, s63, 20480\n"
    "v_exp_f32 v167, v167\n"
    "v_mfma_f32_16x16x32_bf16 v[108:111], a[220:223], v[188:191], v[108:111]\n"
    "buffer_load_dwordx4 v226, s[44:47], s55 offen offset:2048 lds\n"
    "v_mfma_f32_16x16x32_bf16 v[112:115], a[224:227], v[188:191], 0\n"
    "s_add_u32 m0, s58, 8256\n"
    "v_exp_f32 v168, v168\n"
    "v_mfma_f32_16x16x32_bf16 v[112:115], a[160:163], v[184:187], v[112:115]\n"
    "buffer_load_dwordx4 v229, s[40:43], s53 offen lds\n"
    "v_mfma_f32_16x16x32_bf16 v[116:119], a[164:167], v[184:187], 0\n"
    "v_exp_f32 v169, v169\n"
    "v_mfma_f32_16x16x32_bf16 v[116:119], a[228:231], v[188:191], v[116:119]\n"
    "ds_read_b128 a[100:103], v213 offset:20800\n"
    "ds_read_b128 a[96:99], v212 offset:20800\n"
    "v_mfma_f32_16x16x32_bf16 v[120:123], a[232:235], v[188:191], 0\n"
    "v_exp_f32 v170, v170\n"
    "v_mfma_f32_16x16x32_bf16 v[120:123], a[168:171], v[184:187], v[120:123]\n"
    "v_cvt_pk_bf16_f32 v176, v160, v161\n"
    "v_cvt_pk_bf16_f32 v177, v162, v163\n"
    "v_mfma_f32_16x16x32_bf16 v[124:127], a[172:175], v[184:187], 0\n"
    "s_add_u32 m0, s58, 8264\n"
    "v_exp_f32 v171, v171\n"
    "v_mfma_f32_16x16x32_bf16 v[124:127], a[236:239], v[188:191], v[124:127]\n"
    "s_waitcnt lgkmcnt(2)\n"
    "ds_read_b128 a[112:115], v212 offset:24960\n"
    "buffer_load_dwordx4 v229, s[40:43], s53 offen offset:1024 lds\n"
    "v_mfma_f32_16x16x32_bf16 v[160:163], a[80:83], a[0:3], -4.0\n"
    "ds_read_b128 a[116:119], v213 offset:24960\n"
    "v_exp_f32 v172, v172\n"
    "v_mfma_f32_16x16x32_bf16 v[160:163], a[84:87], a[4:7], v[160:163]\n"
    "ds_read_b128 a[120:123], v214 offset:24960\n"
    "ds_read_b128 a[124:127], v215 offset:24960\n"
    "v_mfma_f32_16x16x32_bf16 v[160:163], a[88:91], a[8:11], v[160:163]\n"
    "v_exp_f32 v173, v173\n"
    "v_mfma_f32_16x16x32_bf16 v[160:163], a[92:95], a[12:15], v[160:163]\n"
    "ds_read_b128 a[140:143], v215 offset:29120\n"
    "v_cvt_pk_bf16_f32 v178, v164, v165\n"
    "v_cvt_pk_bf16_f32 v179, v166, v167\n"
    "v_mfma_f32_16x16x32_bf16 v[164:167], a[108:111], a[12:15], -4.0\n"
    "ds_read_b128 a[136:139], v214 offset:29120\n"
    "v_exp_f32 v174, v174\n"
    "v_mfma_f32_16x16x32_bf16 v[164:167], a[104:107], a[8:11], v[164:167]\n"
    "s_waitcnt lgkmcnt(6)\n"
    "ds_read_b128 a[132:135], v213 offset:29120\n"
    "ds_read_b128 a[128:131], v212 offset:29120\n"
    "v_mfma_f32_16x16x32_bf16 v[164:167], a[100:103], a[4:7], v[164:167]\n"
    "v_exp_f32 v175, v175\n"
    "v_mfma_f32_16x16x32_bf16 v[164:167], a[96:99], a[0:3], v[164:167]\n"
    "s_waitcnt lgkmcnt(7)\n"
    "v_cvt_pk_bf16_f32 v180, v168, v169\n"
    "v_cvt_pk_bf16_f32 v181, v170, v171\n"
    "v_mfma_f32_16x16x32_bf16 v[168:171], a[112:115], a[0:3], -4.0\n"
    "s_waitcnt lgkmcnt(6)\n"
    "v_cvt_pk_bf16_f32 v182, v172, v173\n"
    "v_cvt_pk_bf16_f32 v183, v174, v175\n"
    "v_mfma_f32_16x16x32_bf16 v[168:171], a[116:119], a[4:7], v[168:171]\n"
    "s_waitcnt lgkmcnt(4)\n"
    "v_exp_f32 v160, v160\n"
    "v_mfma_f32_16x16x32_bf16 v[168:171], a[120:123], a[8:11], v[168:171]\n"
    "v_mov_b32 v208, 0\n"
    "v_mov_b32 v209, 0\n"
    "v_mfma_f32_16x16x32_bf16 v[168:171], a[124:127], a[12:15], v[168:171]\n"
    "s_waitcnt lgkmcnt(3)\n"
    "v_exp_f32 v161, v161\n"
    "v_mfma_f32_16x16x32_bf16 v[172:175], a[140:143], a[12:15], -4.0\n"
    "s_waitcnt lgkmcnt(2)\n"
    "v_cvt_pk_bf16_f32 v128, v128, v129\n"
    "v_cvt_pk_bf16_f32 v129, v130, v131\n"
    "v_mfma_f32_16x16x32_bf16 v[172:175], a[136:139], a[8:11], v[172:175]\n"
    "s_waitcnt lgkmcnt(0)\n"
    "v_exp_f32 v162, v162\n"
    "v_mfma_f32_16x16x32_bf16 v[172:175], a[132:135], a[4:7], v[172:175]\n"
    "v_cvt_pk_bf16_f32 v130, v132, v133\n"
    "v_cvt_pk_bf16_f32 v131, v134, v135\n"
    "v_mfma_f32_16x16x32_bf16 v[172:175], a[128:131], a[0:3], v[172:175]\n"
    "ds_write_b128 v220, v[128:131] offset:0\n"
    "v_exp_f32 v163, v163\n"
    "v_smfmac_f32_16x16x64_bf16 v[208:211], a[240:243], v[176:183], v237\n"
    "v_cvt_pk_bf16_f32 v136, v136, v137\n"
    "v_mfma_f32_16x16x32_bf16 v[128:131], a[208:211], v[180:183], 0\n"
    "v_exp_f32 v164, v164\n"
    "v_mfma_f32_16x16x32_bf16 v[128:131], a[144:147], v[176:179], v[128:131]\n"
    "v_cvt_pk_bf16_f32 v137, v138, v139\n"
    "v_cvt_pk_bf16_f32 v138, v140, v141\n"
    "v_cvt_pk_bf16_f32 v139, v142, v143\n"
    "v_mfma_f32_16x16x32_bf16 v[132:135], a[148:151], v[176:179], 0\n"
    "ds_write_b128 v221, v[136:139] offset:0\n"
    "v_exp_f32 v165, v165\n"
    "v_mfma_f32_16x16x32_bf16 v[132:135], a[212:215], v[180:183], v[132:135]\n"
    "v_cvt_pk_bf16_f32 v144, v144, v145\n"
    "v_cvt_pk_bf16_f32 v145, v146, v147\n"
    "v_mfma_f32_16x16x32_bf16 v[136:139], a[216:219], v[180:183], 0\n"
    "v_exp_f32 v166, v166\n"
    "v_mfma_f32_16x16x32_bf16 v[136:139], a[152:155], v[176:179], v[136:139]\n"
    "v_cvt_pk_bf16_f32 v146, v148, v149\n"
    "v_cvt_pk_bf16_f32 v147, v150, v151\n"
    "v_mfma_f32_16x16x32_bf16 v[140:143], a[156:159], v[176:179], 0\n"
    "ds_write_b128 v222, v[144:147] offset:0\n"
    "v_exp_f32 v167, v167\n"
    "v_mfma_f32_16x16x32_bf16 v[140:143], a[220:223], v[180:183], v[140:143]\n"
    "v_cvt_pk_bf16_f32 v152, v152, v153\n"
    "v_cvt_pk_bf16_f32 v153, v154, v155\n"
    "v_mfma_f32_16x16x32_bf16 v[144:147], a[224:227], v[180:183], 0\n"
    "v_exp_f32 v168, v168\n"
    "v_mfma_f32_16x16x32_bf16 v[144:147], a[160:163], v[176:179], v[144:147]\n"
    "v_cvt_pk_bf16_f32 v154, v156, v157\n"
    "v_cvt_pk_bf16_f32 v155, v158, v159\n"
    "v_mfma_f32_16x16x32_bf16 v[148:151], a[164:167], v[176:179], 0\n"
    "ds_write_b128 v223, v[152:155] offset:0\n"
    "v_exp_f32 v169, v169\n"
    "v_mfma_f32_16x16x32_bf16 v[148:151], a[228:231], v[180:183], v[148:151]\n"
    "v_cvt_pk_bf16_f32 v184, v160, v161\n"
    "v_cvt_pk_bf16_f32 v185, v162, v163\n"
    "v_mfma_f32_16x16x32_bf16 v[152:155], a[232:235], v[180:183], 0\n"
    "v_exp_f32 v170, v170\n"
    "v_mfma_f32_16x16x32_bf16 v[152:155], a[168:171], v[176:179], v[152:155]\n"
    "ds_read_b64_tr_b16 a[208:209], v216 offset:58048\n"
    "ds_read_b64_tr_b16 a[210:211], v216 offset:62176\n"
    "v_mfma_f32_16x16x32_bf16 v[156:159], a[172:175], v[176:179], 0\n"
    "v_exp_f32 v171, v171\n"
    "v_mfma_f32_16x16x32_bf16 v[156:159], a[236:239], v[180:183], v[156:159]\n"
    "ds_read_b64_tr_b16 a[212:213], v216 offset:58056\n"
    "ds_read_b64_tr_b16 a[214:215], v216 offset:62184\n"
    "s_branch LBODY%=\n"
    "LTAIL%=:\n"
    "v_mfma_f32_16x16x32_bf16 v[160:163], a[80:83], a[16:19], -4.0\n"
    "v_exp_f32 v172, v172\n"
    "v_mfma_f32_16x16x32_bf16 v[160:163], a[84:87], a[20:23], v[160:163]\n"
    "ds_read_b64_tr_b16 a[224:225], v218 offset:49792\n"
    "v_cvt_pk_bf16_f32 v181, v170, v171\n"
    "v_mfma_f32_16x16x32_bf16 v[160:163], a[88:91], a[24:27], v[160:163]\n"
    "v_exp_f32 v173, v173\n"
    "v_mfma_f32_16x16x32_bf16 v[160:163], a[92:95], a[28:31], v[160:163]\n"
    "ds_read_b64_tr_b16 a[226:227], v218 offset:53920\n"
    "ds_read_b64_tr_b16 a[228:229], v218 offset:49800\n"
    "v_mfma_f32_16x16x32_bf16 v[164:167], a[108:111], a[28:31], -4.0\n"
    "v_exp_f32 v174, v174\n"
    "v_mfma_f32_16x16x32_bf16 v[164:167], a[104:107], a[24:27], v[164:167]\n"
    "ds_read_b64_tr_b16 a[230:231], v218 offset:53928\n"
    "v_cvt_pk_bf16_f32 v182, v172, v173\n"
    "v_mfma_f32_16x16x32_bf16 v[164:167], a[100:103], a[20:23], v[164:167]\n"
    "v_exp_f32 v175, v175\n"
    "v_mfma_f32_16x16x32_bf16 v[164:167], a[96:99], a[16:19], v[164:167]\n"
    "ds_read_b64_tr_b16 a[232:233], v219 offset:49792\n"
    "v_cvt_pk_bf16_f32 v183, v174, v175\n"
    "v_mfma_f32_16x16x32_bf16 v[168:171], a[112:115], a[16:19], -4.0\n"
    "s_add_u32 s52, s52, 16384\n"
    "ds_read_b64_tr_b16 a[234:235], v219 offset:53920\n"
    "ds_read_b64_tr_b16 a[236:237], v219 offset:49800\n"
    "v_mfma_f32_16x16x32_bf16 v[168:171], a[116:119], a[20:23], v[168:171]\n"
    "v_exp_f32 v160, v160\n"
    "v_mfma_f32_16x16x32_bf16 v[168:171], a[120:123], a[24:27], v[168:171]\n"
    "s_add_u32 s54, s54, 16384\n"
    "ds_read_b64_tr_b16 a[238:239], v219 offset:53928\n"
    "v_mfma_f32_16x16x32_bf16 v[168:171], a[124:127], a[28:31], v[168:171]\n"
    "v_exp_f32 v161, v161\n"
    "v_mfma_f32_16x16x32_bf16 v[172:175], a[140:143], a[28:31], -4.0\n"
    "s_add_u32 s53, s53, 16384\n"
    "v_mfma_f32_16x16x32_bf16 v[172:175], a[136:139], a[24:27], v[172:175]\n"
    "v_exp_f32 v162, v162\n"
    "v_mfma_f32_16x16x32_bf16 v[172:175], a[132:135], a[20:23], v[172:175]\n"
    "v_mfma_f32_16x16x32_bf16 v[172:175], a[128:131], a[16:19], v[172:175]\n"
    "v_exp_f32 v163, v163\n"
    "v_smfmac_f32_16x16x64_bf16 v[192:195], a[240:243], v[176:183], v237\n"
    "s_waitcnt lgkmcnt(1)\n"
    "v_mfma_f32_16x16x32_bf16 v[0:3], a[208:211], v[180:183], v[0:3]\n"
    "v_exp_f32 v164, v164\n"
    "v_mfma_f32_16x16x32_bf16 v[0:3], a[144:147], v[176:179], v[0:3]\n"
    "v_mfma_f32_16x16x32_bf16 v[4:7], a[148:151], v[176:179], v[4:7]\n"
    "v_exp_f32 v165, v165\n"
    "v_mfma_f32_16x16x32_bf16 v[4:7], a[212:215], v[180:183], v[4:7]\n"
    "v_cvt_pk_bf16_f32 v184, v160, v161\n"
    "v_cvt_pk_bf16_f32 v185, v162, v163\n"
    "v_mfma_f32_16x16x32_bf16 v[8:11], a[216:219], v[180:183], v[8:11]\n"
    "v_exp_f32 v166, v166\n"
    "v_mfma_f32_16x16x32_bf16 v[8:11], a[152:155], v[176:179], v[8:11]\n"
    "v_mfma_f32_16x16x32_bf16 v[12:15], a[156:159], v[176:179], v[12:15]\n"
    "s_waitcnt lgkmcnt(0)\n"
    "v_exp_f32 v167, v167\n"
    "v_mfma_f32_16x16x32_bf16 v[12:15], a[220:223], v[180:183], v[12:15]\n"
    "s_barrier\n"
    "v_mfma_f32_16x16x32_bf16 v[16:19], a[224:227], v[180:183], v[16:19]\n"
    "s_add_u32 m0, s57, 0\n"
    "v_exp_f32 v168, v168\n"
    "v_mfma_f32_16x16x32_bf16 v[16:19], a[160:163], v[176:179], v[16:19]\n"
    "buffer_load_dwordx4 v229, s[36:39], s52 offen lds\n"
    "v_mfma_f32_16x16x32_bf16 v[20:23], a[164:167], v[176:179], v[20:23]\n"
    "v_exp_f32 v169, v169\n"
    "v_mfma_f32_16x16x32_bf16 v[20:23], a[228:231], v[180:183], v[20:23]\n"
    "v_cvt_pk_bf16_f32 v186, v164, v165\n"
    "v_cvt_pk_bf16_f32 v187, v166, v167\n"
    "v_mfma_f32_16x16x32_bf16 v[24:27], a[232:235], v[180:183], v[24:27]\n"
    "v_exp_f32 v170, v170\n"
    "v_mfma_f32_16x16x32_bf16 v[24:27], a[168:171], v[176:179], v[24:27]\n"
    "v_cvt_pk_bf16_f32 v188, v168, v169\n"
    "v_mfma_f32_16x16x32_bf16 v[28:31], a[172:175], v[176:179], v[28:31]\n"
    "v_exp_f32 v171, v171\n"
    "v_mfma_f32_16x16x32_bf16 v[28:31], a[236:239], v[180:183], v[28:31]\n"
    "v_cvt_pk_bf16_f32 v189, v170, v171\n"
    "v_mfma_f32_16x16x32_bf16 v[160:163], a[80:83], a[32:35], -4.0\n"
    "v_exp_f32 v172, v172\n"
    "v_mfma_f32_16x16x32_bf16 v[160:163], a[84:87], a[36:39], v[160:163]\n"
    "v_mfma_f32_16x16x32_bf16 v[160:163], a[88:91], a[40:43], v[160:163]\n"
    "s_add_u32 m0, s57, 0\n"
    "v_exp_f32 v173, v173\n"
    "v_mfma_f32_16x16x32_bf16 v[160:163], a[92:95], a[44:47], v[160:163]\n"
    "buffer_load_dwordx4 v229, s[36:39], s52 offen offset:1024 lds\n"
    "v_mfma_f32_16x16x32_bf16 v[164:167], a[108:111], a[44:47], -4.0\n"
    "v_exp_f32 v174, v174\n"
    "v_mfma_f32_16x16x32_bf16 v[164:167], a[104:107], a[40:43], v[164:167]\n"
    "v_cvt_pk_bf16_f32 v190, v172, v173\n"
    "v_mfma_f32_16x16x32_bf16 v[164:167], a[100:103], a[36:39], v[164:167]\n"
    "v_exp_f32 v175, v175\n"
    "v_mfma_f32_16x16x32_bf16 v[164:167], a[96:99], a[32:35], v[164:167]\n"
    "v_mfma_f32_16x16x32_bf16 v[168:171], a[112:115], a[32:35], -4.0\n"
    "v_cvt_pk_bf16_f32 v191, v174, v175\n"
    "v_mfma_f32_16x16x32_bf16 v[168:171], a[116:119], a[36:39], v[168:171]\n"
    "v_exp_f32 v160, v160\n"
    "v_mfma_f32_16x16x32_bf16 v[168:171], a[120:123], a[40:43], v[168:171]\n"
    "v_mfma_f32_16x16x32_bf16 v[168:171], a[124:127], a[44:47], v[168:171]\n"
    "s_add_u32 m0, s57, 32\n"
    "v_exp_f32 v161, v161\n"
    "v_mfma_f32_16x16x32_bf16 v[172:175], a[140:143], a[44:47], -4.0\n"
    "buffer_load_dwordx4 v229, s[36:39], s52 offen offset:2048 lds\n"
    "v_mfma_f32_16x16x32_bf16 v[172:175], a[136:139], a[40:43], v[172:175]\n"
    "v_exp_f32 v162, v162\n"
    "v_mfma_f32_16x16x32_bf16 v[172:175], a[132:135], a[36:39], v[172:175]\n"
    "v_mfma_f32_16x16x32_bf16 v[172:175], a[128:131], a[32:35], v[172:175]\n"
    "v_exp_f32 v163, v163\n"
    "v_smfmac_f32_16x16x64_bf16 v[196:199], a[240:243], v[184:191], v237\n"
    "ds_read_b128 a[248:251], v227 offset:7168\n"
    "v_mfma_f32_16x16x32_bf16 v[32:35], a[208:211], v[188:191], v[32:35]\n"
    "v_exp_f32 v164, v164\n"
    "v_mfma_f32_16x16x32_bf16 v[32:35], a[144:147], v[184:187], v[32:35]\n"
    "v_cvt_pk_bf16_f32 v176, v160, v161\n"
    "v_mfma_f32_16x16x32_bf16 v[36:39], a[148:151], v[184:187], v[36:39]\n"
    "s_add_u32 m0, s57, 32\n"
    "v_exp_f32 v165, v165\n"
    "v_mfma_f32_16x16x32_bf16 v[36:39], a[212:215], v[188:191], v[36:39]\n"
    "buffer_load_dwordx4 v229, s[36:39], s52 offen offset:3072 lds\n"
    "v_mfma_f32_16x16x32_bf16 v[40:43], a[216:219], v[188:191], v[40:43]\n"
    "v_exp_f32 v166, v166\n"
    "v_mfma_f32_16x16x32_bf16 v[40:43], a[152:155], v[184:187], v[40:43]\n"
    "ds_read_b64_tr_b16 a[176:177], v216 offset:41536\n"
    "v_cvt_pk_bf16_f32 v177, v162, v163\n"
    "v_mfma_f32_16x16x32_bf16 v[44:47], a[156:159], v[184:187], v[44:47]\n"
    "v_exp_f32 v167, v167\n"
    "v_mfma_f32_16x16x32_bf16 v[44:47], a[220:223], v[188:191], v[44:47]\n"
    "s_add_u32 s56, s64, 0\n"
    "v_cvt_pk_bf16_f32 v178, v164, v165\n"
    "v_mfma_f32_16x16x32_bf16 v[48:51], a[224:227], v[188:191], v[48:51]\n"
    "s_waitcnt lgkmcnt(1)\n"
    "buffer_store_dwordx4 a[248:251], v228, s[48:51], s56 offen offset:3072 nt\n"
    "v_exp_f32 v168, v168\n"
    "v_mfma_f32_16x16x32_bf16 v[48:51], a[160:163], v[184:187], v[48:51]\n"
    "ds_read_b64_tr_b16 a[178:179], v216 offset:45664\n"
    "v_cvt_pk_bf16_f32 v179, v166, v167\n"
    "v_mfma_f32_16x16x32_bf16 v[52:55], a[164:167], v[184:187], v[52:55]\n"
    "v_exp_f32 v169, v169\n"
    "v_mfma_f32_16x16x32_bf16 v[52:55], a[228:231], v[188:191], v[52:55]\n"
    "ds_read_b64_tr_b16 a[180:181], v216 offset:41544\n"
    "v_cvt_pk_bf16_f32 v180, v168, v169\n"
    "v_mfma_f32_16x16x32_bf16 v[56:59], a[232:235], v[188:191], v[56:59]\n"
    "s_add_u32 m0, s58, 16512\n"
    "v_exp_f32 v170, v170\n"
    "v_mfma_f32_16x16x32_bf16 v[56:59], a[168:171], v[184:187], v[56:59]\n"
    "buffer_load_dwordx4 v229, s[40:43], s54 offen lds\n"
    "v_mfma_f32_16x16x32_bf16 v[60:63], a[172:175], v[184:187], v[60:63]\n"
    "v_exp_f32 v171, v171\n"
    "v_mfma_f32_16x16x32_bf16 v[60:63], a[236:239], v[188:191], v[60:63]\n"
    "ds_read_b64_tr_b16 a[182:183], v216 offset:45672\n"
    "v_cvt_pk_bf16_f32 v181, v170, v171\n"
    "v_mfma_f32_16x16x32_bf16 v[160:163], a[80:83], a[48:51], -4.0\n"
    "v_exp_f32 v172, v172\n"
    "v_mfma_f32_16x16x32_bf16 v[160:163], a[84:87], a[52:55], v[160:163]\n"
    "ds_read_b64_tr_b16 a[184:185], v217 offset:41536\n"
    "ds_read_b64_tr_b16 a[186:187], v217 offset:45664\n"
    "v_mfma_f32_16x16x32_bf16 v[160:163], a[88:91], a[56:59], v[160:163]\n"
    "v_exp_f32 v173, v173\n"
    "v_mfma_f32_16x16x32_bf16 v[160:163], a[92:95], a[60:63], v[160:163]\n"
    "ds_read_b64_tr_b16 a[188:189], v217 offset:41544\n"
    "v_cvt_pk_bf16_f32 v182, v172, v173\n"
    "v_mfma_f32_16x16x32_bf16 v[164:167], a[108:111], a[60:63], -4.0\n"
    "s_add_u32 m0, s58, 16520\n"
    "v_exp_f32 v174, v174\n"
    "v_mfma_f32_16x16x32_bf16 v[164:167], a[104:107], a[56:59], v[164:167]\n"
    "buffer_load_dwordx4 v229, s[40:43], s54 offen offset:1024 lds\n"
    "v_mfma_f32_16x16x32_bf16 v[164:167], a[100:103], a[52:55], v[164:167]\n"
    "v_exp_f32 v175, v175\n"
    "v_mfma_f32_16x16x32_bf16 v[164:167], a[96:99], a[48:51], v[164:167]\n"
    "ds_read_b64_tr_b16 a[190:191], v217 offset:45672\n"
    "v_cvt_pk_bf16_f32 v183, v174, v175\n"
    "v_mfma_f32_16x16x32_bf16 v[168:171], a[112:115], a[48:51], -4.0\n"
    "ds_read_b64_tr_b16 a[192:193], v218 offset:41536\n"
    "ds_read_b64_tr_b16 a[194:195], v218 offset:45664\n"
    "v_mfma_f32_16x16x32_bf16 v[168:171], a[116:119], a[52:55], v[168:171]\n"
    "v_exp_f32 v160, v160\n"
    "v_mfma_f32_16x16x32_bf16 v[168:171], a[120:123], a[56:59], v[168:171]\n"
    "ds_read_b64_tr_b16 a[196:197], v218 offset:41544\n"
    "ds_read_b64_tr_b16 a[198:199], v218 offset:45672\n"
    "v_mfma_f32_16x16x32_bf16 v[168:171], a[124:127], a[60:63], v[168:171]\n"
    "v_exp_f32 v161, v161\n"
    "v_mfma_f32_16x16x32_bf16 v[172:175], a[140:143], a[60:63], -4.0\n"
    "ds_read_b64_tr_b16 a[200:201], v219 offset:41536\n"
    "ds_read_b64_tr_b16 a[202:203], v219 offset:45664\n"
    "v_mfma_f32_16x16x32_bf16 v[172:175], a[136:139], a[56:59], v[172:175]\n"
    "v_exp_f32 v162, v162\n"
    "v_mfma_f32_16x16x32_bf16 v[172:175], a[132:135], a[52:55], v[172:175]\n"
    "ds_read_b64_tr_b16 a[204:205], v219 offset:41544\n"
    "v_cvt_pk_bf16_f32 v184, v160, v161\n"
    "v_mfma_f32_16x16x32_bf16 v[172:175], a[128:131], a[48:51], v[172:175]\n"
    "v_exp_f32 v163, v163\n"
    "v_smfmac_f32_16x16x64_bf16 v[200:203], a[240:243], v[176:183], v237\n"
    "v_cvt_pk_bf16_f32 v185, v162, v163\n"
    "v_mfma_f32_16x16x32_bf16 v[64:67], a[208:211], v[180:183], v[64:67]\n"
    "v_exp_f32 v164, v164\n"
    "v_mfma_f32_16x16x32_bf16 v[64:67], a[144:147], v[176:179], v[64:67]\n"
    "ds_read_b64_tr_b16 a[206:207], v219 offset:45672\n"
    "v_mfma_f32_16x16x32_bf16 v[68:71], a[148:151], v[176:179], v[68:71]\n"
    "v_exp_f32 v165, v165\n"
    "v_mfma_f32_16x16x32_bf16 v[68:71], a[212:215], v[180:183], v[68:71]\n"
    "v_mfma_f32_16x16x32_bf16 v[72:75], a[216:219], v[180:183], v[72:75]\n"
    "v_exp_f32 v166, v166\n"
    "v_mfma_f32_16x16x32_bf16 v[72:75], a[152:155], v[176:179], v[72:75]\n"
    "v_mfma_f32_16x16x32_bf16 v[76:79], a[156:159], v[176:179], v[76:79]\n"
    "v_exp_f32 v167, v167\n"
    "v_mfma_f32_16x16x32_bf16 v[76:79], a[220:223], v[180:183], v[76:79]\n"
    "v_mfma_f32_16x16x32_bf16 v[80:83], a[224:227], v[180:183], v[80:83]\n"
    "v_exp_f32 v168, v168\n"
    "v_mfma_f32_16x16x32_bf16 v[80:83], a[160:163], v[176:179], v[80:83]\n"
    "v_cvt_pk_bf16_f32 v186, v164, v165\n"
    "v_cvt_pk_bf16_f32 v187, v166, v167\n"
    "v_mfma_f32_16x16x32_bf16 v[84:87], a[164:167], v[176:179], v[84:87]\n"
    "v_exp_f32 v169, v169\n"
    "v_mfma_f32_16x16x32_bf16 v[84:87], a[228:231], v[180:183], v[84:87]\n"
    "v_cvt_pk_bf16_f32 v188, v168, v169\n"
    "v_mfma_f32_16x16x32_bf16 v[88:91], a[232:235], v[180:183], v[88:91]\n"
    "v_exp_f32 v170, v170\n"
    "v_mfma_f32_16x16x32_bf16 v[88:91], a[168:171], v[176:179], v[88:91]\n"
    "v_mfma_f32_16x16x32_bf16 v[92:95], a[172:175], v[176:179], v[92:95]\n"
    "v_exp_f32 v171, v171\n"
    "v_mfma_f32_16x16x32_bf16 v[92:95], a[236:239], v[180:183], v[92:95]\n"
    "v_cvt_pk_bf16_f32 v189, v170, v171\n"
    "v_mfma_f32_16x16x32_bf16 v[160:163], a[80:83], a[64:67], -4.0\n"
    "v_exp_f32 v172, v172\n"
    "v_mfma_f32_16x16x32_bf16 v[160:163], a[84:87], a[68:71], v[160:163]\n"
    "v_mfma_f32_16x16x32_bf16 v[160:163], a[88:91], a[72:75], v[160:163]\n"
    "v_exp_f32 v173, v173\n"
    "v_mfma_f32_16x16x32_bf16 v[160:163], a[92:95], a[76:79], v[160:163]\n"
    "v_cvt_pk_bf16_f32 v190, v172, v173\n"
    "v_mfma_f32_16x16x32_bf16 v[164:167], a[108:111], a[76:79], -4.0\n"
    "v_exp_f32 v174, v174\n"
    "v_mfma_f32_16x16x32_bf16 v[164:167], a[104:107], a[72:75], v[164:167]\n"
    "ds_read_b128 a[252:255], v224 offset:8192\n"
    "v_mfma_f32_16x16x32_bf16 v[164:167], a[100:103], a[68:71], v[164:167]\n"
    "v_exp_f32 v175, v175\n"
    "v_mfma_f32_16x16x32_bf16 v[164:167], a[96:99], a[64:67], v[164:167]\n"
    "s_waitcnt vmcnt(13) lgkmcnt(1)\n"
    "v_cvt_pk_bf16_f32 v191, v174, v175\n"
    "v_mfma_f32_16x16x32_bf16 v[168:171], a[112:115], a[64:67], -4.0\n"
    "s_barrier\n"
    "v_mfma_f32_16x16x32_bf16 v[168:171], a[116:119], a[68:71], v[168:171]\n"
    "v_exp_f32 v160, v160\n"
    "v_mfma_f32_16x16x32_bf16 v[168:171], a[120:123], a[72:75], v[168:171]\n"
    "ds_read_b128 a[80:83], v212 offset:16640\n"
    "ds_read_b128 a[84:87], v213 offset:16640\n"
    "v_mfma_f32_16x16x32_bf16 v[168:171], a[124:127], a[76:79], v[168:171]\n"
    "v_exp_f32 v161, v161\n"
    "v_mfma_f32_16x16x32_bf16 v[172:175], a[140:143], a[76:79], -4.0\n"
    "ds_read_b128 a[88:91], v214 offset:16640\n"
    "ds_read_b128 a[92:95], v215 offset:16640\n"
    "v_mfma_f32_16x16x32_bf16 v[172:175], a[136:139], a[72:75], v[172:175]\n"
    "v_exp_f32 v162, v162\n"
    "v_mfma_f32_16x16x32_bf16 v[172:175], a[132:135], a[68:71], v[172:175]\n"
    "s_add_u32 s56, s64, 4096\n"
    "ds_read_b128 a[108:111], v215 offset:20800\n"
    "ds_read_b128 a[104:107], v214 offset:20800\n"
    "v_mfma_f32_16x16x32_bf16 v[172:175], a[128:131], a[64:67], v[172:175]\n"
    "s_waitcnt lgkmcnt(6)\n"
    "buffer_store_dwordx4 a[252:255], v228, s[48:51], s56 offen nt\n"
    "v_exp_f32 v163, v163\n"
    "v_smfmac_f32_16x16x64_bf16 v[204:207], a[240:243], v[184:191], v237\n"
    "ds_read_b128 a[100:103], v213 offset:20800\n"
    "ds_read_b128 a[96:99], v212 offset:20800\n"
    "v_mfma_f32_16x16x32_bf16 v[96:99], a[208:211], v[188:191], v[96:99]\n"
    "v_exp_f32 v164, v164\n"
    "v_mfma_f32_16x16x32_bf16 v[96:99], a[144:147], v[184:187], v[96:99]\n"
    "v_cvt_pk_bf16_f32 v176, v160, v161\n"
    "v_cvt_pk_bf16_f32 v177, v162, v163\n"
    "v_mfma_f32_16x16x32_bf16 v[100:103], a[148:151], v[184:187], v[100:103]\n"
    "v_exp_f32 v165, v165\n"
    "v_mfma_f32_16x16x32_bf16 v[100:103], a[212:215], v[188:191], v[100:103]\n"
    "ds_read_b128 a[112:115], v212 offset:24960\n"
    "ds_read_b128 a[116:119], v213 offset:24960\n"
    "v_mfma_f32_16x16x32_bf16 v[104:107], a[216:219], v[188:191], v[104:107]\n"
    "s_add_u32 m0, s58, 8256\n"
    "v_exp_f32 v166, v166\n"
    "v_mfma_f32_16x16x32_bf16 v[104:107], a[152:155], v[184:187], v[104:107]\n"
    "buffer_load_dwordx4 v229, s[40:43], s53 offen lds\n"
    "v_mfma_f32_16x16x32_bf16 v[108:111], a[156:159], v[184:187], v[108:111]\n"
    "s_add_u32 m0, s58, 8264\n"
    "v_exp_f32 v167, v167\n"
    "v_mfma_f32_16x16x32_bf16 v[108:111], a[220:223], v[188:191], v[108:111]\n"
    "buffer_load_dwordx4 v229, s[40:43], s53 offen offset:1024 lds\n"
    "v_mfma_f32_16x16x32_bf16 v[112:115], a[224:227], v[188:191], v[112:115]\n"
    "v_exp_f32 v168, v168\n"
    "v_mfma_f32_16x16x32_bf16 v[112:115], a[160:163], v[184:187], v[112:115]\n"
    "ds_read_b128 a[120:123], v214 offset:24960\n"
    "ds_read_b128 a[124:127], v215 offset:24960\n"
    "v_mfma_f32_16x16x32_bf16 v[116:119], a[164:167], v[184:187], v[116:119]\n"
    "v_exp_f32 v169, v169\n"
    "v_mfma_f32_16x16x32_bf16 v[116:119], a[228:231], v[188:191], v[116:119]\n"
    "v_cvt_pk_bf16_f32 v178, v164, v165\n"
    "v_cvt_pk_bf16_f32 v179, v166, v167\n"
    "v_mfma_f32_16x16x32_bf16 v[120:123], a[232:235], v[188:191], v[120:123]\n"
    "v_exp_f32 v170, v170\n"
    "v_mfma_f32_16x16x32_bf16 v[120:123], a[168:171], v[184:187], v[120:123]\n"
    "ds_read_b128 a[140:143], v215 offset:29120\n"
    "ds_read_b128 a[136:139], v214 offset:29120\n"
    "v_mfma_f32_16x16x32_bf16 v[124:127], a[172:175], v[184:187], v[124:127]\n"
    "v_exp_f32 v171, v171\n"
    "v_mfma_f32_16x16x32_bf16 v[124:127], a[236:239], v[188:191], v[124:127]\n"
    "s_waitcnt lgkmcnt(4)\n"
    "ds_read_b128 a[132:135], v213 offset:29120\n"
    "ds_read_b128 a[128:131], v212 offset:29120\n"
    "v_mfma_f32_16x16x32_bf16 v[160:163], a[80:83], a[0:3], -4.0\n"
    "v_exp_f32 v172, v172\n"
    "v_mfma_f32_16x16x32_bf16 v[160:163], a[84:87], a[4:7], v[160:163]\n"
    "v_cvt_pk_bf16_f32 v180, v168, v169\n"
    "v_cvt_pk_bf16_f32 v181, v170, v171\n"
    "v_mfma_f32_16x16x32_bf16 v[160:163], a[88:91], a[8:11], v[160:163]\n"
    "v_exp_f32 v173, v173\n"
    "v_mfma_f32_16x16x32_bf16 v[160:163], a[92:95], a[12:15], v[160:163]\n"
    "v_cvt_pk_bf16_f32 v182, v172, v173\n"
    "v_mfma_f32_16x16x32_bf16 v[164:167], a[108:111], a[12:15], -4.0\n"
    "v_exp_f32 v174, v174\n"
    "v_mfma_f32_16x16x32_bf16 v[164:167], a[104:107], a[8:11], v[164:167]\n"
    "v_mfma_f32_16x16x32_bf16 v[164:167], a[100:103], a[4:7], v[164:167]\n"
    "v_exp_f32 v175, v175\n"
    "v_mfma_f32_16x16x32_bf16 v[164:167], a[96:99], a[0:3], v[164:167]\n"
    "v_mfma_f32_16x16x32_bf16 v[168:171], a[112:115], a[0:3], -4.0\n"
    "v_cvt_pk_bf16_f32 v183, v174, v175\n"
    "v_mfma_f32_16x16x32_bf16 v[168:171], a[116:119], a[4:7], v[168:171]\n"
    "s_waitcnt lgkmcnt(0)\n"
    "v_exp_f32 v160, v160\n"
    "v_mfma_f32_16x16x32_bf16 v[168:171], a[120:123], a[8:11], v[168:171]\n"
    "v_mfma_f32_16x16x32_bf16 v[168:171], a[124:127], a[12:15], v[168:171]\n"
    "v_exp_f32 v161, v161\n"
    "v_mfma_f32_16x16x32_bf16 v[172:175], a[140:143], a[12:15], -4.0\n"
    "v_mfma_f32_16x16x32_bf16 v[172:175], a[136:139], a[8:11], v[172:175]\n"
    "v_exp_f32 v162, v162\n"
    "v_mfma_f32_16x16x32_bf16 v[172:175], a[132:135], a[4:7], v[172:175]\n"
    "v_mfma_f32_16x16x32_bf16 v[172:175], a[128:131], a[0:3], v[172:175]\n"
    "v_exp_f32 v163, v163\n"
    "v_smfmac_f32_16x16x64_bf16 v[208:211], a[240:243], v[176:183], v237\n"
    "ds_read_b128 a[244:247], v225 offset:9216\n"
    "v_mfma_f32_16x16x32_bf16 v[128:131], a[208:211], v[180:183], v[128:131]\n"
    "v_exp_f32 v164, v164\n"
    "v_mfma_f32_16x16x32_bf16 v[128:131], a[144:147], v[176:179], v[128:131]\n"
    "v_cvt_pk_bf16_f32 v184, v160, v161\n"
    "v_mfma_f32_16x16x32_bf16 v[132:135], a[148:151], v[176:179], v[132:135]\n"
    "v_exp_f32 v165, v165\n"
    "v_mfma_f32_16x16x32_bf16 v[132:135], a[212:215], v[180:183], v[132:135]\n"
    "ds_read_b64_tr_b16 a[208:209], v216 offset:58048\n"
    "v_cvt_pk_bf16_f32 v185, v162, v163\n"
    "v_mfma_f32_16x16x32_bf16 v[136:139], a[216:219], v[180:183], v[136:139]\n"
    "v_exp_f32 v166, v166\n"
    "v_mfma_f32_16x16x32_bf16 v[136:139], a[152:155], v[176:179], v[136:139]\n"
    "ds_read_b64_tr_b16 a[210:211], v216 offset:62176\n"
    "ds_read_b64_tr_b16 a[212:213], v216 offset:58056\n"
    "v_mfma_f32_16x16x32_bf16 v[140:143], a[156:159], v[176:179], v[140:143]\n"
    "v_exp_f32 v167, v167\n"
    "v_mfma_f32_16x16x32_bf16 v[140:143], a[220:223], v[180:183], v[140:143]\n"
    "ds_read_b64_tr_b16 a[214:215], v216 offset:62184\n"
    "v_mfma_f32_16x16x32_bf16 v[144:147], a[224:227], v[180:183], v[144:147]\n"
    "s_waitcnt lgkmcnt(4)\n"
    "buffer_store_dwordx4 a[244:247], v228, s[48:51], s56 offen offset:1024 nt\n"
    "v_exp_f32 v168, v168\n"
    "v_mfma_f32_16x16x32_bf16 v[144:147], a[160:163], v[176:179], v[144:147]\n"
    "v_cvt_pk_bf16_f32 v186, v164, v165\n"
    "v_cvt_pk_bf16_f32 v187, v166, v167\n"
    "v_mfma_f32_16x16x32_bf16 v[148:151], a[164:167], v[176:179], v[148:151]\n"
    "v_exp_f32 v169, v169\n"
    "v_mfma_f32_16x16x32_bf16 v[148:151], a[228:231], v[180:183], v[148:151]\n"
    "ds_read_b64_tr_b16 a[216:217], v217 offset:58048\n"
    "ds_read_b64_tr_b16 a[218:219], v217 offset:62176\n"
    "v_mfma_f32_16x16x32_bf16 v[152:155], a[232:235], v[180:183], v[152:155]\n"
    "v_exp_f32 v170, v170\n"
    "v_mfma_f32_16x16x32_bf16 v[152:155], a[168:171], v[176:179], v[152:155]\n"
    "ds_read_b64_tr_b16 a[220:221], v217 offset:58056\n"
    "ds_read_b64_tr_b16 a[222:223], v217 offset:62184\n"
    "v_mfma_f32_16x16x32_bf16 v[156:159], a[172:175], v[176:179], v[156:159]\n"
    "v_exp_f32 v171, v171\n"
    "v_mfma_f32_16x16x32_bf16 v[156:159], a[236:239], v[180:183], v[156:159]\n"
    "v_cvt_pk_bf16_f32 v188, v168, v169\n"
    "v_cvt_pk_bf16_f32 v189, v170, v171\n"
    "v_mfma_f32_16x16x32_bf16 v[160:163], a[80:83], a[16:19], -4.0\n"
    "v_exp_f32 v172, v172\n"
    "v_mfma_f32_16x16x32_bf16 v[160:163], a[84:87], a[20:23], v[160:163]\n"
    "ds_read_b64_tr_b16 a[224:225], v218 offset:58048\n"
    "ds_read_b64_tr_b16 a[226:227], v218 offset:62176\n"
    "v_mfma_f32_16x16x32_bf16 v[160:163], a[88:91], a[24:27], v[160:163]\n"
    "v_exp_f32 v173, v173\n"
    "v_mfma_f32_16x16x32_bf16 v[160:163], a[92:95], a[28:31], v[160:163]\n"
    "ds_read_b64_tr_b16 a[228:229], v218 offset:58056\n"
    "ds_read_b64_tr_b16 a[230:231], v218 offset:62184\n"
    "v_mfma_f32_16x16x32_bf16 v[164:167], a[108:111], a[28:31], -4.0\n"
    "v_exp_f32 v174, v174\n"
    "v_mfma_f32_16x16x32_bf16 v[164:167], a[104:107], a[24:27], v[164:167]\n"
    "ds_read_b64_tr_b16 a[232:233], v219 offset:58048\n"
    "v_cvt_pk_bf16_f32 v190, v172, v173\n"
    "v_mfma_f32_16x16x32_bf16 v[164:167], a[100:103], a[20:23], v[164:167]\n"
    "v_exp_f32 v175, v175\n"
    "v_mfma_f32_16x16x32_bf16 v[164:167], a[96:99], a[16:19], v[164:167]\n"
    "ds_read_b64_tr_b16 a[234:235], v219 offset:62176\n"
    "v_cvt_pk_bf16_f32 v191, v174, v175\n"
    "v_mfma_f32_16x16x32_bf16 v[168:171], a[112:115], a[16:19], -4.0\n"
    "s_add_u32 s52, s52, 16384\n"
    "ds_read_b64_tr_b16 a[236:237], v219 offset:58056\n"
    "ds_read_b64_tr_b16 a[238:239], v219 offset:62184\n"
    "v_mfma_f32_16x16x32_bf16 v[168:171], a[116:119], a[20:23], v[168:171]\n"
    "v_exp_f32 v160, v160\n"
    "v_mfma_f32_16x16x32_bf16 v[168:171], a[120:123], a[24:27], v[168:171]\n"
    "s_add_u32 s54, s54, 16384\n"
    "v_mfma_f32_16x16x32_bf16 v[168:171], a[124:127], a[28:31], v[168:171]\n"
    "v_exp_f32 v161, v161\n"
    "v_mfma_f32_16x16x32_bf16 v[172:175], a[140:143], a[28:31], -4.0\n"
    "s_add_u32 s53, s53, 16384\n"
    "v_cvt_pk_bf16_f32 v176, v160, v161\n"
    "v_mfma_f32_16x16x32_bf16 v[172:175], a[136:139], a[24:27], v[172:175]\n"
    "v_exp_f32 v162, v162\n"
    "v_mfma_f32_16x16x32_bf16 v[172:175], a[132:135], a[20:23], v[172:175]\n"
    "v_mfma_f32_16x16x32_bf16 v[172:175], a[128:131], a[16:19], v[172:175]\n"
    "v_exp_f32 v163, v163\n"
    "v_smfmac_f32_16x16x64_bf16 v[192:195], a[240:243], v[184:191], v237\n"
    "s_waitcnt lgkmcnt(0)\n"
    "v_cvt_pk_bf16_f32 v177, v162, v163\n"
    "v_mfma_f32_16x16x32_bf16 v[0:3], a[208:211], v[188:191], v[0:3]\n"
    "v_exp_f32 v164, v164\n"
    "v_mfma_f32_16x16x32_bf16 v[0:3], a[176:179], v[184:187], v[0:3]\n"
    "v_mfma_f32_16x16x32_bf16 v[4:7], a[180:183], v[184:187], v[4:7]\n"
    "v_exp_f32 v165, v165\n"
    "v_mfma_f32_16x16x32_bf16 v[4:7], a[212:215], v[188:191], v[4:7]\n"
    "v_cvt_pk_bf16_f32 v178, v164, v165\n"
    "v_mfma_f32_16x16x32_bf16 v[8:11], a[216:219], v[188:191], v[8:11]\n"
    "v_exp_f32 v166, v166\n"
    "v_mfma_f32_16x16x32_bf16 v[8:11], a[184:187], v[184:187], v[8:11]\n"
    "v_mfma_f32_16x16x32_bf16 v[12:15], a[188:191], v[184:187], v[12:15]\n"
    "s_waitcnt vmcnt(13)\n"
    "v_exp_f32 v167, v167\n"
    "v_mfma_f32_16x16x32_bf16 v[12:15], a[220:223], v[188:191], v[12:15]\n"
    "s_barrier\n"
    "v_mfma_f32_16x16x32_bf16 v[16:19], a[224:227], v[188:191], v[16:19]\n"
    "v_exp_f32 v168, v168\n"
    "v_mfma_f32_16x16x32_bf16 v[16:19], a[192:195], v[184:187], v[16:19]\n"
    "v_mfma_f32_16x16x32_bf16 v[20:23], a[196:199], v[184:187], v[20:23]\n"
    "s_add_u32 m0, s57, 16640\n"
    "v_exp_f32 v169, v169\n"
    "v_mfma_f32_16x16x32_bf16 v[20:23], a[228:231], v[188:191], v[20:23]\n"
    "buffer_load_dwordx4 v229, s[36:39], s52 offen lds\n"
    "v_mfma_f32_16x16x32_bf16 v[24:27], a[232:235], v[188:191], v[24:27]\n"
    "v_exp_f32 v170, v170\n"
    "v_mfma_f32_16x16x32_bf16 v[24:27], a[200:203], v[184:187], v[24:27]\n"
    "v_cvt_pk_bf16_f32 v179, v166, v167\n"
    "v_cvt_pk_bf16_f32 v180, v168, v169\n"
    "v_mfma_f32_16x16x32_bf16 v[28:31], a[204:207], v[184:187], v[28:31]\n"
    "v_exp_f32 v171, v171\n"
    "v_mfma_f32_16x16x32_bf16 v[28:31], a[236:239], v[188:191], v[28:31]\n"
    "v_cvt_pk_bf16_f32 v181, v170, v171\n"
    "v_mfma_f32_16x16x32_bf16 v[160:163], a[80:83], a[32:35], -4.0\n"
    "v_exp_f32 v172, v172\n"
    "v_mfma_f32_16x16x32_bf16 v[160:163], a[84:87], a[36:39], v[160:163]\n"
    "v_mfma_f32_16x16x32_bf16 v[160:163], a[88:91], a[40:43], v[160:163]\n"
    "s_add_u32 m0, s57, 16640\n"
    "v_exp_f32 v173, v173\n"
    "v_mfma_f32_16x16x32_bf16 v[160:163], a[92:95], a[44:47], v[160:163]\n"
    "buffer_load_dwordx4 v229, s[36:39], s52 offen offset:1024 lds\n"
    "v_mfma_f32_16x16x32_bf16 v[164:167], a[108:111], a[44:47], -4.0\n"
    "v_exp_f32 v174, v174\n"
    "v_mfma_f32_16x16x32_bf16 v[164:167], a[104:107], a[40:43], v[164:167]\n"
    "ds_read_b64_tr_b16 a[144:145], v216 offset:33280\n"
    "v_cvt_pk_bf16_f32 v182, v172, v173\n"
    "v_mfma_f32_16x16x32_bf16 v[164:167], a[100:103], a[36:39], v[164:167]\n"
    "v_exp_f32 v175, v175\n"
    "v_mfma_f32_16x16x32_bf16 v[164:167], a[96:99], a[32:35], v[164:167]\n"
    "v_cvt_pk_bf16_f32 v183, v174, v175\n"
    "v_mfma_f32_16x16x32_bf16 v[168:171], a[112:115], a[32:35], -4.0\n"
    "ds_read_b64_tr_b16 a[146:147], v216 offset:37408\n"
    "ds_read_b128 a[248:251], v226 offset:10240\n"
    "v_mfma_f32_16x16x32_bf16 v[168:171], a[116:119], a[36:39], v[168:171]\n"
    "v_exp_f32 v160, v160\n"
    "v_mfma_f32_16x16x32_bf16 v[168:171], a[120:123], a[40:43], v[168:171]\n"
    "v_mfma_f32_16x16x32_bf16 v[168:171], a[124:127], a[44:47], v[168:171]\n"
    "s_add_u32 m0, s57, 16672\n"
    "v_exp_f32 v161, v161\n"
    "v_mfma_f32_16x16x32_bf16 v[172:175], a[140:143], a[44:47], -4.0\n"
    "buffer_load_dwordx4 v229, s[36:39], s52 offen offset:2048 lds\n"
    "v_mfma_f32_16x16x32_bf16 v[172:175], a[136:139], a[40:43], v[172:175]\n"
    "v_exp_f32 v162, v162\n"
    "v_mfma_f32_16x16x32_bf16 v[172:175], a[132:135], a[36:39], v[172:175]\n"
    "ds_read_b64_tr_b16 a[148:149], v216 offset:33288\n"
    "v_cvt_pk_bf16_f32 v184, v160, v161\n"
    "v_mfma_f32_16x16x32_bf16 v[172:175], a[128:131], a[32:35], v[172:175]\n"
    "v_exp_f32 v163, v163\n"
    "v_smfmac_f32_16x16x64_bf16 v[196:199], a[240:243], v[176:183], v237\n"
    "ds_read_b64_tr_b16 a[150:151], v216 offset:37416\n"
    "v_cvt_pk_bf16_f32 v185, v162, v163\n"
    "v_mfma_f32_16x16x32_bf16 v[32:35], a[208:211], v[180:183], v[32:35]\n"
    "s_waitcnt lgkmcnt(2)\n"
    "buffer_store_dwordx4 a[248:251], v228, s[48:51], s56 offen offset:2048 nt\n"
    "v_exp_f32 v164, v164\n"
    "v_mfma_f32_16x16x32_bf16 v[32:35], a[176:179], v[176:179], v[32:35]\n"
    "ds_read_b64_tr_b16 a[152:153], v217 offset:33280\n"
    "v_mfma_f32_16x16x32_bf16 v[36:39], a[180:183], v[176:179], v[36:39]\n"
    "s_add_u32 m0, s57, 16672\n"
    "v_exp_f32 v165, v165\n"
    "v_mfma_f32_16x16x32_bf16 v[36:39], a[212:215], v[180:183], v[36:39]\n"
    "buffer_load_dwordx4 v229, s[36:39], s52 offen offset:3072 lds\n"
    "v_mfma_f32_16x16x32_bf16 v[40:43], a[216:219], v[180:183], v[40:43]\n"
    "v_exp_f32 v166, v166\n"
    "v_mfma_f32_16x16x32_bf16 v[40:43], a[184:187], v[176:179], v[40:43]\n"
    "ds_read_b64_tr_b16 a[154:155], v217 offset:37408\n"
    "v_cvt_pk_bf16_f32 v186, v164, v165\n"
    "v_mfma_f32_16x16x32_bf16 v[44:47], a[188:191], v[176:179], v[44:47]\n"
    "v_exp_f32 v167, v167\n"
    "v_mfma_f32_16x16x32_bf16 v[44:47], a[220:223], v[180:183], v[44:47]\n"
    "ds_read_b64_tr_b16 a[156:157], v217 offset:33288\n"
    "v_cvt_pk_bf16_f32 v187, v166, v167\n"
    "v_mfma_f32_16x16x32_bf16 v[48:51], a[224:227], v[180:183], v[48:51]\n"
    "v_exp_f32 v168, v168\n"
    "v_mfma_f32_16x16x32_bf16 v[48:51], a[192:195], v[176:179], v[48:51]\n"
    "ds_read_b64_tr_b16 a[158:159], v217 offset:37416\n"
    "v_mfma_f32_16x16x32_bf16 v[52:55], a[196:199], v[176:179], v[52:55]\n"
    "v_exp_f32 v169, v169\n"
    "v_mfma_f32_16x16x32_bf16 v[52:55], a[228:231], v[180:183], v[52:55]\n"
    "v_cvt_pk_bf16_f32 v188, v168, v169\n"
    "v_mfma_f32_16x16x32_bf16 v[56:59], a[232:235], v[180:183], v[56:59]\n"
    "s_add_u32 m0, s58, 24768\n"
    "v_exp_f32 v170, v170\n"
    "v_mfma_f32_16x16x32_bf16 v[56:59], a[200:203], v[176:179], v[56:59]\n"
    "buffer_load_dwordx4 v229, s[40:43], s54 offen lds\n"
    "v_mfma_f32_16x16x32_bf16 v[60:63], a[204:207], v[176:179], v[60:63]\n"
    "v_exp_f32 v171, v171\n"
    "v_mfma_f32_16x16x32_bf16 v[60:63], a[236:239], v[180:183], v[60:63]\n"
    "v_cvt_pk_bf16_f32 v189, v170, v171\n"
    "v_mfma_f32_16x16x32_bf16 v[160:163], a[80:83], a[48:51], -4.0\n"
    "v_exp_f32 v172, v172\n"
    "v_mfma_f32_16x16x32_bf16 v[160:163], a[84:87], a[52:55], v[160:163]\n"
    "v_mfma_f32_16x16x32_bf16 v[160:163], a[88:91], a[56:59], v[160:163]\n"
    "v_exp_f32 v173, v173\n"
    "v_mfma_f32_16x16x32_bf16 v[160:163], a[92:95], a[60:63], v[160:163]\n"
    "v_cvt_pk_bf16_f32 v190, v172, v173\n"
    "v_mfma_f32_16x16x32_bf16 v[164:167], a[108:111], a[60:63], -4.0\n"
    "s_add_u32 m0, s58, 24776\n"
    "v_exp_f32 v174, v174\n"
    "v_mfma_f32_16x16x32_bf16 v[164:167], a[104:107], a[56:59], v[164:167]\n"
    "buffer_load_dwordx4 v229, s[40:43], s54 offen offset:1024 lds\n"
    "v_mfma_f32_16x16x32_bf16 v[164:167], a[100:103], a[52:55], v[164:167]\n"
    "v_exp_f32 v175, v175\n"
    "v_mfma_f32_16x16x32_bf16 v[164:167], a[96:99], a[48:51], v[164:167]\n"
    "v_mfma_f32_16x16x32_bf16 v[168:171], a[112:115], a[48:51], -4.0\n"
    "ds_read_b64_tr_b16 a[160:161], v218 offset:33280\n"
    "v_cvt_pk_bf16_f32 v191, v174, v175\n"
    "v_mfma_f32_16x16x32_bf16 v[168:171], a[116:119], a[52:55], v[168:171]\n"
    "v_exp_f32 v160, v160\n"
    "v_mfma_f32_16x16x32_bf16 v[168:171], a[120:123], a[56:59], v[168:171]\n"
    "ds_read_b64_tr_b16 a[162:163], v218 offset:37408\n"
    "ds_read_b64_tr_b16 a[164:165], v218 offset:33288\n"
    "v_mfma_f32_16x16x32_bf16 v[168:171], a[124:127], a[60:63], v[168:171]\n"
    "v_exp_f32 v161, v161\n"
    "v_mfma_f32_16x16x32_bf16 v[172:175], a[140:143], a[60:63], -4.0\n"
    "ds_read_b64_tr_b16 a[166:167], v218 offset:37416\n"
    "v_mfma_f32_16x16x32_bf16 v[172:175], a[136:139], a[56:59], v[172:175]\n"
    "v_exp_f32 v162, v162\n"
    "v_mfma_f32_16x16x32_bf16 v[172:175], a[132:135], a[52:55], v[172:175]\n"
    "ds_read_b64_tr_b16 a[168:169], v219 offset:33280\n"
    "ds_read_b64_tr_b16 a[170:171], v219 offset:37408\n"
    "v_mfma_f32_16x16x32_bf16 v[172:175], a[128:131], a[48:51], v[172:175]\n"
    "v_exp_f32 v163, v163\n"
    "v_smfmac_f32_16x16x64_bf16 v[200:203], a[240:243], v[184:191], v237\n"
    "v_cvt_pk_bf16_f32 v176, v160, v161\n"
    "v_mfma_f32_16x16x32_bf16 v[64:67], a[208:211], v[188:191], v[64:67]\n"
    "v_exp_f32 v164, v164\n"
    "v_mfma_f32_16x16x32_bf16 v[64:67], a[176:179], v[184:187], v[64:67]\n"
    "ds_read_b64_tr_b16 a[172:173], v219 offset:33288\n"
    "v_mfma_f32_16x16x32_bf16 v[68:71], a[180:183], v[184:187], v[68:71]\n"
    "v_exp_f32 v165, v165\n"
    "v_mfma_f32_16x16x32_bf16 v[68:71], a[212:215], v[188:191], v[68:71]\n"
    "ds_read_b64_tr_b16 a[174:175], v219 offset:37416\n"
    "v_cvt_pk_bf16_f32 v177, v162, v163\n"
    "v_mfma_f32_16x16x32_bf16 v[72:75], a[216:219], v[188:191], v[72:75]\n"
    "v_exp_f32 v166, v166\n"
    "v_mfma_f32_16x16x32_bf16 v[72:75], a[184:187], v[184:187], v[72:75]\n"
    "v_mfma_f32_16x16x32_bf16 v[76:79], a[188:191], v[184:187], v[76:79]\n"
    "v_exp_f32 v167, v167\n"
    "v_mfma_f32_16x16x32_bf16 v[76:79], a[220:223], v[188:191], v[76:79]\n"
    "v_cvt_pk_bf16_f32 v178, v164, v165\n"
    "v_mfma_f32_16x16x32_bf16 v[80:83], a[224:227], v[188:191], v[80:83]\n"
    "v_exp_f32 v168, v168\n"
    "v_mfma_f32_16x16x32_bf16 v[80:83], a[192:195], v[184:187], v[80:83]\n"
    "v_cvt_pk_bf16_f32 v179, v166, v167\n"
    "v_mfma_f32_16x16x32_bf16 v[84:87], a[196:199], v[184:187], v[84:87]\n"
    "v_exp_f32 v169, v169\n"
    "v_mfma_f32_16x16x32_bf16 v[84:87], a[228:231], v[188:191], v[84:87]\n"
    "v_cvt_pk_bf16_f32 v180, v168, v169\n"
    "v_mfma_f32_16x16x32_bf16 v[88:91], a[232:235], v[188:191], v[88:91]\n"
    "v_exp_f32 v170, v170\n"
    "v_mfma_f32_16x16x32_bf16 v[88:91], a[200:203], v[184:187], v[88:91]\n"
    "ds_read_b128 a[252:255], v227 offset:11264\n"
    "v_mfma_f32_16x16x32_bf16 v[92:95], a[204:207], v[184:187], v[92:95]\n"
    "v_exp_f32 v171, v171\n"
    "v_mfma_f32_16x16x32_bf16 v[92:95], a[236:239], v[188:191], v[92:95]\n"
    "v_cvt_pk_bf16_f32 v181, v170, v171\n"
    "v_mfma_f32_16x16x32_bf16 v[160:163], a[80:83], a[64:67], -4.0\n"
    "v_exp_f32 v172, v172\n"
    "v_mfma_f32_16x16x32_bf16 v[160:163], a[84:87], a[68:71], v[160:163]\n"
    "v_mfma_f32_16x16x32_bf16 v[160:163], a[88:91], a[72:75], v[160:163]\n"
    "v_exp_f32 v173, v173\n"
    "v_mfma_f32_16x16x32_bf16 v[160:163], a[92:95], a[76:79], v[160:163]\n"
    "v_cvt_pk_bf16_f32 v182, v172, v173\n"
    "v_mfma_f32_16x16x32_bf16 v[164:167], a[108:111], a[76:79], -4.0\n"
    "v_exp_f32 v174, v174\n"
    "v_mfma_f32_16x16x32_bf16 v[164:167], a[104:107], a[72:75], v[164:167]\n"
    "v_mfma_f32_16x16x32_bf16 v[164:167], a[100:103], a[68:71], v[164:167]\n"
    "s_waitcnt lgkmcnt(0)\n"
    "buffer_store_dwordx4 a[252:255], v228, s[48:51], s56 offen offset:3072 nt\n"
    "v_exp_f32 v175, v175\n"
    "v_mfma_f32_16x16x32_bf16 v[164:167], a[96:99], a[64:67], v[164:167]\n"
    "s_waitcnt vmcnt(12)\n"
    "v_cvt_pk_bf16_f32 v183, v174, v175\n"
    "v_mfma_f32_16x16x32_bf16 v[168:171], a[112:115], a[64:67], -4.0\n"
    "s_barrier\n"
    "v_mfma_f32_16x16x32_bf16 v[168:171], a[116:119], a[68:71], v[168:171]\n"
    "v_exp_f32 v160, v160\n"
    "v_mfma_f32_16x16x32_bf16 v[168:171], a[120:123], a[72:75], v[168:171]\n"
    "ds_read_b128 a[80:83], v212 offset:0\n"
    "ds_read_b128 a[84:87], v213 offset:0\n"
    "v_mfma_f32_16x16x32_bf16 v[168:171], a[124:127], a[76:79], v[168:171]\n"
    "v_exp_f32 v161, v161\n"
    "v_mfma_f32_16x16x32_bf16 v[172:175], a[140:143], a[76:79], -4.0\n"
    "ds_read_b128 a[88:91], v214 offset:0\n"
    "ds_read_b128 a[92:95], v215 offset:0\n"
    "v_mfma_f32_16x16x32_bf16 v[172:175], a[136:139], a[72:75], v[172:175]\n"
    "v_exp_f32 v162, v162\n"
    "v_mfma_f32_16x16x32_bf16 v[172:175], a[132:135], a[68:71], v[172:175]\n"
    "ds_read_b128 a[108:111], v215 offset:4160\n"
    "ds_read_b128 a[104:107], v214 offset:4160\n"
    "v_mfma_f32_16x16x32_bf16 v[172:175], a[128:131], a[64:67], v[172:175]\n"
    "v_exp_f32 v163, v163\n"
    "v_smfmac_f32_16x16x64_bf16 v[204:207], a[240:243], v[176:183], v237\n"
    "ds_read_b128 a[100:103], v213 offset:4160\n"
    "ds_read_b128 a[96:99], v212 offset:4160\n"
    "v_mfma_f32_16x16x32_bf16 v[96:99], a[208:211], v[180:183], v[96:99]\n"
    "v_exp_f32 v164, v164\n"
    "v_mfma_f32_16x16x32_bf16 v[96:99], a[176:179], v[176:179], v[96:99]\n"
    "v_cvt_pk_bf16_f32 v184, v160, v161\n"
    "v_cvt_pk_bf16_f32 v185, v162, v163\n"
    "v_mfma_f32_16x16x32_bf16 v[100:103], a[180:183], v[176:179], v[100:103]\n"
    "v_exp_f32 v165, v165\n"
    "v_mfma_f32_16x16x32_bf16 v[100:103], a[212:215], v[180:183], v[100:103]\n"
    "ds_read_b128 a[112:115], v212 offset:8320\n"
    "v_mfma_f32_16x16x32_bf16 v[104:107], a[216:219], v[180:183], v[104:107]\n"
    "s_add_u32 m0, s58, 0\n"
    "v_exp_f32 v166, v166\n"
    "v_mfma_f32_16x16x32_bf16 v[104:107], a[184:187], v[176:179], v[104:107]\n"
    "buffer_load_dwordx4 v229, s[40:43], s53 offen lds\n"
    "v_mfma_f32_16x16x32_bf16 v[108:111], a[188:191], v[176:179], v[108:111]\n"
    "v_exp_f32 v167, v167\n"
    "v_mfma_f32_16x16x32_bf16 v[108:111], a[220:223], v[180:183], v[108:111]\n"
    "v_mfma_f32_16x16x32_bf16 v[112:115], a[224:227], v[180:183], v[112:115]\n"
    "s_add_u32 m0, s58, 8\n"
    "v_exp_f32 v168, v168\n"
    "v_mfma_f32_16x16x32_bf16 v[112:115], a[192:195], v[176:179], v[112:115]\n"
    "buffer_load_dwordx4 v229, s[40:43], s53 offen offset:1024 lds\n"
    "v_mfma_f32_16x16x32_bf16 v[116:119], a[196:199], v[176:179], v[116:119]\n"
    "v_exp_f32 v169, v169\n"
    "v_mfma_f32_16x16x32_bf16 v[116:119], a[228:231], v[180:183], v[116:119]\n"
    "ds_read_b128 a[116:119], v213 offset:8320\n"
    "ds_read_b128 a[120:123], v214 offset:8320\n"
    "v_mfma_f32_16x16x32_bf16 v[120:123], a[232:235], v[180:183], v[120:123]\n"
    "v_exp_f32 v170, v170\n"
    "v_mfma_f32_16x16x32_bf16 v[120:123], a[200:203], v[176:179], v[120:123]\n"
    "ds_read_b128 a[124:127], v215 offset:8320\n"
    "v_cvt_pk_bf16_f32 v186, v164, v165\n"
    "v_mfma_f32_16x16x32_bf16 v[124:127], a[204:207], v[176:179], v[124:127]\n"
    "v_exp_f32 v171, v171\n"
    "v_mfma_f32_16x16x32_bf16 v[124:127], a[236:239], v[180:183], v[124:127]\n"
    "s_waitcnt lgkmcnt(3)\n"
    "ds_read_b128 a[140:143], v215 offset:12480\n"
    "v_cvt_pk_bf16_f32 v187, v166, v167\n"
    "v_mfma_f32_16x16x32_bf16 v[160:163], a[80:83], a[0:3], -4.0\n"
    "v_exp_f32 v172, v172\n"
    "v_mfma_f32_16x16x32_bf16 v[160:163], a[84:87], a[4:7], v[160:163]\n"
    "ds_read_b128 a[136:139], v214 offset:12480\n"
    "ds_read_b128 a[132:135], v213 offset:12480\n"
    "v_mfma_f32_16x16x32_bf16 v[160:163], a[88:91], a[8:11], v[160:163]\n"
    "v_exp_f32 v173, v173\n"
    "v_mfma_f32_16x16x32_bf16 v[160:163], a[92:95], a[12:15], v[160:163]\n"
    "ds_read_b128 a[128:131], v212 offset:12480\n"
    "v_cvt_pk_bf16_f32 v188, v168, v169\n"
    "v_mfma_f32_16x16x32_bf16 v[164:167], a[108:111], a[12:15], -4.0\n"
    "v_exp_f32 v174, v174\n"
    "v_mfma_f32_16x16x32_bf16 v[164:167], a[104:107], a[8:11], v[164:167]\n"
    "v_cvt_pk_bf16_f32 v189, v170, v171\n"
    "v_cvt_pk_bf16_f32 v190, v172, v173\n"
    "v_mfma_f32_16x16x32_bf16 v[164:167], a[100:103], a[4:7], v[164:167]\n"
    "v_exp_f32 v175, v175\n"
    "v_mfma_f32_16x16x32_bf16 v[164:167], a[96:99], a[0:3], v[164:167]\n"
    "v_cvt_pk_bf16_f32 v191, v174, v175\n"
    "v_mfma_f32_16x16x32_bf16 v[168:171], a[112:115], a[0:3], -4.0\n"
    "s_waitcnt lgkmcnt(3)\n"
    "v_mfma_f32_16x16x32_bf16 v[168:171], a[116:119], a[4:7], v[168:171]\n"
    "v_exp_f32 v160, v160\n"
    "v_mfma_f32_16x16x32_bf16 v[168:171], a[120:123], a[8:11], v[168:171]\n"
    "v_mfma_f32_16x16x32_bf16 v[168:171], a[124:127], a[12:15], v[168:171]\n"
    "v_exp_f32 v161, v161\n"
    "v_mfma_f32_16x16x32_bf16 v[172:175], a[140:143], a[12:15], -4.0\n"
    "s_waitcnt lgkmcnt(0)\n"
    "v_cvt_pk_bf16_f32 v176, v160, v161\n"
    "v_mfma_f32_16x16x32_bf16 v[172:175], a[136:139], a[8:11], v[172:175]\n"
    "v_exp_f32 v162, v162\n"
    "v_mfma_f32_16x16x32_bf16 v[172:175], a[132:135], a[4:7], v[172:175]\n"
    "v_mfma_f32_16x16x32_bf16 v[172:175], a[128:131], a[0:3], v[172:175]\n"
    "v_exp_f32 v163, v163\n"
    "v_smfmac_f32_16x16x64_bf16 v[208:211], a[240:243], v[184:191], v237\n"
    "v_cvt_pk_bf16_f32 v177, v162, v163\n"
    "v_mfma_f32_16x16x32_bf16 v[128:131], a[208:211], v[188:191], v[128:131]\n"
    "v_exp_f32 v164, v164\n"
    "v_mfma_f32_16x16x32_bf16 v[128:131], a[176:179], v[184:187], v[128:131]\n"
    "ds_read_b64_tr_b16 a[208:209], v216 offset:49792\n"
    "ds_read_b64_tr_b16 a[210:211], v216 offset:53920\n"
    "v_mfma_f32_16x16x32_bf16 v[132:135], a[180:183], v[184:187], v[132:135]\n"
    "v_exp_f32 v165, v165\n"
    "v_mfma_f32_16x16x32_bf16 v[132:135], a[212:215], v[188:191], v[132:135]\n"
    "ds_read_b64_tr_b16 a[212:213], v216 offset:49800\n"
    "ds_read_b64_tr_b16 a[214:215], v216 offset:53928\n"
    "v_mfma_f32_16x16x32_bf16 v[136:139], a[216:219], v[188:191], v[136:139]\n"
    "v_exp_f32 v166, v166\n"
    "v_mfma_f32_16x16x32_bf16 v[136:139], a[184:187], v[184:187], v[136:139]\n"
    "ds_read_b64_tr_b16 a[216:217], v217 offset:49792\n"
    "v_cvt_pk_bf16_f32 v178, v164, v165\n"
    "v_mfma_f32_16x16x32_bf16 v[140:143], a[188:191], v[184:187], v[140:143]\n"
    "v_exp_f32 v167, v167\n"
    "v_mfma_f32_16x16x32_bf16 v[140:143], a[220:223], v[188:191], v[140:143]\n"
    "ds_read_b64_tr_b16 a[218:219], v217 offset:53920\n"
    "v_cvt_pk_bf16_f32 v179, v166, v167\n"
    "v_mfma_f32_16x16x32_bf16 v[144:147], a[224:227], v[188:191], v[144:147]\n"
    "v_exp_f32 v168, v168\n"
    "v_mfma_f32_16x16x32_bf16 v[144:147], a[192:195], v[184:187], v[144:147]\n"
    "ds_read_b64_tr_b16 a[220:221], v217 offset:49800\n"
    "ds_read_b64_tr_b16 a[222:223], v217 offset:53928\n"
    "v_mfma_f32_16x16x32_bf16 v[148:151], a[196:199], v[184:187], v[148:151]\n"
    "v_exp_f32 v169, v169\n"
    "v_mfma_f32_16x16x32_bf16 v[148:151], a[228:231], v[188:191], v[148:151]\n"
    "ds_read_b64_tr_b16 a[224:225], v218 offset:49792\n"
    "v_cvt_pk_bf16_f32 v180, v168, v169\n"
    "v_mfma_f32_16x16x32_bf16 v[152:155], a[232:235], v[188:191], v[152:155]\n"
    "v_exp_f32 v170, v170\n"
    "v_mfma_f32_16x16x32_bf16 v[152:155], a[200:203], v[184:187], v[152:155]\n"
    "ds_read_b64_tr_b16 a[226:227], v218 offset:53920\n"
    "ds_read_b64_tr_b16 a[228:229], v218 offset:49800\n"
    "v_mfma_f32_16x16x32_bf16 v[156:159], a[204:207], v[184:187], v[156:159]\n"
    "v_exp_f32 v171, v171\n"
    "v_mfma_f32_16x16x32_bf16 v[156:159], a[236:239], v[188:191], v[156:159]\n"
    "ds_read_b64_tr_b16 a[230:231], v218 offset:53928\n"
    "v_cvt_pk_bf16_f32 v181, v170, v171\n"
    "v_mfma_f32_16x16x32_bf16 v[160:163], a[80:83], a[16:19], -4.0\n"
    "v_exp_f32 v172, v172\n"
    "v_mfma_f32_16x16x32_bf16 v[160:163], a[84:87], a[20:23], v[160:163]\n"
    "ds_read_b64_tr_b16 a[232:233], v219 offset:49792\n"
    "ds_read_b64_tr_b16 a[234:235], v219 offset:53920\n"
    "v_mfma_f32_16x16x32_bf16 v[160:163], a[88:91], a[24:27], v[160:163]\n"
    "v_exp_f32 v173, v173\n"
    "v_mfma_f32_16x16x32_bf16 v[160:163], a[92:95], a[28:31], v[160:163]\n"
    "ds_read_b64_tr_b16 a[236:237], v219 offset:49800\n"
    "v_cvt_pk_bf16_f32 v182, v172, v173\n"
    "v_mfma_f32_16x16x32_bf16 v[164:167], a[108:111], a[28:31], -4.0\n"
    "v_exp_f32 v174, v174\n"
    "v_mfma_f32_16x16x32_bf16 v[164:167], a[104:107], a[24:27], v[164:167]\n"
    "ds_read_b64_tr_b16 a[238:239], v219 offset:53928\n"
    "v_mfma_f32_16x16x32_bf16 v[164:167], a[100:103], a[20:23], v[164:167]\n"
    "v_exp_f32 v175, v175\n"
    "v_mfma_f32_16x16x32_bf16 v[164:167], a[96:99], a[16:19], v[164:167]\n"
    "v_mfma_f32_16x16x32_bf16 v[168:171], a[112:115], a[16:19], -4.0\n"
    "s_add_u32 s52, s52, 16384\n"
    "v_cvt_pk_bf16_f32 v183, v174, v175\n"
    "v_mfma_f32_16x16x32_bf16 v[168:171], a[116:119], a[20:23], v[168:171]\n"
    "v_exp_f32 v160, v160\n"
    "v_mfma_f32_16x16x32_bf16 v[168:171], a[120:123], a[24:27], v[168:171]\n"
    "s_add_u32 s54, s54, 16384\n"
    "ds_read_b128 a[244:247], v224 offset:12288\n"
    "v_mfma_f32_16x16x32_bf16 v[168:171], a[124:127], a[28:31], v[168:171]\n"
    "v_exp_f32 v161, v161\n"
    "v_mfma_f32_16x16x32_bf16 v[172:175], a[140:143], a[28:31], -4.0\n"
    "s_add_u32 s53, s53, 16384\n"
    "v_mfma_f32_16x16x32_bf16 v[172:175], a[136:139], a[24:27], v[172:175]\n"
    "v_exp_f32 v162, v162\n"
    "v_mfma_f32_16x16x32_bf16 v[172:175], a[132:135], a[20:23], v[172:175]\n"
    "v_mfma_f32_16x16x32_bf16 v[172:175], a[128:131], a[16:19], v[172:175]\n"
    "v_exp_f32 v163, v163\n"
    "v_smfmac_f32_16x16x64_bf16 v[192:195], a[240:243], v[176:183], v237\n"
    "s_waitcnt lgkmcnt(1)\n"
    "v_mfma_f32_16x16x32_bf16 v[0:3], a[208:211], v[180:183], v[0:3]\n"
    "v_exp_f32 v164, v164\n"
    "v_mfma_f32_16x16x32_bf16 v[0:3], a[144:147], v[176:179], v[0:3]\n"
    "s_add_u32 s56, s64, 8192\n"
    "v_mfma_f32_16x16x32_bf16 v[4:7], a[148:151], v[176:179], v[4:7]\n"
    "s_waitcnt lgkmcnt(0)\n"
    "buffer_store_dwordx4 a[244:247], v228, s[48:51], s56 offen nt\n"
    "v_exp_f32 v165, v165\n"
    "v_mfma_f32_16x16x32_bf16 v[4:7], a[212:215], v[180:183], v[4:7]\n"
    "v_cvt_pk_bf16_f32 v184, v160, v161\n"
    "v_cvt_pk_bf16_f32 v185, v162, v163\n"
    "v_mfma_f32_16x16x32_bf16 v[8:11], a[216:219], v[180:183], v[8:11]\n"
    "v_exp_f32 v166, v166\n"
    "v_mfma_f32_16x16x32_bf16 v[8:11], a[152:155], v[176:179], v[8:11]\n"
    "v_mfma_f32_16x16x32_bf16 v[12:15], a[156:159], v[176:179], v[12:15]\n"
    "s_waitcnt vmcnt(12)\n"
    "v_exp_f32 v167, v167\n"
    "v_mfma_f32_16x16x32_bf16 v[12:15], a[220:223], v[180:183], v[12:15]\n"
    "s_barrier\n"
    "v_mfma_f32_16x16x32_bf16 v[16:19], a[224:227], v[180:183], v[16:19]\n"
    "s_add_u32 m0, s57, 0\n"
    "v_exp_f32 v168, v168\n"
    "v_mfma_f32_16x16x32_bf16 v[16:19], a[160:163], v[176:179], v[16:19]\n"
    "buffer_load_dwordx4 v229, s[36:39], s52 offen lds\n"
    "v_mfma_f32_16x16x32_bf16 v[20:23], a[164:167], v[176:179], v[20:23]\n"
    "v_exp_f32 v169, v169\n"
    "v_mfma_f32_16x16x32_bf16 v[20:23], a[228:231], v[180:183], v[20:23]\n"
    "v_cvt_pk_bf16_f32 v186, v164, v165\n"
    "v_mfma_f32_16x16x32_bf16 v[24:27], a[232:235], v[180:183], v[24:27]\n"
    "v_exp_f32 v170, v170\n"
    "v_mfma_f32_16x16x32_bf16 v[24:27], a[168:171], v[176:179], v[24:27]\n"
    "v_cvt_pk_bf16_f32 v187, v166, v167\n"
    "v_cvt_pk_bf16_f32 v188, v168, v169\n"
    "v_mfma_f32_16x16x32_bf16 v[28:31], a[172:175], v[176:179], v[28:31]\n"
    "v_exp_f32 v171, v171\n"
    "v_mfma_f32_16x16x32_bf16 v[28:31], a[236:239], v[180:183], v[28:31]\n"
    "v_cvt_pk_bf16_f32 v189, v170, v171\n"
    "v_mfma_f32_16x16x32_bf16 v[160:163], a[80:83], a[32:35], -4.0\n"
    "v_exp_f32 v172, v172\n"
    "v_mfma_f32_16x16x32_bf16 v[160:163], a[84:87], a[36:39], v[160:163]\n"
    "v_mfma_f32_16x16x32_bf16 v[160:163], a[88:91], a[40:43], v[160:163]\n"
    "s_add_u32 m0, s57, 0\n"
    "v_exp_f32 v173, v173\n"
    "v_mfma_f32_16x16x32_bf16 v[160:163], a[92:95], a[44:47], v[160:163]\n"
    "buffer_load_dwordx4 v229, s[36:39], s52 offen offset:1024 lds\n"
    "v_mfma_f32_16x16x32_bf16 v[164:167], a[108:111], a[44:47], -4.0\n"
    "v_exp_f32 v174, v174\n"
    "v_mfma_f32_16x16x32_bf16 v[164:167], a[104:107], a[40:43], v[164:167]\n"
    "v_cvt_pk_bf16_f32 v190, v172, v173\n"
    "v_mfma_f32_16x16x32_bf16 v[164:167], a[100:103], a[36:39], v[164:167]\n"
    "v_exp_f32 v175, v175\n"
    "v_mfma_f32_16x16x32_bf16 v[164:167], a[96:99], a[32:35], v[164:167]\n"
    "v_cvt_pk_bf16_f32 v191, v174, v175\n"
    "v_mfma_f32_16x16x32_bf16 v[168:171], a[112:115], a[32:35], -4.0\n"
    "v_mfma_f32_16x16x32_bf16 v[168:171], a[116:119], a[36:39], v[168:171]\n"
    "v_exp_f32 v160, v160\n"
    "v_mfma_f32_16x16x32_bf16 v[168:171], a[120:123], a[40:43], v[168:171]\n"
    "v_mfma_f32_16x16x32_bf16 v[168:171], a[124:127], a[44:47], v[168:171]\n"
    "s_add_u32 m0, s57, 32\n"
    "v_exp_f32 v161, v161\n"
    "v_mfma_f32_16x16x32_bf16 v[172:175], a[140:143], a[44:47], -4.0\n"
    "buffer_load_dwordx4 v229, s[36:39], s52 offen offset:2048 lds\n"
    "v_mfma_f32_16x16x32_bf16 v[172:175], a[136:139], a[40:43], v[172:175]\n"
    "v_exp_f32 v162, v162\n"
    "v_mfma_f32_16x16x32_bf16 v[172:175], a[132:135], a[36:39], v[172:175]\n"
    "v_cvt_pk_bf16_f32 v176, v160, v161\n"
    "v_mfma_f32_16x16x32_bf16 v[172:175], a[128:131], a[32:35], v[172:175]\n"
    "v_exp_f32 v163, v163\n"
    "v_smfmac_f32_16x16x64_bf16 v[196:199], a[240:243], v[184:191], v237\n"
    "ds_read_b64_tr_b16 a[176:177], v216 offset:41536\n"
    "v_cvt_pk_bf16_f32 v177, v162, v163\n"
    "v_mfma_f32_16x16x32_bf16 v[32:35], a[208:211], v[188:191], v[32:35]\n"
    "v_exp_f32 v164, v164\n"
    "v_mfma_f32_16x16x32_bf16 v[32:35], a[144:147], v[184:187], v[32:35]\n"
    "v_mfma_f32_16x16x32_bf16 v[36:39], a[148:151], v[184:187], v[36:39]\n"
    "s_add_u32 m0, s57, 32\n"
    "v_exp_f32 v165, v165\n"
    "v_mfma_f32_16x16x32_bf16 v[36:39], a[212:215], v[188:191], v[36:39]\n"
    "buffer_load_dwordx4 v229, s[36:39], s52 offen offset:3072 lds\n"
    "v_mfma_f32_16x16x32_bf16 v[40:43], a[216:219], v[188:191], v[40:43]\n"
    "v_exp_f32 v166, v166\n"
    "v_mfma_f32_16x16x32_bf16 v[40:43], a[152:155], v[184:187], v[40:43]\n"
    "v_mfma_f32_16x16x32_bf16 v[44:47], a[156:159], v[184:187], v[44:47]\n"
    "v_exp_f32 v167, v167\n"
    "v_mfma_f32_16x16x32_bf16 v[44:47], a[220:223], v[188:191], v[44:47]\n"
    "v_mfma_f32_16x16x32_bf16 v[48:51], a[224:227], v[188:191], v[48:51]\n"
    "v_exp_f32 v168, v168\n"
    "v_mfma_f32_16x16x32_bf16 v[48:51], a[160:163], v[184:187], v[48:51]\n"
    "v_cvt_pk_bf16_f32 v178, v164, v165\n"
    "v_cvt_pk_bf16_f32 v179, v166, v167\n"
    "v_mfma_f32_16x16x32_bf16 v[52:55], a[164:167], v[184:187], v[52:55]\n"
    "v_exp_f32 v169, v169\n"
    "v_mfma_f32_16x16x32_bf16 v[52:55], a[228:231], v[188:191], v[52:55]\n"
    "ds_read_b64_tr_b16 a[178:179], v216 offset:45664\n"
    "ds_read_b64_tr_b16 a[180:181], v216 offset:41544\n"
    "v_mfma_f32_16x16x32_bf16 v[56:59], a[232:235], v[188:191], v[56:59]\n"
    "s_add_u32 m0, s58, 16512\n"
    "v_exp_f32 v170, v170\n"
    "v_mfma_f32_16x16x32_bf16 v[56:59], a[168:171], v[184:187], v[56:59]\n"
    "buffer_load_dwordx4 v229, s[40:43], s54 offen lds\n"
    "v_mfma_f32_16x16x32_bf16 v[60:63], a[172:175], v[184:187], v[60:63]\n"
    "v_exp_f32 v171, v171\n"
    "v_mfma_f32_16x16x32_bf16 v[60:63], a[236:239], v[188:191], v[60:63]\n"
    "v_cvt_pk_bf16_f32 v180, v168, v169\n"
    "v_cvt_pk_bf16_f32 v181, v170, v171\n"
    "v_mfma_f32_16x16x32_bf16 v[160:163], a[80:83], a[48:51], -4.0\n"
    "v_exp_f32 v172, v172\n"
    "v_mfma_f32_16x16x32_bf16 v[160:163], a[84:87], a[52:55], v[160:163]\n"
    "ds_read_b64_tr_b16 a[182:183], v216 offset:45672\n"
    "ds_read_b64_tr_b16 a[184:185], v217 offset:41536\n"
    "v_mfma_f32_16x16x32_bf16 v[160:163], a[88:91], a[56:59], v[160:163]\n"
    "v_exp_f32 v173, v173\n"
    "v_mfma_f32_16x16x32_bf16 v[160:163], a[92:95], a[60:63], v[160:163]\n"
    "ds_read_b128 a[248:251], v225 offset:13312\n"
    "v_cvt_pk_bf16_f32 v182, v172, v173\n"
    "v_mfma_f32_16x16x32_bf16 v[164:167], a[108:111], a[60:63], -4.0\n"
    "s_add_u32 m0, s58, 16520\n"
    "v_exp_f32 v174, v174\n"
    "v_mfma_f32_16x16x32_bf16 v[164:167], a[104:107], a[56:59], v[164:167]\n"
    "buffer_load_dwordx4 v229, s[40:43], s54 offen offset:1024 lds\n"
    "v_mfma_f32_16x16x32_bf16 v[164:167], a[100:103], a[52:55], v[164:167]\n"
    "v_exp_f32 v175, v175\n"
    "v_mfma_f32_16x16x32_bf16 v[164:167], a[96:99], a[48:51], v[164:167]\n"
    "ds_read_b64_tr_b16 a[186:187], v217 offset:45664\n"
    "v_cvt_pk_bf16_f32 v183, v174, v175\n"
    "v_mfma_f32_16x16x32_bf16 v[168:171], a[112:115], a[48:51], -4.0\n"
    "ds_read_b64_tr_b16 a[188:189], v217 offset:41544\n"
    "v_mfma_f32_16x16x32_bf16 v[168:171], a[116:119], a[52:55], v[168:171]\n"
    "v_exp_f32 v160, v160\n"
    "v_mfma_f32_16x16x32_bf16 v[168:171], a[120:123], a[56:59], v[168:171]\n"
    "ds_read_b64_tr_b16 a[190:191], v217 offset:45672\n"
    "ds_read_b64_tr_b16 a[192:193], v218 offset:41536\n"
    "v_mfma_f32_16x16x32_bf16 v[168:171], a[124:127], a[60:63], v[168:171]\n"
    "v_exp_f32 v161, v161\n"
    "v_mfma_f32_16x16x32_bf16 v[172:175], a[140:143], a[60:63], -4.0\n"
    "ds_read_b64_tr_b16 a[194:195], v218 offset:45664\n"
    "ds_read_b64_tr_b16 a[196:197], v218 offset:41544\n"
    "v_mfma_f32_16x16x32_bf16 v[172:175], a[136:139], a[56:59], v[172:175]\n"
    "s_waitcnt lgkmcnt(6)\n"
    "buffer_store_dwordx4 a[248:251], v228, s[48:51], s56 offen offset:1024 nt\n"
    "v_exp_f32 v162, v162\n"
    "v_mfma_f32_16x16x32_bf16 v[172:175], a[132:135], a[52:55], v[172:175]\n"
    "ds_read_b64_tr_b16 a[198:199], v218 offset:45672\n"
    "ds_read_b64_tr_b16 a[200:201], v219 offset:41536\n"
    "v_mfma_f32_16x16x32_bf16 v[172:175], a[128:131], a[48:51], v[172:175]\n"
    "v_exp_f32 v163, v163\n"
    "v_smfmac_f32_16x16x64_bf16 v[200:203], a[240:243], v[176:183], v237\n"
    "ds_read_b64_tr_b16 a[202:203], v219 offset:45664\n"
    "v_mfma_f32_16x16x32_bf16 v[64:67], a[208:211], v[180:183], v[64:67]\n"
    "v_exp_f32 v164, v164\n"
    "v_mfma_f32_16x16x32_bf16 v[64:67], a[144:147], v[176:179], v[64:67]\n"
    "ds_read_b64_tr_b16 a[204:205], v219 offset:41544\n"
    "v_mfma_f32_16x16x32_bf16 v[68:71], a[148:151], v[176:179], v[68:71]\n"
    "v_exp_f32 v165, v165\n"
    "v_mfma_f32_16x16x32_bf16 v[68:71], a[212:215], v[180:183], v[68:71]\n"
    "ds_read_b64_tr_b16 a[206:207], v219 offset:45672\n"
    "v_cvt_pk_bf16_f32 v184, v160, v161\n"
    "v_mfma_f32_16x16x32_bf16 v[72:75], a[216:219], v[180:183], v[72:75]\n"
    "v_exp_f32 v166, v166\n"
    "v_mfma_f32_16x16x32_bf16 v[72:75], a[152:155], v[176:179], v[72:75]\n"
    "v_cvt_pk_bf16_f32 v185, v162, v163\n"
    "v_cvt_pk_bf16_f32 v186, v164, v165\n"
    "v_mfma_f32_16x16x32_bf16 v[76:79], a[156:159], v[176:179], v[76:79]\n"
    "v_exp_f32 v167, v167\n"
    "v_mfma_f32_16x16x32_bf16 v[76:79], a[220:223], v[180:183], v[76:79]\n"
    "v_cvt_pk_bf16_f32 v187, v166, v167\n"
    "v_mfma_f32_16x16x32_bf16 v[80:83], a[224:227], v[180:183], v[80:83]\n"
    "v_exp_f32 v168, v168\n"
    "v_mfma_f32_16x16x32_bf16 v[80:83], a[160:163], v[176:179], v[80:83]\n"
    "v_mfma_f32_16x16x32_bf16 v[84:87], a[164:167], v[176:179], v[84:87]\n"
    "v_exp_f32 v169, v169\n"
    "v_mfma_f32_16x16x32_bf16 v[84:87], a[228:231], v[180:183], v[84:87]\n"
    "v_cvt_pk_bf16_f32 v188, v168, v169\n"
    "v_mfma_f32_16x16x32_bf16 v[88:91], a[232:235], v[180:183], v[88:91]\n"
    "v_exp_f32 v170, v170\n"
    "v_mfma_f32_16x16x32_bf16 v[88:91], a[168:171], v[176:179], v[88:91]\n"
    "v_mfma_f32_16x16x32_bf16 v[92:95], a[172:175], v[176:179], v[92:95]\n"
    "v_exp_f32 v171, v171\n"
    "v_mfma_f32_16x16x32_bf16 v[92:95], a[236:239], v[180:183], v[92:95]\n"
    "v_cvt_pk_bf16_f32 v189, v170, v171\n"
    "v_mfma_f32_16x16x32_bf16 v[160:163], a[80:83], a[64:67], -4.0\n"
    "v_exp_f32 v172, v172\n"
    "v_mfma_f32_16x16x32_bf16 v[160:163], a[84:87], a[68:71], v[160:163]\n"
    "v_mfma_f32_16x16x32_bf16 v[160:163], a[88:91], a[72:75], v[160:163]\n"
    "v_exp_f32 v173, v173\n"
    "v_mfma_f32_16x16x32_bf16 v[160:163], a[92:95], a[76:79], v[160:163]\n"
    "v_cvt_pk_bf16_f32 v190, v172, v173\n"
    "v_mfma_f32_16x16x32_bf16 v[164:167], a[108:111], a[76:79], -4.0\n"
    "v_exp_f32 v174, v174\n"
    "v_mfma_f32_16x16x32_bf16 v[164:167], a[104:107], a[72:75], v[164:167]\n"
    "v_mfma_f32_16x16x32_bf16 v[164:167], a[100:103], a[68:71], v[164:167]\n"
    "v_exp_f32 v175, v175\n"
    "v_mfma_f32_16x16x32_bf16 v[164:167], a[96:99], a[64:67], v[164:167]\n"
    "s_waitcnt vmcnt(11) lgkmcnt(0)\n"
    "v_mfma_f32_16x16x32_bf16 v[168:171], a[112:115], a[64:67], -4.0\n"
    "s_barrier\n"
    "v_mfma_f32_16x16x32_bf16 v[168:171], a[116:119], a[68:71], v[168:171]\n"
    "v_exp_f32 v160, v160\n"
    "v_mfma_f32_16x16x32_bf16 v[168:171], a[120:123], a[72:75], v[168:171]\n"
    "v_cvt_pk_bf16_f32 v191, v174, v175\n"
    "v_mfma_f32_16x16x32_bf16 v[168:171], a[124:127], a[76:79], v[168:171]\n"
    "v_exp_f32 v161, v161\n"
    "v_mfma_f32_16x16x32_bf16 v[172:175], a[140:143], a[76:79], -4.0\n"
    "ds_read_b128 a[80:83], v212 offset:16640\n"
    "ds_read_b128 a[84:87], v213 offset:16640\n"
    "v_mfma_f32_16x16x32_bf16 v[172:175], a[136:139], a[72:75], v[172:175]\n"
    "v_exp_f32 v162, v162\n"
    "v_mfma_f32_16x16x32_bf16 v[172:175], a[132:135], a[68:71], v[172:175]\n"
    "ds_read_b128 a[88:91], v214 offset:16640\n"
    "ds_read_b128 a[92:95], v215 offset:16640\n"
    "v_mfma_f32_16x16x32_bf16 v[172:175], a[128:131], a[64:67], v[172:175]\n"
    "v_exp_f32 v163, v163\n"
    "v_smfmac_f32_16x16x64_bf16 v[204:207], a[240:243], v[184:191], v237\n"
    "ds_read_b128 a[108:111], v215 offset:20800\n"
    "ds_read_b128 a[104:107], v214 offset:20800\n"
    "v_mfma_f32_16x16x32_bf16 v[96:99], a[208:211], v[188:191], v[96:99]\n"
    "v_exp_f32 v164, v164\n"
    "v_mfma_f32_16x16x32_bf16 v[96:99], a[144:147], v[184:187], v[96:99]\n"
    "ds_read_b128 a[100:103], v213 offset:20800\n"
    "ds_read_b128 a[96:99], v212 offset:20800\n"
    "v_mfma_f32_16x16x32_bf16 v[100:103], a[148:151], v[184:187], v[100:103]\n"
    "v_exp_f32 v165, v165\n"
    "v_mfma_f32_16x16x32_bf16 v[100:103], a[212:215], v[188:191], v[100:103]\n"
    "v_cvt_pk_bf16_f32 v176, v160, v161\n"
    "v_cvt_pk_bf16_f32 v177, v162, v163\n"
    "v_mfma_f32_16x16x32_bf16 v[104:107], a[216:219], v[188:191], v[104:107]\n"
    "s_add_u32 m0, s58, 8256\n"
    "v_exp_f32 v166, v166\n"
    "v_mfma_f32_16x16x32_bf16 v[104:107], a[152:155], v[184:187], v[104:107]\n"
    "buffer_load_dwordx4 v229, s[40:43], s53 offen lds\n"
    "v_mfma_f32_16x16x32_bf16 v[108:111], a[156:159], v[184:187], v[108:111]\n"
    "v_exp_f32 v167, v167\n"
    "v_mfma_f32_16x16x32_bf16 v[108:111], a[220:223], v[188:191], v[108:111]\n"
    "ds_read_b128 a[112:115], v212 offset:24960\n"
    "v_mfma_f32_16x16x32_bf16 v[112:115], a[224:227], v[188:191], v[112:115]\n"
    "s_add_u32 m0, s58, 8264\n"
    "v_exp_f32 v168, v168\n"
    "v_mfma_f32_16x16x32_bf16 v[112:115], a[160:163], v[184:187], v[112:115]\n"
    "buffer_load_dwordx4 v229, s[40:43], s53 offen offset:1024 lds\n"
    "v_mfma_f32_16x16x32_bf16 v[116:119], a[164:167], v[184:187], v[116:119]\n"
    "v_exp_f32 v169, v169\n"
    "v_mfma_f32_16x16x32_bf16 v[116:119], a[228:231], v[188:191], v[116:119]\n"
    "ds_read_b128 a[116:119], v213 offset:24960\n"
    "ds_read_b128 a[120:123], v214 offset:24960\n"
    "v_mfma_f32_16x16x32_bf16 v[120:123], a[232:235], v[188:191], v[120:123]\n"
    "v_exp_f32 v170, v170\n"
    "v_mfma_f32_16x16x32_bf16 v[120:123], a[168:171], v[184:187], v[120:123]\n"
    "ds_read_b128 a[252:255], v226 offset:14336\n"
    "ds_read_b128 a[124:127], v215 offset:24960\n"
    "v_mfma_f32_16x16x32_bf16 v[124:127], a[172:175], v[184:187], v[124:127]\n"
    "v_exp_f32 v171, v171\n"
    "v_mfma_f32_16x16x32_bf16 v[124:127], a[236:239], v[188:191], v[124:127]\n"
    "s_waitcnt lgkmcnt(4)\n"
    "v_cvt_pk_bf16_f32 v178, v164, v165\n"
    "v_cvt_pk_bf16_f32 v179, v166, v167\n"
    "v_mfma_f32_16x16x32_bf16 v[160:163], a[80:83], a[0:3], -4.0\n"
    "v_exp_f32 v172, v172\n"
    "v_mfma_f32_16x16x32_bf16 v[160:163], a[84:87], a[4:7], v[160:163]\n"
    "ds_read_b128 a[140:143], v215 offset:29120\n"
    "ds_read_b128 a[136:139], v214 offset:29120\n"
    "v_mfma_f32_16x16x32_bf16 v[160:163], a[88:91], a[8:11], v[160:163]\n"
    "v_exp_f32 v173, v173\n"
    "v_mfma_f32_16x16x32_bf16 v[160:163], a[92:95], a[12:15], v[160:163]\n"
    "ds_read_b128 a[132:135], v213 offset:29120\n"
    "ds_read_b128 a[128:131], v212 offset:29120\n"
    "v_mfma_f32_16x16x32_bf16 v[164:167], a[108:111], a[12:15], -4.0\n"
    "v_exp_f32 v174, v174\n"
    "v_mfma_f32_16x16x32_bf16 v[164:167], a[104:107], a[8:11], v[164:167]\n"
    "v_cvt_pk_bf16_f32 v180, v168, v169\n"
    "v_cvt_pk_bf16_f32 v181, v170, v171\n"
    "v_mfma_f32_16x16x32_bf16 v[164:167], a[100:103], a[4:7], v[164:167]\n"
    "s_waitcnt lgkmcnt(4)\n"
    "buffer_store_dwordx4 a[252:255], v228, s[48:51], s56 offen offset:2048 nt\n"
    "v_exp_f32 v175, v175\n"
    "v_mfma_f32_16x16x32_bf16 v[164:167], a[96:99], a[0:3], v[164:167]\n"
    "v_cvt_pk_bf16_f32 v182, v172, v173\n"
    "v_cvt_pk_bf16_f32 v183, v174, v175\n"
    "v_mfma_f32_16x16x32_bf16 v[168:171], a[112:115], a[0:3], -4.0\n"
    "v_mfma_f32_16x16x32_bf16 v[168:171], a[116:119], a[4:7], v[168:171]\n"
    "v_exp_f32 v160, v160\n"
    "v_mfma_f32_16x16x32_bf16 v[168:171], a[120:123], a[8:11], v[168:171]\n"
    "v_mfma_f32_16x16x32_bf16 v[168:171], a[124:127], a[12:15], v[168:171]\n"
    "s_waitcnt lgkmcnt(0)\n"
    "v_exp_f32 v161, v161\n"
    "v_mfma_f32_16x16x32_bf16 v[172:175], a[140:143], a[12:15], -4.0\n"
    "v_cvt_pk_bf16_f32 v184, v160, v161\n"
    "v_mfma_f32_16x16x32_bf16 v[172:175], a[136:139], a[8:11], v[172:175]\n"
    "v_exp_f32 v162, v162\n"
    "v_mfma_f32_16x16x32_bf16 v[172:175], a[132:135], a[4:7], v[172:175]\n"
    "v_mfma_f32_16x16x32_bf16 v[172:175], a[128:131], a[0:3], v[172:175]\n"
    "v_exp_f32 v163, v163\n"
    "v_smfmac_f32_16x16x64_bf16 v[208:211], a[240:243], v[176:183], v237\n"
    "v_cvt_pk_bf16_f32 v185, v162, v163\n"
    "v_mfma_f32_16x16x32_bf16 v[128:131], a[208:211], v[180:183], v[128:131]\n"
    "v_exp_f32 v164, v164\n"
    "v_mfma_f32_16x16x32_bf16 v[128:131], a[144:147], v[176:179], v[128:131]\n"
    "ds_read_b64_tr_b16 a[208:209], v216 offset:58048\n"
    "v_mfma_f32_16x16x32_bf16 v[132:135], a[148:151], v[176:179], v[132:135]\n"
    "v_exp_f32 v165, v165\n"
    "v_mfma_f32_16x16x32_bf16 v[132:135], a[212:215], v[180:183], v[132:135]\n"
    "v_cvt_pk_bf16_f32 v186, v164, v165\n"
    "v_mfma_f32_16x16x32_bf16 v[136:139], a[216:219], v[180:183], v[136:139]\n"
    "v_exp_f32 v166, v166\n"
    "v_mfma_f32_16x16x32_bf16 v[136:139], a[152:155], v[176:179], v[136:139]\n"
    "ds_read_b64_tr_b16 a[210:211], v216 offset:62176\n"
    "ds_read_b64_tr_b16 a[212:213], v216 offset:58056\n"
    "v_mfma_f32_16x16x32_bf16 v[140:143], a[156:159], v[176:179], v[140:143]\n"
    "v_exp_f32 v167, v167\n"
    "v_mfma_f32_16x16x32_bf16 v[140:143], a[220:223], v[180:183], v[140:143]\n"
    "v_mfma_f32_16x16x32_bf16 v[144:147], a[224:227], v[180:183], v[144:147]\n"
    "v_exp_f32 v168, v168\n"
    "v_mfma_f32_16x16x32_bf16 v[144:147], a[160:163], v[176:179], v[144:147]\n"
    "ds_read_b64_tr_b16 a[214:215], v216 offset:62184\n"
    "v_cvt_pk_bf16_f32 v187, v166, v167\n"
    "v_mfma_f32_16x16x32_bf16 v[148:151], a[164:167], v[176:179], v[148:151]\n"
    "v_exp_f32 v169, v169\n"
    "v_mfma_f32_16x16x32_bf16 v[148:151], a[228:231], v[180:183], v[148:151]\n"
    "ds_read_b64_tr_b16 a[216:217], v217 offset:58048\n"
    "ds_read_b64_tr_b16 a[218:219], v217 offset:62176\n"
    "v_mfma_f32_16x16x32_bf16 v[152:155], a[232:235], v[180:183], v[152:155]\n"
    "v_exp_f32 v170, v170\n"
    "v_mfma_f32_16x16x32_bf16 v[152:155], a[168:171], v[176:179], v[152:155]\n"
    "ds_read_b64_tr_b16 a[220:221], v217 offset:58056\n"
    "ds_read_b64_tr_b16 a[222:223], v217 offset:62184\n"
    "v_mfma_f32_16x16x32_bf16 v[156:159], a[172:175], v[176:179], v[156:159]\n"
    "v_exp_f32 v171, v171\n"
    "v_mfma_f32_16x16x32_bf16 v[156:159], a[236:239], v[180:183], v[156:159]\n"
    "v_cvt_pk_bf16_f32 v188, v168, v169\n"
    "v_cvt_pk_bf16_f32 v189, v170, v171\n"
    "v_mfma_f32_16x16x32_bf16 v[160:163], a[80:83], a[16:19], -4.0\n"
    "v_exp_f32 v172, v172\n"
    "v_mfma_f32_16x16x32_bf16 v[160:163], a[84:87], a[20:23], v[160:163]\n"
    "ds_read_b64_tr_b16 a[224:225], v218 offset:58048\n"
    "ds_read_b64_tr_b16 a[226:227], v218 offset:62176\n"
    "v_mfma_f32_16x16x32_bf16 v[160:163], a[88:91], a[24:27], v[160:163]\n"
    "v_exp_f32 v173, v173\n"
    "v_mfma_f32_16x16x32_bf16 v[160:163], a[92:95], a[28:31], v[160:163]\n"
    "ds_read_b64_tr_b16 a[228:229], v218 offset:58056\n"
    "ds_read_b64_tr_b16 a[230:231], v218 offset:62184\n"
    "v_mfma_f32_16x16x32_bf16 v[164:167], a[108:111], a[28:31], -4.0\n"
    "v_exp_f32 v174, v174\n"
    "v_mfma_f32_16x16x32_bf16 v[164:167], a[104:107], a[24:27], v[164:167]\n"
    "ds_read_b64_tr_b16 a[232:233], v219 offset:58048\n"
    "v_cvt_pk_bf16_f32 v190, v172, v173\n"
    "v_mfma_f32_16x16x32_bf16 v[164:167], a[100:103], a[20:23], v[164:167]\n"
    "v_exp_f32 v175, v175\n"
    "v_mfma_f32_16x16x32_bf16 v[164:167], a[96:99], a[16:19], v[164:167]\n"
    "ds_read_b64_tr_b16 a[234:235], v219 offset:62176\n"
    "v_cvt_pk_bf16_f32 v191, v174, v175\n"
    "v_mfma_f32_16x16x32_bf16 v[168:171], a[112:115], a[16:19], -4.0\n"
    "s_add_u32 s52, s52, 16384\n"
    "ds_read_b64_tr_b16 a[236:237], v219 offset:58056\n"
    "ds_read_b64_tr_b16 a[238:239], v219 offset:62184\n"
    "v_mfma_f32_16x16x32_bf16 v[168:171], a[116:119], a[20:23], v[168:171]\n"
    "v_exp_f32 v160, v160\n"
    "v_mfma_f32_16x16x32_bf16 v[168:171], a[120:123], a[24:27], v[168:171]\n"
    "s_add_u32 s54, s54, 16384\n"
    "v_mfma_f32_16x16x32_bf16 v[168:171], a[124:127], a[28:31], v[168:171]\n"
    "v_exp_f32 v161, v161\n"
    "v_mfma_f32_16x16x32_bf16 v[172:175], a[140:143], a[28:31], -4.0\n"
    "s_add_u32 s53, s62, s60\n"
    "v_mfma_f32_16x16x32_bf16 v[172:175], a[136:139], a[24:27], v[172:175]\n"
    "v_exp_f32 v162, v162\n"
    "v_mfma_f32_16x16x32_bf16 v[172:175], a[132:135], a[20:23], v[172:175]\n"
    "ds_read_b128 a[244:247], v227 offset:15360\n"
    "v_mfma_f32_16x16x32_bf16 v[172:175], a[128:131], a[16:19], v[172:175]\n"
    "v_exp_f32 v163, v163\n"
    "v_smfmac_f32_16x16x64_bf16 v[192:195], a[240:243], v[184:191], v237\n"
    "s_waitcnt lgkmcnt(1)\n"
    "v_cvt_pk_bf16_f32 v176, v160, v161\n"
    "v_mfma_f32_16x16x32_bf16 v[0:3], a[208:211], v[188:191], v[0:3]\n"
    "v_exp_f32 v164, v164\n"
    "v_mfma_f32_16x16x32_bf16 v[0:3], a[176:179], v[184:187], v[0:3]\n"
    "v_cvt_pk_bf16_f32 v177, v162, v163\n"
    "v_mfma_f32_16x16x32_bf16 v[4:7], a[180:183], v[184:187], v[4:7]\n"
    "v_exp_f32 v165, v165\n"
    "v_mfma_f32_16x16x32_bf16 v[4:7], a[212:215], v[188:191], v[4:7]\n"
    "v_cvt_pk_bf16_f32 v178, v164, v165\n"
    "v_mfma_f32_16x16x32_bf16 v[8:11], a[216:219], v[188:191], v[8:11]\n"
    "v_exp_f32 v166, v166\n"
    "v_mfma_f32_16x16x32_bf16 v[8:11], a[184:187], v[184:187], v[8:11]\n"
    "v_mfma_f32_16x16x32_bf16 v[12:15], a[188:191], v[184:187], v[12:15]\n"
    "s_waitcnt vmcnt(11) lgkmcnt(0)\n"
    "buffer_store_dwordx4 a[244:247], v228, s[48:51], s56 offen offset:3072 nt\n"
    "v_exp_f32 v167, v167\n"
    "v_mfma_f32_16x16x32_bf16 v[12:15], a[220:223], v[188:191], v[12:15]\n"
    "s_barrier\n"
    "v_mfma_f32_16x16x32_bf16 v[16:19], a[224:227], v[188:191], v[16:19]\n"
    "s_add_u32 m0, s57, 16640\n"
    "v_exp_f32 v168, v168\n"
    "v_mfma_f32_16x16x32_bf16 v[16:19], a[192:195], v[184:187], v[16:19]\n"
    "buffer_load_dwordx4 v229, s[36:39], s52 offen lds\n"
    "v_mfma_f32_16x16x32_bf16 v[20:23], a[196:199], v[184:187], v[20:23]\n"
    "v_exp_f32 v169, v169\n"
    "v_mfma_f32_16x16x32_bf16 v[20:23], a[228:231], v[188:191], v[20:23]\n"
    "v_cvt_pk_bf16_f32 v179, v166, v167\n"
    "v_cvt_pk_bf16_f32 v180, v168, v169\n"
    "v_mfma_f32_16x16x32_bf16 v[24:27], a[232:235], v[188:191], v[24:27]\n"
    "v_exp_f32 v170, v170\n"
    "v_mfma_f32_16x16x32_bf16 v[24:27], a[200:203], v[184:187], v[24:27]\n"
    "v_mfma_f32_16x16x32_bf16 v[28:31], a[204:207], v[184:187], v[28:31]\n"
    "v_exp_f32 v171, v171\n"
    "v_mfma_f32_16x16x32_bf16 v[28:31], a[236:239], v[188:191], v[28:31]\n"
    "v_cvt_pk_bf16_f32 v181, v170, v171\n"
    "v_mfma_f32_16x16x32_bf16 v[160:163], a[80:83], a[32:35], -4.0\n"
    "v_exp_f32 v172, v172\n"
    "v_mfma_f32_16x16x32_bf16 v[160:163], a[84:87], a[36:39], v[160:163]\n"
    "v_mfma_f32_16x16x32_bf16 v[160:163], a[88:91], a[40:43], v[160:163]\n"
    "s_add_u32 m0, s57, 16640\n"
    "v_exp_f32 v173, v173\n"
    "v_mfma_f32_16x16x32_bf16 v[160:163], a[92:95], a[44:47], v[160:163]\n"
    "buffer_load_dwordx4 v229, s[36:39], s52 offen offset:1024 lds\n"
    "v_mfma_f32_16x16x32_bf16 v[164:167], a[108:111], a[44:47], -4.0\n"
    "v_exp_f32 v174, v174\n"
    "v_mfma_f32_16x16x32_bf16 v[164:167], a[104:107], a[40:43], v[164:167]\n"
    "v_cvt_pk_bf16_f32 v182, v172, v173\n"
    "v_mfma_f32_16x16x32_bf16 v[164:167], a[100:103], a[36:39], v[164:167]\n"
    "v_exp_f32 v175, v175\n"
    "v_mfma_f32_16x16x32_bf16 v[164:167], a[96:99], a[32:35], v[164:167]\n"
    "v_mfma_f32_16x16x32_bf16 v[168:171], a[112:115], a[32:35], -4.0\n"
    "v_cvt_pk_bf16_f32 v183, v174, v175\n"
    "v_mfma_f32_16x16x32_bf16 v[168:171], a[116:119], a[36:39], v[168:171]\n"
    "v_exp_f32 v160, v160\n"
    "v_mfma_f32_16x16x32_bf16 v[168:171], a[120:123], a[40:43], v[168:171]\n"
    "v_mfma_f32_16x16x32_bf16 v[168:171], a[124:127], a[44:47], v[168:171]\n"
    "s_add_u32 m0, s57, 16672\n"
    "v_exp_f32 v161, v161\n"
    "v_mfma_f32_16x16x32_bf16 v[172:175], a[140:143], a[44:47], -4.0\n"
    "buffer_load_dwordx4 v229, s[36:39], s52 offen offset:2048 lds\n"
    "v_mfma_f32_16x16x32_bf16 v[172:175], a[136:139], a[40:43], v[172:175]\n"
    "v_exp_f32 v162, v162\n"
    "v_mfma_f32_16x16x32_bf16 v[172:175], a[132:135], a[36:39], v[172:175]\n"
    "ds_read_b64_tr_b16 a[144:145], v216 offset:33280\n"
    "v_cvt_pk_bf16_f32 v184, v160, v161\n"
    "v_mfma_f32_16x16x32_bf16 v[172:175], a[128:131], a[32:35], v[172:175]\n"
    "v_exp_f32 v163, v163\n"
    "v_smfmac_f32_16x16x64_bf16 v[196:199], a[240:243], v[176:183], v237\n"
    "ds_read_b64_tr_b16 a[146:147], v216 offset:37408\n"
    "v_cvt_pk_bf16_f32 v185, v162, v163\n"
    "v_mfma_f32_16x16x32_bf16 v[32:35], a[208:211], v[180:183], v[32:35]\n"
    "v_exp_f32 v164, v164\n"
    "v_mfma_f32_16x16x32_bf16 v[32:35], a[176:179], v[176:179], v[32:35]\n"
    "ds_read_b64_tr_b16 a[148:149], v216 offset:33288\n"
    "ds_read_b64_tr_b16 a[150:151], v216 offset:37416\n"
    "v_mfma_f32_16x16x32_bf16 v[36:39], a[180:183], v[176:179], v[36:39]\n"
    "s_add_u32 m0, s57, 16672\n"
    "v_exp_f32 v165, v165\n"
    "v_mfma_f32_16x16x32_bf16 v[36:39], a[212:215], v[180:183], v[36:39]\n"
    "buffer_load_dwordx4 v229, s[36:39], s52 offen offset:3072 lds\n"
    "v_mfma_f32_16x16x32_bf16 v[40:43], a[216:219], v[180:183], v[40:43]\n"
    "v_exp_f32 v166, v166\n"
    "v_mfma_f32_16x16x32_bf16 v[40:43], a[184:187], v[176:179], v[40:43]\n"
    "ds_read_b64_tr_b16 a[152:153], v217 offset:33280\n"
    "v_cvt_pk_bf16_f32 v186, v164, v165\n"
    "v_mfma_f32_16x16x32_bf16 v[44:47], a[188:191], v[176:179], v[44:47]\n"
    "v_exp_f32 v167, v167\n"
    "v_mfma_f32_16x16x32_bf16 v[44:47], a[220:223], v[180:183], v[44:47]\n"
    "v_cvt_pk_bf16_f32 v187, v166, v167\n"
    "v_mfma_f32_16x16x32_bf16 v[48:51], a[224:227], v[180:183], v[48:51]\n"
    "v_exp_f32 v168, v168\n"
    "v_mfma_f32_16x16x32_bf16 v[48:51], a[192:195], v[176:179], v[48:51]\n"
    "ds_read_b64_tr_b16 a[154:155], v217 offset:37408\n"
    "ds_read_b64_tr_b16 a[156:157], v217 offset:33288\n"
    "v_mfma_f32_16x16x32_bf16 v[52:55], a[196:199], v[176:179], v[52:55]\n"
    "v_exp_f32 v169, v169\n"
    "v_mfma_f32_16x16x32_bf16 v[52:55], a[228:231], v[180:183], v[52:55]\n"
    "ds_read_b64_tr_b16 a[158:159], v217 offset:37416\n"
    "v_cvt_pk_bf16_f32 v188, v168, v169\n"
    "v_mfma_f32_16x16x32_bf16 v[56:59], a[232:235], v[180:183], v[56:59]\n"
    "s_add_u32 m0, s58, 24768\n"
    "v_exp_f32 v170, v170\n"
    "v_mfma_f32_16x16x32_bf16 v[56:59], a[200:203], v[176:179], v[56:59]\n"
    "buffer_load_dwordx4 v229, s[40:43], s54 offen lds\n"
    "v_mfma_f32_16x16x32_bf16 v[60:63], a[204:207], v[176:179], v[60:63]\n"
    "v_exp_f32 v171, v171\n"
    "v_mfma_f32_16x16x32_bf16 v[60:63], a[236:239], v[180:183], v[60:63]\n"
    "ds_read_b128 a[248:251], v224 offset:16384\n"
    "v_cvt_pk_bf16_f32 v189, v170, v171\n"
    "v_mfma_f32_16x16x32_bf16 v[160:163], a[80:83], a[48:51], -4.0\n"
    "v_exp_f32 v172, v172\n"
    "v_mfma_f32_16x16x32_bf16 v[160:163], a[84:87], a[52:55], v[160:163]\n"
    "ds_read_b64_tr_b16 a[160:161], v218 offset:33280\n"
    "ds_read_b64_tr_b16 a[162:163], v218 offset:37408\n"
    "v_mfma_f32_16x16x32_bf16 v[160:163], a[88:91], a[56:59], v[160:163]\n"
    "v_exp_f32 v173, v173\n"
    "v_mfma_f32_16x16x32_bf16 v[160:163], a[92:95], a[60:63], v[160:163]\n"
    "ds_read_b64_tr_b16 a[164:165], v218 offset:33288\n"
    "v_cvt_pk_bf16_f32 v190, v172, v173\n"
    "v_mfma_f32_16x16x32_bf16 v[164:167], a[108:111], a[60:63], -4.0\n"
    "s_add_u32 m0, s58, 24776\n"
    "v_exp_f32 v174, v174\n"
    "v_mfma_f32_16x16x32_bf16 v[164:167], a[104:107], a[56:59], v[164:167]\n"
    "buffer_load_dwordx4 v229, s[40:43], s54 offen offset:1024 lds\n"
    "v_mfma_f32_16x16x32_bf16 v[164:167], a[100:103], a[52:55], v[164:167]\n"
    "v_exp_f32 v175, v175\n"
    "v_mfma_f32_16x16x32_bf16 v[164:167], a[96:99], a[48:51], v[164:167]\n"
    "ds_read_b64_tr_b16 a[166:167], v218 offset:37416\n"
    "v_cvt_pk_bf16_f32 v191, v174, v175\n"
    "v_mfma_f32_16x16x32_bf16 v[168:171], a[112:115], a[48:51], -4.0\n"
    "s_add_u32 s56, s64, 12288\n"
    "ds_read_b64_tr_b16 a[168:169], v219 offset:33280\n"
    "v_mfma_f32_16x16x32_bf16 v[168:171], a[116:119], a[52:55], v[168:171]\n"
    "s_waitcnt lgkmcnt(3)\n"
    "buffer_store_dwordx4 a[248:251], v228, s[48:51], s56 offen nt\n"
    "v_exp_f32 v160, v160\n"
    "v_mfma_f32_16x16x32_bf16 v[168:171], a[120:123], a[56:59], v[168:171]\n"
    "v_mfma_f32_16x16x32_bf16 v[168:171], a[124:127], a[60:63], v[168:171]\n"
    "v_exp_f32 v161, v161\n"
    "v_mfma_f32_16x16x32_bf16 v[172:175], a[140:143], a[60:63], -4.0\n"
    "ds_read_b64_tr_b16 a[170:171], v219 offset:37408\n"
    "v_cvt_pk_bf16_f32 v176, v160, v161\n"
    "v_mfma_f32_16x16x32_bf16 v[172:175], a[136:139], a[56:59], v[172:175]\n"
    "v_exp_f32 v162, v162\n"
    "v_mfma_f32_16x16x32_bf16 v[172:175], a[132:135], a[52:55], v[172:175]\n"
    "ds_read_b64_tr_b16 a[172:173], v219 offset:33288\n"
    "v_mfma_f32_16x16x32_bf16 v[172:175], a[128:131], a[48:51], v[172:175]\n"
    "v_exp_f32 v163, v163\n"
    "v_smfmac_f32_16x16x64_bf16 v[200:203], a[240:243], v[184:191], v237\n"
    "v_cvt_pk_bf16_f32 v177, v162, v163\n"
    "v_mfma_f32_16x16x32_bf16 v[64:67], a[208:211], v[188:191], v[64:67]\n"
    "v_exp_f32 v164, v164\n"
    "v_mfma_f32_16x16x32_bf16 v[64:67], a[176:179], v[184:187], v[64:67]\n"
    "ds_read_b64_tr_b16 a[174:175], v219 offset:37416\n"
    "v_mfma_f32_16x16x32_bf16 v[68:71], a[180:183], v[184:187], v[68:71]\n"
    "v_exp_f32 v165, v165\n"
    "v_mfma_f32_16x16x32_bf16 v[68:71], a[212:215], v[188:191], v[68:71]\n"
    "v_mfma_f32_16x16x32_bf16 v[72:75], a[216:219], v[188:191], v[72:75]\n"
    "v_exp_f32 v166, v166\n"
    "v_mfma_f32_16x16x32_bf16 v[72:75], a[184:187], v[184:187], v[72:75]\n"
    "v_cvt_pk_bf16_f32 v178, v164, v165\n"
    "v_mfma_f32_16x16x32_bf16 v[76:79], a[188:191], v[184:187], v[76:79]\n"
    "v_exp_f32 v167, v167\n"
    "v_mfma_f32_16x16x32_bf16 v[76:79], a[220:223], v[188:191], v[76:79]\n"
    "v_mfma_f32_16x16x32_bf16 v[80:83], a[224:227], v[188:191], v[80:83]\n"
    "v_exp_f32 v168, v168\n"
    "v_mfma_f32_16x16x32_bf16 v[80:83], a[192:195], v[184:187], v[80:83]\n"
    "v_cvt_pk_bf16_f32 v179, v166, v167\n"
    "v_mfma_f32_16x16x32_bf16 v[84:87], a[196:199], v[184:187], v[84:87]\n"
    "v_exp_f32 v169, v169\n"
    "v_mfma_f32_16x16x32_bf16 v[84:87], a[228:231], v[188:191], v[84:87]\n"
    "v_cvt_pk_bf16_f32 v180, v168, v169\n"
    "v_mfma_f32_16x16x32_bf16 v[88:91], a[232:235], v[188:191], v[88:91]\n"
    "v_exp_f32 v170, v170\n"
    "v_mfma_f32_16x16x32_bf16 v[88:91], a[200:203], v[184:187], v[88:91]\n"
    "v_mfma_f32_16x16x32_bf16 v[92:95], a[204:207], v[184:187], v[92:95]\n"
    "v_exp_f32 v171, v171\n"
    "v_mfma_f32_16x16x32_bf16 v[92:95], a[236:239], v[188:191], v[92:95]\n"
    "v_cvt_pk_bf16_f32 v181, v170, v171\n"
    "v_mfma_f32_16x16x32_bf16 v[160:163], a[80:83], a[64:67], -4.0\n"
    "v_exp_f32 v172, v172\n"
    "v_mfma_f32_16x16x32_bf16 v[160:163], a[84:87], a[68:71], v[160:163]\n"
    "v_mfma_f32_16x16x32_bf16 v[160:163], a[88:91], a[72:75], v[160:163]\n"
    "v_exp_f32 v173, v173\n"
    "v_mfma_f32_16x16x32_bf16 v[160:163], a[92:95], a[76:79], v[160:163]\n"
    "v_cvt_pk_bf16_f32 v182, v172, v173\n"
    "v_mfma_f32_16x16x32_bf16 v[164:167], a[108:111], a[76:79], -4.0\n"
    "v_exp_f32 v174, v174\n"
    "v_mfma_f32_16x16x32_bf16 v[164:167], a[104:107], a[72:75], v[164:167]\n"
    "v_mfma_f32_16x16x32_bf16 v[164:167], a[100:103], a[68:71], v[164:167]\n"
    "v_exp_f32 v175, v175\n"
    "v_mfma_f32_16x16x32_bf16 v[164:167], a[96:99], a[64:67], v[164:167]\n"
    "s_waitcnt vmcnt(12) lgkmcnt(0)\n"
    "v_cvt_pk_bf16_f32 v183, v174, v175\n"
    "v_mfma_f32_16x16x32_bf16 v[168:171], a[112:115], a[64:67], -4.0\n"
    "s_barrier\n"
    "v_mfma_f32_16x16x32_bf16 v[168:171], a[116:119], a[68:71], v[168:171]\n"
    "v_exp_f32 v160, v160\n"
    "v_mfma_f32_16x16x32_bf16 v[168:171], a[120:123], a[72:75], v[168:171]\n"
    "ds_read_b128 a[80:83], v212 offset:0\n"
    "ds_read_b128 a[84:87], v213 offset:0\n"
    "v_mfma_f32_16x16x32_bf16 v[168:171], a[124:127], a[76:79], v[168:171]\n"
    "v_exp_f32 v161, v161\n"
    "v_mfma_f32_16x16x32_bf16 v[172:175], a[140:143], a[76:79], -4.0\n"
    "ds_read_b128 a[88:91], v214 offset:0\n"
    "ds_read_b128 a[92:95], v215 offset:0\n"
    "v_mfma_f32_16x16x32_bf16 v[172:175], a[136:139], a[72:75], v[172:175]\n"
    "v_exp_f32 v162, v162\n"
    "v_mfma_f32_16x16x32_bf16 v[172:175], a[132:135], a[68:71], v[172:175]\n"
    "ds_read_b128 a[108:111], v215 offset:4160\n"
    "ds_read_b128 a[104:107], v214 offset:4160\n"
    "v_mfma_f32_16x16x32_bf16 v[172:175], a[128:131], a[64:67], v[172:175]\n"
    "v_exp_f32 v163, v163\n"
    "v_smfmac_f32_16x16x64_bf16 v[204:207], a[240:243], v[176:183], v237\n"
    "ds_read_b128 a[100:103], v213 offset:4160\n"
    "ds_read_b128 a[96:99], v212 offset:4160\n"
    "v_mfma_f32_16x16x32_bf16 v[96:99], a[208:211], v[180:183], v[96:99]\n"
    "v_exp_f32 v164, v164\n"
    "v_mfma_f32_16x16x32_bf16 v[96:99], a[176:179], v[176:179], v[96:99]\n"
    "v_cvt_pk_bf16_f32 v184, v160, v161\n"
    "v_cvt_pk_bf16_f32 v185, v162, v163\n"
    "v_mfma_f32_16x16x32_bf16 v[100:103], a[180:183], v[176:179], v[100:103]\n"
    "v_exp_f32 v165, v165\n"
    "v_mfma_f32_16x16x32_bf16 v[100:103], a[212:215], v[180:183], v[100:103]\n"
    "ds_read_b128 a[112:115], v212 offset:8320\n"
    "ds_read_b128 a[116:119], v213 offset:8320\n"
    "v_mfma_f32_16x16x32_bf16 v[104:107], a[216:219], v[180:183], v[104:107]\n"
    "s_add_u32 m0, s58, 0\n"
    "v_exp_f32 v166, v166\n"
    "v_mfma_f32_16x16x32_bf16 v[104:107], a[184:187], v[176:179], v[104:107]\n"
    "v_mfma_f32_16x16x32_bf16 v[108:111], a[188:191], v[176:179], v[108:111]\n"
    "s_add_u32 m0, s58, 8\n"
    "v_exp_f32 v167, v167\n"
    "v_mfma_f32_16x16x32_bf16 v[108:111], a[220:223], v[180:183], v[108:111]\n"
    "v_mfma_f32_16x16x32_bf16 v[112:115], a[224:227], v[180:183], v[112:115]\n"
    "v_exp_f32 v168, v168\n"
    "v_mfma_f32_16x16x32_bf16 v[112:115], a[192:195], v[176:179], v[112:115]\n"
    "ds_read_b128 a[120:123], v214 offset:8320\n"
    "ds_read_b128 a[124:127], v215 offset:8320\n"
    "v_mfma_f32_16x16x32_bf16 v[116:119], a[196:199], v[176:179], v[116:119]\n"
    "v_exp_f32 v169, v169\n"
    "v_mfma_f32_16x16x32_bf16 v[116:119], a[228:231], v[180:183], v[116:119]\n"
    "v_cvt_pk_bf16_f32 v186, v164, v165\n"
    "v_cvt_pk_bf16_f32 v187, v166, v167\n"
    "v_mfma_f32_16x16x32_bf16 v[120:123], a[232:235], v[180:183], v[120:123]\n"
    "v_exp_f32 v170, v170\n"
    "v_mfma_f32_16x16x32_bf16 v[120:123], a[200:203], v[176:179], v[120:123]\n"
    "ds_read_b128 a[140:143], v215 offset:12480\n"
    "ds_read_b128 a[136:139], v214 offset:12480\n"
    "v_mfma_f32_16x16x32_bf16 v[124:127], a[204:207], v[176:179], v[124:127]\n"
    "v_exp_f32 v171, v171\n"
    "v_mfma_f32_16x16x32_bf16 v[124:127], a[236:239], v[180:183], v[124:127]\n"
    "s_waitcnt lgkmcnt(4)\n"
    "ds_read_b128 a[132:135], v213 offset:12480\n"
    "ds_read_b128 a[128:131], v212 offset:12480\n"
    "v_mfma_f32_16x16x32_bf16 v[160:163], a[80:83], a[0:3], -4.0\n"
    "v_exp_f32 v172, v172\n"
    "v_mfma_f32_16x16x32_bf16 v[160:163], a[84:87], a[4:7], v[160:163]\n"
    "ds_read_b128 a[252:255], v225 offset:17408\n"
    "v_cvt_pk_bf16_f32 v188, v168, v169\n"
    "v_mfma_f32_16x16x32_bf16 v[160:163], a[88:91], a[8:11], v[160:163]\n"
    "v_exp_f32 v173, v173\n"
    "v_mfma_f32_16x16x32_bf16 v[160:163], a[92:95], a[12:15], v[160:163]\n"
    "v_cvt_pk_bf16_f32 v189, v170, v171\n"
    "v_cvt_pk_bf16_f32 v190, v172, v173\n"
    "v_mfma_f32_16x16x32_bf16 v[164:167], a[108:111], a[12:15], -4.0\n"
    "v_exp_f32 v174, v174\n"
    "v_mfma_f32_16x16x32_bf16 v[164:167], a[104:107], a[8:11], v[164:167]\n"
    "v_mfma_f32_16x16x32_bf16 v[164:167], a[100:103], a[4:7], v[164:167]\n"
    "v_exp_f32 v175, v175\n"
    "v_mfma_f32_16x16x32_bf16 v[164:167], a[96:99], a[0:3], v[164:167]\n"
    "v_mfma_f32_16x16x32_bf16 v[168:171], a[112:115], a[0:3], -4.0\n"
    "v_cvt_pk_bf16_f32 v191, v174, v175\n"
    "v_mfma_f32_16x16x32_bf16 v[168:171], a[116:119], a[4:7], v[168:171]\n"
    "s_waitcnt lgkmcnt(0)\n"
    "v_exp_f32 v160, v160\n"
    "v_mfma_f32_16x16x32_bf16 v[168:171], a[120:123], a[8:11], v[168:171]\n"
    "v_mfma_f32_16x16x32_bf16 v[168:171], a[124:127], a[12:15], v[168:171]\n"
    "buffer_store_dwordx4 a[252:255], v228, s[48:51], s56 offen offset:1024 nt\n"
    "v_exp_f32 v161, v161\n"
    "v_mfma_f32_16x16x32_bf16 v[172:175], a[140:143], a[12:15], -4.0\n"
    "v_mfma_f32_16x16x32_bf16 v[172:175], a[136:139], a[8:11], v[172:175]\n"
    "v_exp_f32 v162, v162\n"
    "v_mfma_f32_16x16x32_bf16 v[172:175], a[132:135], a[4:7], v[172:175]\n"
    "v_cvt_pk_bf16_f32 v176, v160, v161\n"
    "v_mfma_f32_16x16x32_bf16 v[172:175], a[128:131], a[0:3], v[172:175]\n"
    "v_exp_f32 v163, v163\n"
    "v_smfmac_f32_16x16x64_bf16 v[208:211], a[240:243], v[184:191], v237\n"
    "v_mfma_f32_16x16x32_bf16 v[128:131], a[208:211], v[188:191], v[128:131]\n"
    "v_exp_f32 v164, v164\n"
    "v_mfma_f32_16x16x32_bf16 v[128:131], a[176:179], v[184:187], v[128:131]\n"
    "v_mfma_f32_16x16x32_bf16 v[132:135], a[180:183], v[184:187], v[132:135]\n"
    "v_exp_f32 v165, v165\n"
    "v_mfma_f32_16x16x32_bf16 v[132:135], a[212:215], v[188:191], v[132:135]\n"
    "ds_read_b64_tr_b16 a[208:209], v216 offset:49792\n"
    "v_cvt_pk_bf16_f32 v177, v162, v163\n"
    "v_mfma_f32_16x16x32_bf16 v[136:139], a[216:219], v[188:191], v[136:139]\n"
    "v_exp_f32 v166, v166\n"
    "v_mfma_f32_16x16x32_bf16 v[136:139], a[184:187], v[184:187], v[136:139]\n"
    "ds_read_b64_tr_b16 a[210:211], v216 offset:53920\n"
    "ds_read_b64_tr_b16 a[212:213], v216 offset:49800\n"
    "v_mfma_f32_16x16x32_bf16 v[140:143], a[188:191], v[184:187], v[140:143]\n"
    "v_exp_f32 v167, v167\n"
    "v_mfma_f32_16x16x32_bf16 v[140:143], a[220:223], v[188:191], v[140:143]\n"
    "ds_read_b64_tr_b16 a[214:215], v216 offset:53928\n"
    "v_mfma_f32_16x16x32_bf16 v[144:147], a[224:227], v[188:191], v[144:147]\n"
    "v_exp_f32 v168, v168\n"
    "v_mfma_f32_16x16x32_bf16 v[144:147], a[192:195], v[184:187], v[144:147]\n"
    "v_cvt_pk_bf16_f32 v178, v164, v165\n"
    "v_cvt_pk_bf16_f32 v179, v166, v167\n"
    "v_mfma_f32_16x16x32_bf16 v[148:151], a[196:199], v[184:187], v[148:151]\n"
    "v_exp_f32 v169, v169\n"
    "v_mfma_f32_16x16x32_bf16 v[148:151], a[228:231], v[188:191], v[148:151]\n"
    "ds_read_b64_tr_b16 a[216:217], v217 offset:49792\n"
    "ds_read_b64_tr_b16 a[218:219], v217 offset:53920\n"
    "v_mfma_f32_16x16x32_bf16 v[152:155], a[232:235], v[188:191], v[152:155]\n"
    "v_exp_f32 v170, v170\n"
    "v_mfma_f32_16x16x32_bf16 v[152:155], a[200:203], v[184:187], v[152:155]\n"
    "ds_read_b64_tr_b16 a[220:221], v217 offset:49800\n"
    "ds_read_b64_tr_b16 a[222:223], v217 offset:53928\n"
    "v_mfma_f32_16x16x32_bf16 v[156:159], a[204:207], v[184:187], v[156:159]\n"
    "v_exp_f32 v171, v171\n"
    "v_mfma_f32_16x16x32_bf16 v[156:159], a[236:239], v[188:191], v[156:159]\n"
    "v_cvt_pk_bf16_f32 v180, v168, v169\n"
    "v_cvt_pk_bf16_f32 v181, v170, v171\n"
    "v_mfma_f32_16x16x32_bf16 v[160:163], a[80:83], a[16:19], -4.0\n"
    "v_exp_f32 v172, v172\n"
    "v_mfma_f32_16x16x32_bf16 v[160:163], a[84:87], a[20:23], v[160:163]\n"
    "ds_read_b64_tr_b16 a[224:225], v218 offset:49792\n"
    "ds_read_b64_tr_b16 a[226:227], v218 offset:53920\n"
    "v_mfma_f32_16x16x32_bf16 v[160:163], a[88:91], a[24:27], v[160:163]\n"
    "v_exp_f32 v173, v173\n"
    "v_mfma_f32_16x16x32_bf16 v[160:163], a[92:95], a[28:31], v[160:163]\n"
    "ds_read_b64_tr_b16 a[228:229], v218 offset:49800\n"
    "ds_read_b64_tr_b16 a[230:231], v218 offset:53928\n"
    "v_mfma_f32_16x16x32_bf16 v[164:167], a[108:111], a[28:31], -4.0\n"
    "v_exp_f32 v174, v174\n"
    "v_mfma_f32_16x16x32_bf16 v[164:167], a[104:107], a[24:27], v[164:167]\n"
    "ds_read_b64_tr_b16 a[232:233], v219 offset:49792\n"
    "v_cvt_pk_bf16_f32 v182, v172, v173\n"
    "v_mfma_f32_16x16x32_bf16 v[164:167], a[100:103], a[20:23], v[164:167]\n"
    "v_exp_f32 v175, v175\n"
    "v_mfma_f32_16x16x32_bf16 v[164:167], a[96:99], a[16:19], v[164:167]\n"
    "ds_read_b64_tr_b16 a[234:235], v219 offset:53920\n"
    "v_cvt_pk_bf16_f32 v183, v174, v175\n"
    "v_mfma_f32_16x16x32_bf16 v[168:171], a[112:115], a[16:19], -4.0\n"
    "ds_read_b64_tr_b16 a[236:237], v219 offset:49800\n"
    "ds_read_b64_tr_b16 a[238:239], v219 offset:53928\n"
    "v_mfma_f32_16x16x32_bf16 v[168:171], a[116:119], a[20:23], v[168:171]\n"
    "v_exp_f32 v160, v160\n"
    "v_mfma_f32_16x16x32_bf16 v[168:171], a[120:123], a[24:27], v[168:171]\n"
    "ds_read_b128 a[244:247], v226 offset:18432\n"
    "v_mfma_f32_16x16x32_bf16 v[168:171], a[124:127], a[28:31], v[168:171]\n"
    "v_exp_f32 v161, v161\n"
    "v_mfma_f32_16x16x32_bf16 v[172:175], a[140:143], a[28:31], -4.0\n"
    "v_cvt_pk_bf16_f32 v184, v160, v161\n"
    "v_mfma_f32_16x16x32_bf16 v[172:175], a[136:139], a[24:27], v[172:175]\n"
    "v_exp_f32 v162, v162\n"
    "v_mfma_f32_16x16x32_bf16 v[172:175], a[132:135], a[20:23], v[172:175]\n"
    "v_mfma_f32_16x16x32_bf16 v[172:175], a[128:131], a[16:19], v[172:175]\n"
    "v_exp_f32 v163, v163\n"
    "v_smfmac_f32_16x16x64_bf16 v[192:195], a[240:243], v[176:183], v237\n"
    "s_waitcnt lgkmcnt(1)\n"
    "v_cvt_pk_bf16_f32 v185, v162, v163\n"
    "v_mfma_f32_16x16x32_bf16 v[0:3], a[208:211], v[180:183], v[0:3]\n"
    "v_exp_f32 v164, v164\n"
    "v_mfma_f32_16x16x32_bf16 v[0:3], a[144:147], v[176:179], v[0:3]\n"
    "v_mfma_f32_16x16x32_bf16 v[4:7], a[148:151], v[176:179], v[4:7]\n"
    "s_waitcnt lgkmcnt(0)\n"
    "buffer_store_dwordx4 a[244:247], v228, s[48:51], s56 offen offset:2048 nt\n"
    "v_exp_f32 v165, v165\n"
    "v_mfma_f32_16x16x32_bf16 v[4:7], a[212:215], v[180:183], v[4:7]\n"
    "v_cvt_pk_bf16_f32 v186, v164, v165\n"
    "v_mfma_f32_16x16x32_bf16 v[8:11], a[216:219], v[180:183], v[8:11]\n"
    "v_exp_f32 v166, v166\n"
    "v_mfma_f32_16x16x32_bf16 v[8:11], a[152:155], v[176:179], v[8:11]\n"
    "v_mfma_f32_16x16x32_bf16 v[12:15], a[156:159], v[176:179], v[12:15]\n"
    "v_exp_f32 v167, v167\n"
    "v_mfma_f32_16x16x32_bf16 v[12:15], a[220:223], v[180:183], v[12:15]\n"
    "v_cvt_pk_bf16_f32 v187, v166, v167\n"
    "v_mfma_f32_16x16x32_bf16 v[16:19], a[224:227], v[180:183], v[16:19]\n"
    "v_exp_f32 v168, v168\n"
    "v_mfma_f32_16x16x32_bf16 v[16:19], a[160:163], v[176:179], v[16:19]\n"
    "v_mfma_f32_16x16x32_bf16 v[20:23], a[164:167], v[176:179], v[20:23]\n"
    "v_exp_f32 v169, v169\n"
    "v_mfma_f32_16x16x32_bf16 v[20:23], a[228:231], v[180:183], v[20:23]\n"
    "v_cvt_pk_bf16_f32 v188, v168, v169\n"
    "v_mfma_f32_16x16x32_bf16 v[24:27], a[232:235], v[180:183], v[24:27]\n"
    "v_exp_f32 v170, v170\n"
    "v_mfma_f32_16x16x32_bf16 v[24:27], a[168:171], v[176:179], v[24:27]\n"
    "v_mfma_f32_16x16x32_bf16 v[28:31], a[172:175], v[176:179], v[28:31]\n"
    "v_exp_f32 v171, v171\n"
    "v_mfma_f32_16x16x32_bf16 v[28:31], a[236:239], v[180:183], v[28:31]\n"
    "v_cvt_pk_bf16_f32 v189, v170, v171\n"
    "v_mfma_f32_16x16x32_bf16 v[160:163], a[80:83], a[32:35], -4.0\n"
    "v_exp_f32 v172, v172\n"
    "v_mfma_f32_16x16x32_bf16 v[160:163], a[84:87], a[36:39], v[160:163]\n"
    "v_mfma_f32_16x16x32_bf16 v[160:163], a[88:91], a[40:43], v[160:163]\n"
    "v_exp_f32 v173, v173\n"
    "v_mfma_f32_16x16x32_bf16 v[160:163], a[92:95], a[44:47], v[160:163]\n"
    "v_cvt_pk_bf16_f32 v190, v172, v173\n"
    "v_mfma_f32_16x16x32_bf16 v[164:167], a[108:111], a[44:47], -4.0\n"
    "v_exp_f32 v174, v174\n"
    "v_mfma_f32_16x16x32_bf16 v[164:167], a[104:107], a[40:43], v[164:167]\n"
    "v_mfma_f32_16x16x32_bf16 v[164:167], a[100:103], a[36:39], v[164:167]\n"
    "v_exp_f32 v175, v175\n"
    "v_mfma_f32_16x16x32_bf16 v[164:167], a[96:99], a[32:35], v[164:167]\n"
    "v_cvt_pk_bf16_f32 v191, v174, v175\n"
    "v_mfma_f32_16x16x32_bf16 v[168:171], a[112:115], a[32:35], -4.0\n"
    "v_mfma_f32_16x16x32_bf16 v[168:171], a[116:119], a[36:39], v[168:171]\n"
    "v_exp_f32 v160, v160\n"
    "v_mfma_f32_16x16x32_bf16 v[168:171], a[120:123], a[40:43], v[168:171]\n"
    "v_mfma_f32_16x16x32_bf16 v[168:171], a[124:127], a[44:47], v[168:171]\n"
    "v_exp_f32 v161, v161\n"
    "v_mfma_f32_16x16x32_bf16 v[172:175], a[140:143], a[44:47], -4.0\n"
    "v_cvt_pk_bf16_f32 v176, v160, v161\n"
    "v_mfma_f32_16x16x32_bf16 v[172:175], a[136:139], a[40:43], v[172:175]\n"
    "v_exp_f32 v162, v162\n"
    "v_mfma_f32_16x16x32_bf16 v[172:175], a[132:135], a[36:39], v[172:175]\n"
    "v_mfma_f32_16x16x32_bf16 v[172:175], a[128:131], a[32:35], v[172:175]\n"
    "v_exp_f32 v163, v163\n"
    "v_smfmac_f32_16x16x64_bf16 v[196:199], a[240:243], v[184:191], v237\n"
    "v_cvt_pk_bf16_f32 v177, v162, v163\n"
    "v_mfma_f32_16x16x32_bf16 v[32:35], a[208:211], v[188:191], v[32:35]\n"
    "v_exp_f32 v164, v164\n"
    "v_mfma_f32_16x16x32_bf16 v[32:35], a[144:147], v[184:187], v[32:35]\n"
    "v_mfma_f32_16x16x32_bf16 v[36:39], a[148:151], v[184:187], v[36:39]\n"
    "v_exp_f32 v165, v165\n"
    "v_mfma_f32_16x16x32_bf16 v[36:39], a[212:215], v[188:191], v[36:39]\n"
    "v_cvt_pk_bf16_f32 v178, v164, v165\n"
    "v_mfma_f32_16x16x32_bf16 v[40:43], a[216:219], v[188:191], v[40:43]\n"
    "v_exp_f32 v166, v166\n"
    "v_mfma_f32_16x16x32_bf16 v[40:43], a[152:155], v[184:187], v[40:43]\n"
    "v_mfma_f32_16x16x32_bf16 v[44:47], a[156:159], v[184:187], v[44:47]\n"
    "v_exp_f32 v167, v167\n"
    "v_mfma_f32_16x16x32_bf16 v[44:47], a[220:223], v[188:191], v[44:47]\n"
    "v_cvt_pk_bf16_f32 v179, v166, v167\n"
    "v_mfma_f32_16x16x32_bf16 v[48:51], a[224:227], v[188:191], v[48:51]\n"
    "v_exp_f32 v168, v168\n"
    "v_mfma_f32_16x16x32_bf16 v[48:51], a[160:163], v[184:187], v[48:51]\n"
    "v_mfma_f32_16x16x32_bf16 v[52:55], a[164:167], v[184:187], v[52:55]\n"
    "v_exp_f32 v169, v169\n"
    "v_mfma_f32_16x16x32_bf16 v[52:55], a[228:231], v[188:191], v[52:55]\n"
    "v_cvt_pk_bf16_f32 v180, v168, v169\n"
    "v_mfma_f32_16x16x32_bf16 v[56:59], a[232:235], v[188:191], v[56:59]\n"
    "v_exp_f32 v170, v170\n"
    "v_mfma_f32_16x16x32_bf16 v[56:59], a[168:171], v[184:187], v[56:59]\n"
    "v_mfma_f32_16x16x32_bf16 v[60:63], a[172:175], v[184:187], v[60:63]\n"
    "v_exp_f32 v171, v171\n"
    "v_mfma_f32_16x16x32_bf16 v[60:63], a[236:239], v[188:191], v[60:63]\n"
    "v_cvt_pk_bf16_f32 v181, v170, v171\n"
    "v_mfma_f32_16x16x32_bf16 v[160:163], a[80:83], a[48:51], -4.0\n"
    "v_exp_f32 v172, v172\n"
    "v_mfma_f32_16x16x32_bf16 v[160:163], a[84:87], a[52:55], v[160:163]\n"
    "v_mfma_f32_16x16x32_bf16 v[160:163], a[88:91], a[56:59], v[160:163]\n"
    "v_exp_f32 v173, v173\n"
    "v_mfma_f32_16x16x32_bf16 v[160:163], a[92:95], a[60:63], v[160:163]\n"
    "v_cvt_pk_bf16_f32 v182, v172, v173\n"
    "v_mfma_f32_16x16x32_bf16 v[164:167], a[108:111], a[60:63], -4.0\n"
    "v_exp_f32 v174, v174\n"
    "v_mfma_f32_16x16x32_bf16 v[164:167], a[104:107], a[56:59], v[164:167]\n"
    "v_mfma_f32_16x16x32_bf16 v[164:167], a[100:103], a[52:55], v[164:167]\n"
    "v_exp_f32 v175, v175\n"
    "v_mfma_f32_16x16x32_bf16 v[164:167], a[96:99], a[48:51], v[164:167]\n"
    "v_cvt_pk_bf16_f32 v183, v174, v175\n"
    "v_mfma_f32_16x16x32_bf16 v[168:171], a[112:115], a[48:51], -4.0\n"
    "v_mfma_f32_16x16x32_bf16 v[168:171], a[116:119], a[52:55], v[168:171]\n"
    "v_exp_f32 v160, v160\n"
    "v_mfma_f32_16x16x32_bf16 v[168:171], a[120:123], a[56:59], v[168:171]\n"
    "ds_read_b128 a[248:251], v227 offset:19456\n"
    "v_mfma_f32_16x16x32_bf16 v[168:171], a[124:127], a[60:63], v[168:171]\n"
    "v_exp_f32 v161, v161\n"
    "v_mfma_f32_16x16x32_bf16 v[172:175], a[140:143], a[60:63], -4.0\n"
    "v_cvt_pk_bf16_f32 v184, v160, v161\n"
    "v_mfma_f32_16x16x32_bf16 v[172:175], a[136:139], a[56:59], v[172:175]\n"
    "v_exp_f32 v162, v162\n"
    "v_mfma_f32_16x16x32_bf16 v[172:175], a[132:135], a[52:55], v[172:175]\n"
    "v_mfma_f32_16x16x32_bf16 v[172:175], a[128:131], a[48:51], v[172:175]\n"
    "v_exp_f32 v163, v163\n"
    "v_smfmac_f32_16x16x64_bf16 v[200:203], a[240:243], v[176:183], v237\n"
    "v_cvt_pk_bf16_f32 v185, v162, v163\n"
    "v_mfma_f32_16x16x32_bf16 v[64:67], a[208:211], v[180:183], v[64:67]\n"
    "v_exp_f32 v164, v164\n"
    "v_mfma_f32_16x16x32_bf16 v[64:67], a[144:147], v[176:179], v[64:67]\n"
    "v_mfma_f32_16x16x32_bf16 v[68:71], a[148:151], v[176:179], v[68:71]\n"
    "s_waitcnt lgkmcnt(0)\n"
    "buffer_store_dwordx4 a[248:251], v228, s[48:51], s56 offen offset:3072 nt\n"
    "v_exp_f32 v165, v165\n"
    "v_mfma_f32_16x16x32_bf16 v[68:71], a[212:215], v[180:183], v[68:71]\n"
    "s_mov_b32 s64, s70\n"
    "v_cvt_pk_bf16_f32 v186, v164, v165\n"
    "v_mfma_f32_16x16x32_bf16 v[72:75], a[216:219], v[180:183], v[72:75]\n"
    "v_exp_f32 v166, v166\n"
    "v_mfma_f32_16x16x32_bf16 v[72:75], a[152:155], v[176:179], v[72:75]\n"
    "v_mfma_f32_16x16x32_bf16 v[76:79], a[156:159], v[176:179], v[76:79]\n"
    "v_exp_f32 v167, v167\n"
    "v_mfma_f32_16x16x32_bf16 v[76:79], a[220:223], v[180:183], v[76:79]\n"
    "v_cvt_pk_bf16_f32 v187, v166, v167\n"
    "v_mfma_f32_16x16x32_bf16 v[80:83], a[224:227], v[180:183], v[80:83]\n"
    "v_exp_f32 v168, v168\n"
    "v_mfma_f32_16x16x32_bf16 v[80:83], a[160:163], v[176:179], v[80:83]\n"
    "v_mfma_f32_16x16x32_bf16 v[84:87], a[164:167], v[176:179], v[84:87]\n"
    "v_exp_f32 v169, v169\n"
    "v_mfma_f32_16x16x32_bf16 v[84:87], a[228:231], v[180:183], v[84:87]\n"
    "v_cvt_pk_bf16_f32 v188, v168, v169\n"
    "v_mfma_f32_16x16x32_bf16 v[88:91], a[232:235], v[180:183], v[88:91]\n"
    "v_exp_f32 v170, v170\n"
    "v_mfma_f32_16x16x32_bf16 v[88:91], a[168:171], v[176:179], v[88:91]\n"
    "v_mfma_f32_16x16x32_bf16 v[92:95], a[172:175], v[176:179], v[92:95]\n"
    "v_exp_f32 v171, v171\n"
    "v_mfma_f32_16x16x32_bf16 v[92:95], a[236:239], v[180:183], v[92:95]\n"
    "v_cvt_pk_bf16_f32 v189, v170, v171\n"
    "v_mfma_f32_16x16x32_bf16 v[160:163], a[80:83], a[64:67], -4.0\n"
    "v_exp_f32 v172, v172\n"
    "v_mfma_f32_16x16x32_bf16 v[160:163], a[84:87], a[68:71], v[160:163]\n"
    "v_mfma_f32_16x16x32_bf16 v[160:163], a[88:91], a[72:75], v[160:163]\n"
    "v_exp_f32 v173, v173\n"
    "v_mfma_f32_16x16x32_bf16 v[160:163], a[92:95], a[76:79], v[160:163]\n"
    "v_cvt_pk_bf16_f32 v190, v172, v173\n"
    "v_mfma_f32_16x16x32_bf16 v[164:167], a[108:111], a[76:79], -4.0\n"
    "v_exp_f32 v174, v174\n"
    "v_mfma_f32_16x16x32_bf16 v[164:167], a[104:107], a[72:75], v[164:167]\n"
    "v_mfma_f32_16x16x32_bf16 v[164:167], a[100:103], a[68:71], v[164:167]\n"
    "v_exp_f32 v175, v175\n"
    "v_mfma_f32_16x16x32_bf16 v[164:167], a[96:99], a[64:67], v[164:167]\n"
    "s_waitcnt vmcnt(6)\n"
    "v_cvt_pk_bf16_f32 v191, v174, v175\n"
    "v_mfma_f32_16x16x32_bf16 v[168:171], a[112:115], a[64:67], -4.0\n"
    "s_barrier\n"
    "v_mfma_f32_16x16x32_bf16 v[168:171], a[116:119], a[68:71], v[168:171]\n"
    "v_exp_f32 v160, v160\n"
    "v_mfma_f32_16x16x32_bf16 v[168:171], a[120:123], a[72:75], v[168:171]\n"
    "ds_read_b128 a[80:83], v212 offset:16640\n"
    "ds_read_b128 a[84:87], v213 offset:16640\n"
    "v_mfma_f32_16x16x32_bf16 v[168:171], a[124:127], a[76:79], v[168:171]\n"
    "v_exp_f32 v161, v161\n"
    "v_mfma_f32_16x16x32_bf16 v[172:175], a[140:143], a[76:79], -4.0\n"
    "ds_read_b128 a[88:91], v214 offset:16640\n"
    "ds_read_b128 a[92:95], v215 offset:16640\n"
    "v_mfma_f32_16x16x32_bf16 v[172:175], a[136:139], a[72:75], v[172:175]\n"
    "v_exp_f32 v162, v162\n"
    "v_mfma_f32_16x16x32_bf16 v[172:175], a[132:135], a[68:71], v[172:175]\n"
    "ds_read_b128 a[108:111], v215 offset:20800\n"
    "ds_read_b128 a[104:107], v214 offset:20800\n"
    "v_mfma_f32_16x16x32_bf16 v[172:175], a[128:131], a[64:67], v[172:175]\n"
    "v_exp_f32 v163, v163\n"
    "v_smfmac_f32_16x16x64_bf16 v[204:207], a[240:243], v[184:191], v237\n"
    "ds_read_b128 a[100:103], v213 offset:20800\n"
    "ds_read_b128 a[96:99], v212 offset:20800\n"
    "v_mfma_f32_16x16x32_bf16 v[96:99], a[208:211], v[188:191], v[96:99]\n"
    "v_exp_f32 v164, v164\n"
    "v_mfma_f32_16x16x32_bf16 v[96:99], a[144:147], v[184:187], v[96:99]\n"
    "v_cvt_pk_bf16_f32 v176, v160, v161\n"
    "v_cvt_pk_bf16_f32 v177, v162, v163\n"
    "v_mfma_f32_16x16x32_bf16 v[100:103], a[148:151], v[184:187], v[100:103]\n"
    "v_exp_f32 v165, v165\n"
    "v_mfma_f32_16x16x32_bf16 v[100:103], a[212:215], v[188:191], v[100:103]\n"
    "ds_read_b128 a[112:115], v212 offset:24960\n"
    "ds_read_b128 a[116:119], v213 offset:24960\n"
    "v_mfma_f32_16x16x32_bf16 v[104:107], a[216:219], v[188:191], v[104:107]\n"
    "v_exp_f32 v166, v166\n"
    "v_mfma_f32_16x16x32_bf16 v[104:107], a[152:155], v[184:187], v[104:107]\n"
    "ds_read_b128 a[120:123], v214 offset:24960\n"
    "ds_read_b128 a[124:127], v215 offset:24960\n"
    "v_mfma_f32_16x16x32_bf16 v[108:111], a[156:159], v[184:187], v[108:111]\n"
    "v_exp_f32 v167, v167\n"
    "v_mfma_f32_16x16x32_bf16 v[108:111], a[220:223], v[188:191], v[108:111]\n"
    "v_cvt_pk_bf16_f32 v178, v164, v165\n"
    "v_cvt_pk_bf16_f32 v179, v166, v167\n"
    "v_mfma_f32_16x16x32_bf16 v[112:115], a[224:227], v[188:191], v[112:115]\n"
    "v_exp_f32 v168, v168\n"
    "v_mfma_f32_16x16x32_bf16 v[112:115], a[160:163], v[184:187], v[112:115]\n"
    "ds_read_b128 a[140:143], v215 offset:29120\n"
    "ds_read_b128 a[136:139], v214 offset:29120\n"
    "v_mfma_f32_16x16x32_bf16 v[116:119], a[164:167], v[184:187], v[116:119]\n"
    "v_exp_f32 v169, v169\n"
    "v_mfma_f32_16x16x32_bf16 v[116:119], a[228:231], v[188:191], v[116:119]\n"
    "ds_read_b128 a[132:135], v213 offset:29120\n"
    "ds_read_b128 a[128:131], v212 offset:29120\n"
    "v_mfma_f32_16x16x32_bf16 v[120:123], a[232:235], v[188:191], v[120:123]\n"
    "v_exp_f32 v170, v170\n"
    "v_mfma_f32_16x16x32_bf16 v[120:123], a[168:171], v[184:187], v[120:123]\n"
    "ds_read_b64_tr_b16 a[176:177], v216 offset:41536\n"
    "v_cvt_pk_bf16_f32 v180, v168, v169\n"
    "v_mfma_f32_16x16x32_bf16 v[124:127], a[172:175], v[184:187], v[124:127]\n"
    "v_exp_f32 v171, v171\n"
    "v_mfma_f32_16x16x32_bf16 v[124:127], a[236:239], v[188:191], v[124:127]\n"
    "s_waitcnt lgkmcnt(5)\n"
    "ds_read_b64_tr_b16 a[178:179], v216 offset:45664\n"
    "v_cvt_pk_bf16_f32 v181, v170, v171\n"
    "v_mfma_f32_16x16x32_bf16 v[160:163], a[80:83], a[0:3], -4.0\n"
    "v_exp_f32 v172, v172\n"
    "v_mfma_f32_16x16x32_bf16 v[160:163], a[84:87], a[4:7], v[160:163]\n"
    "ds_read_b64_tr_b16 a[180:181], v216 offset:41544\n"
    "ds_read_b64_tr_b16 a[182:183], v216 offset:45672\n"
    "v_mfma_f32_16x16x32_bf16 v[160:163], a[88:91], a[8:11], v[160:163]\n"
    "v_exp_f32 v173, v173\n"
    "v_mfma_f32_16x16x32_bf16 v[160:163], a[92:95], a[12:15], v[160:163]\n"
    "ds_read_b64_tr_b16 a[184:185], v217 offset:41536\n"
    "v_cvt_pk_bf16_f32 v182, v172, v173\n"
    "v_mfma_f32_16x16x32_bf16 v[164:167], a[108:111], a[12:15], -4.0\n"
    "v_exp_f32 v174, v174\n"
    "v_mfma_f32_16x16x32_bf16 v[164:167], a[104:107], a[8:11], v[164:167]\n"
    "ds_read_b64_tr_b16 a[186:187], v217 offset:45664\n"
    "ds_read_b64_tr_b16 a[188:189], v217 offset:41544\n"
    "v_mfma_f32_16x16x32_bf16 v[164:167], a[100:103], a[4:7], v[164:167]\n"
    "v_exp_f32 v175, v175\n"
    "v_mfma_f32_16x16x32_bf16 v[164:167], a[96:99], a[0:3], v[164:167]\n"
    "s_waitcnt vmcnt(4)\n"
    "ds_read_b64_tr_b16 a[190:191], v217 offset:45672\n"
    "v_cvt_pk_bf16_f32 v183, v174, v175\n"
    "v_mfma_f32_16x16x32_bf16 v[168:171], a[112:115], a[0:3], -4.0\n"
    "s_barrier\n"
    "v_mfma_f32_16x16x32_bf16 v[168:171], a[116:119], a[4:7], v[168:171]\n"
    "v_exp_f32 v160, v160\n"
    "v_mfma_f32_16x16x32_bf16 v[168:171], a[120:123], a[8:11], v[168:171]\n"
    "ds_read_b64_tr_b16 a[192:193], v218 offset:41536\n"
    "ds_read_b64_tr_b16 a[194:195], v218 offset:45664\n"
    "v_mfma_f32_16x16x32_bf16 v[168:171], a[124:127], a[12:15], v[168:171]\n"
    "s_waitcnt lgkmcnt(5)\n"
    "v_exp_f32 v161, v161\n"
    "v_mfma_f32_16x16x32_bf16 v[172:175], a[140:143], a[12:15], -4.0\n"
    "ds_read_b64_tr_b16 a[196:197], v218 offset:41544\n"
    "v_cvt_pk_bf16_f32 v184, v160, v161\n"
    "v_mfma_f32_16x16x32_bf16 v[172:175], a[136:139], a[8:11], v[172:175]\n"
    "v_exp_f32 v162, v162\n"
    "v_mfma_f32_16x16x32_bf16 v[172:175], a[132:135], a[4:7], v[172:175]\n"
    "ds_read_b64_tr_b16 a[198:199], v218 offset:45672\n"
    "ds_read_b64_tr_b16 a[200:201], v219 offset:41536\n"
    "v_mfma_f32_16x16x32_bf16 v[172:175], a[128:131], a[0:3], v[172:175]\n"
    "v_exp_f32 v163, v163\n"
    "v_smfmac_f32_16x16x64_bf16 v[208:211], a[240:243], v[176:183], v237\n"
    "ds_read_b64_tr_b16 a[202:203], v219 offset:45664\n"
    "v_cvt_pk_bf16_f32 v185, v162, v163\n"
    "v_mfma_f32_16x16x32_bf16 v[128:131], a[208:211], v[180:183], v[128:131]\n"
    "v_exp_f32 v164, v164\n"
    "v_mfma_f32_16x16x32_bf16 v[128:131], a[144:147], v[176:179], v[128:131]\n"
    "ds_read_b64_tr_b16 a[208:209], v216 offset:58048\n"
    "ds_read_b64_tr_b16 a[210:211], v216 offset:62176\n"
    "v_mfma_f32_16x16x32_bf16 v[132:135], a[148:151], v[176:179], v[132:135]\n"
    "v_exp_f32 v165, v165\n"
    "v_mfma_f32_16x16x32_bf16 v[132:135], a[212:215], v[180:183], v[132:135]\n"
    "ds_read_b64_tr_b16 a[212:213], v216 offset:58056\n"
    "v_cvt_pk_bf16_f32 v186, v164, v165\n"
    "v_mfma_f32_16x16x32_bf16 v[136:139], a[216:219], v[180:183], v[136:139]\n"
    "v_exp_f32 v166, v166\n"
    "v_mfma_f32_16x16x32_bf16 v[136:139], a[152:155], v[176:179], v[136:139]\n"
    "ds_read_b64_tr_b16 a[214:215], v216 offset:62184\n"
    "ds_read_b64_tr_b16 a[216:217], v217 offset:58048\n"
    "v_mfma_f32_16x16x32_bf16 v[140:143], a[156:159], v[176:179], v[140:143]\n"
    "v_exp_f32 v167, v167\n"
    "v_mfma_f32_16x16x32_bf16 v[140:143], a[220:223], v[180:183], v[140:143]\n"
    "ds_read_b64_tr_b16 a[218:219], v217 offset:62176\n"
    "v_cvt_pk_bf16_f32 v187, v166, v167\n"
    "v_mfma_f32_16x16x32_bf16 v[144:147], a[224:227], v[180:183], v[144:147]\n"
    "v_exp_f32 v168, v168\n"
    "v_mfma_f32_16x16x32_bf16 v[144:147], a[160:163], v[176:179], v[144:147]\n"
    "ds_read_b64_tr_b16 a[220:221], v217 offset:58056\n"
    "ds_read_b64_tr_b16 a[222:223], v217 offset:62184\n"
    "v_mfma_f32_16x16x32_bf16 v[148:151], a[164:167], v[176:179], v[148:151]\n"
    "v_exp_f32 v169, v169\n"
    "v_mfma_f32_16x16x32_bf16 v[148:151], a[228:231], v[180:183], v[148:151]\n"
    "ds_read_b64_tr_b16 a[224:225], v218 offset:58048\n"
    "v_cvt_pk_bf16_f32 v188, v168, v169\n"
    "v_mfma_f32_16x16x32_bf16 v[152:155], a[232:235], v[180:183], v[152:155]\n"
    "v_exp_f32 v170, v170\n"
    "v_mfma_f32_16x16x32_bf16 v[152:155], a[168:171], v[176:179], v[152:155]\n"
    "ds_read_b64_tr_b16 a[226:227], v218 offset:62176\n"
    "ds_read_b64_tr_b16 a[228:229], v218 offset:58056\n"
    "v_mfma_f32_16x16x32_bf16 v[156:159], a[172:175], v[176:179], v[156:159]\n"
    "v_exp_f32 v171, v171\n"
    "v_mfma_f32_16x16x32_bf16 v[156:159], a[236:239], v[180:183], v[156:159]\n"
    "ds_read_b64_tr_b16 a[230:231], v218 offset:62184\n"
    "v_cvt_pk_bf16_f32 v189, v170, v171\n"
    "v_mfma_f32_16x16x32_bf16 v[160:163], a[80:83], a[16:19], -4.0\n"
    "v_exp_f32 v172, v172\n"
    "v_mfma_f32_16x16x32_bf16 v[160:163], a[84:87], a[20:23], v[160:163]\n"
    "ds_read_b64_tr_b16 a[232:233], v219 offset:58048\n"
    "ds_read_b64_tr_b16 a[234:235], v219 offset:62176\n"
    "v_mfma_f32_16x16x32_bf16 v[160:163], a[88:91], a[24:27], v[160:163]\n"
    "v_exp_f32 v173, v173\n"
    "v_mfma_f32_16x16x32_bf16 v[160:163], a[92:95], a[28:31], v[160:163]\n"
    "ds_read_b64_tr_b16 a[204:205], v219 offset:41544\n"
    "v_cvt_pk_bf16_f32 v190, v172, v173\n"
    "v_mfma_f32_16x16x32_bf16 v[164:167], a[108:111], a[28:31], -4.0\n"
    "v_exp_f32 v174, v174\n"
    "v_mfma_f32_16x16x32_bf16 v[164:167], a[104:107], a[24:27], v[164:167]\n"
    "ds_read_b64_tr_b16 a[206:207], v219 offset:45672\n"
    "ds_read_b64_tr_b16 a[236:237], v219 offset:58056\n"
    "v_mfma_f32_16x16x32_bf16 v[164:167], a[100:103], a[20:23], v[164:167]\n"
    "v_exp_f32 v175, v175\n"
    "v_mfma_f32_16x16x32_bf16 v[164:167], a[96:99], a[16:19], v[164:167]\n"
    "ds_read_b64_tr_b16 a[238:239], v219 offset:62184\n"
    "v_cvt_pk_bf16_f32 v191, v174, v175\n"
    "v_mfma_f32_16x16x32_bf16 v[168:171], a[112:115], a[16:19], -4.0\n"
    "v_mfma_f32_16x16x32_bf16 v[168:171], a[116:119], a[20:23], v[168:171]\n"
    "v_exp_f32 v160, v160\n"
    "v_mfma_f32_16x16x32_bf16 v[168:171], a[120:123], a[24:27], v[168:171]\n"
    "v_mfma_f32_16x16x32_bf16 v[168:171], a[124:127], a[28:31], v[168:171]\n"
    "v_exp_f32 v161, v161\n"
    "v_mfma_f32_16x16x32_bf16 v[172:175], a[140:143], a[28:31], -4.0\n"
    "v_cvt_pk_bf16_f32 v176, v160, v161\n"
    "v_mfma_f32_16x16x32_bf16 v[172:175], a[136:139], a[24:27], v[172:175]\n"
    "v_exp_f32 v162, v162\n"
    "v_mfma_f32_16x16x32_bf16 v[172:175], a[132:135], a[20:23], v[172:175]\n"
    "v_mfma_f32_16x16x32_bf16 v[172:175], a[128:131], a[16:19], v[172:175]\n"
    "v_exp_f32 v163, v163\n"
    "v_smfmac_f32_16x16x64_bf16 v[192:195], a[240:243], v[184:191], v237\n"
    "s_waitcnt lgkmcnt(0)\n"
    "v_cvt_pk_bf16_f32 v177, v162, v163\n"
    "v_mfma_f32_16x16x32_bf16 v[0:3], a[208:211], v[188:191], v[0:3]\n"
    "v_exp_f32 v164, v164\n"
    "v_mfma_f32_16x16x32_bf16 v[0:3], a[176:179], v[184:187], v[0:3]\n"
    "v_mfma_f32_16x16x32_bf16 v[4:7], a[180:183], v[184:187], v[4:7]\n"
    "v_exp_f32 v165, v165\n"
    "v_mfma_f32_16x16x32_bf16 v[4:7], a[212:215], v[188:191], v[4:7]\n"
    "v_cvt_pk_bf16_f32 v178, v164, v165\n"
    "v_mfma_f32_16x16x32_bf16 v[8:11], a[216:219], v[188:191], v[8:11]\n"
    "v_exp_f32 v166, v166\n"
    "v_mfma_f32_16x16x32_bf16 v[8:11], a[184:187], v[184:187], v[8:11]\n"
    "v_add_f32 v231, v192, v193\n"
    "v_mfma_f32_16x16x32_bf16 v[12:15], a[188:191], v[184:187], v[12:15]\n"
    "v_exp_f32 v167, v167\n"
    "v_mfma_f32_16x16x32_bf16 v[12:15], a[220:223], v[188:191], v[12:15]\n"
    "v_cvt_pk_bf16_f32 v179, v166, v167\n"
    "v_mfma_f32_16x16x32_bf16 v[16:19], a[224:227], v[188:191], v[16:19]\n"
    "v_exp_f32 v168, v168\n"
    "v_mfma_f32_16x16x32_bf16 v[16:19], a[192:195], v[184:187], v[16:19]\n"
    "v_rcp_f32 v230, v231\n"
    "v_mfma_f32_16x16x32_bf16 v[20:23], a[196:199], v[184:187], v[20:23]\n"
    "v_exp_f32 v169, v169\n"
    "v_mfma_f32_16x16x32_bf16 v[20:23], a[228:231], v[188:191], v[20:23]\n"
    "v_cvt_pk_bf16_f32 v180, v168, v169\n"
    "v_subrev_u32 v234, s75, v231\n"
    "v_mfma_f32_16x16x32_bf16 v[24:27], a[232:235], v[188:191], v[24:27]\n"
    "v_exp_f32 v170, v170\n"
    "v_mfma_f32_16x16x32_bf16 v[24:27], a[200:203], v[184:187], v[24:27]\n"
    "v_cmp_lt_u32 vcc, s77, v234\n"
    "v_mfma_f32_16x16x32_bf16 v[28:31], a[204:207], v[184:187], v[28:31]\n"
    "v_exp_f32 v171, v171\n"
    "v_mfma_f32_16x16x32_bf16 v[28:31], a[236:239], v[188:191], v[28:31]\n"
    "v_cvt_pk_bf16_f32 v181, v170, v171\n"
    "v_mfma_f32_16x16x32_bf16 v[160:163], a[80:83], a[32:35], -4.0\n"
    "s_cmp_lg_u64 vcc, 0\n"
    "s_addc_u32 s78, s78, s78\n"
    "v_exp_f32 v172, v172\n"
    "v_mfma_f32_16x16x32_bf16 v[160:163], a[84:87], a[36:39], v[160:163]\n"
    "v_mfma_f32_16x16x32_bf16 v[160:163], a[88:91], a[40:43], v[160:163]\n"
    "v_exp_f32 v173, v173\n"
    "v_mfma_f32_16x16x32_bf16 v[160:163], a[92:95], a[44:47], v[160:163]\n"
    "v_cvt_pk_bf16_f32 v182, v172, v173\n"
    "v_mfma_f32_16x16x32_bf16 v[164:167], a[108:111], a[44:47], -4.0\n"
    "v_exp_f32 v174, v174\n"
    "v_mfma_f32_16x16x32_bf16 v[164:167], a[104:107], a[40:43], v[164:167]\n"
    "v_pk_mul_f32 v[0:1], v[0:1], v[230:231] op_sel_hi:[1,0]\n"
    "v_pk_mul_f32 v[2:3], v[2:3], v[230:231] op_sel_hi:[1,0]\n"
    "v_pk_mul_f32 v[4:5], v[4:5], v[230:231] op_sel_hi:[1,0]\n"
    "v_pk_mul_f32 v[6:7], v[6:7], v[230:231] op_sel_hi:[1,0]\n"
    "v_pk_mul_f32 v[8:9], v[8:9], v[230:231] op_sel_hi:[1,0]\n"
    "v_pk_mul_f32 v[10:11], v[10:11], v[230:231] op_sel_hi:[1,0]\n"
    "v_pk_mul_f32 v[12:13], v[12:13], v[230:231] op_sel_hi:[1,0]\n"
    "v_pk_mul_f32 v[14:15], v[14:15], v[230:231] op_sel_hi:[1,0]\n"
    "v_pk_mul_f32 v[16:17], v[16:17], v[230:231] op_sel_hi:[1,0]\n"
    "v_pk_mul_f32 v[18:19], v[18:19], v[230:231] op_sel_hi:[1,0]\n"
    "v_pk_mul_f32 v[20:21], v[20:21], v[230:231] op_sel_hi:[1,0]\n"
    "v_pk_mul_f32 v[22:23], v[22:23], v[230:231] op_sel_hi:[1,0]\n"
    "v_pk_mul_f32 v[24:25], v[24:25], v[230:231] op_sel_hi:[1,0]\n"
    "v_pk_mul_f32 v[26:27], v[26:27], v[230:231] op_sel_hi:[1,0]\n"
    "v_pk_mul_f32 v[28:29], v[28:29], v[230:231] op_sel_hi:[1,0]\n"
    "v_pk_mul_f32 v[30:31], v[30:31], v[230:231] op_sel_hi:[1,0]\n"
    "v_mfma_f32_16x16x32_bf16 v[164:167], a[100:103], a[36:39], v[164:167]\n"
    "v_exp_f32 v175, v175\n"
    "v_mfma_f32_16x16x32_bf16 v[164:167], a[96:99], a[32:35], v[164:167]\n"
    "v_cvt_pk_bf16_f32 v183, v174, v175\n"
    "v_cvt_pk_bf16_f32 v0, v0, v1\n"
    "v_mfma_f32_16x16x32_bf16 v[168:171], a[112:115], a[32:35], -4.0\n"
    "v_cvt_pk_bf16_f32 v1, v2, v3\n"
    "v_cvt_pk_bf16_f32 v2, v4, v5\n"
    "v_mfma_f32_16x16x32_bf16 v[168:171], a[116:119], a[36:39], v[168:171]\n"
    "v_exp_f32 v160, v160\n"
    "v_mfma_f32_16x16x32_bf16 v[168:171], a[120:123], a[40:43], v[168:171]\n"
    "v_cvt_pk_bf16_f32 v3, v6, v7\n"
    "v_cvt_pk_bf16_f32 v8, v8, v9\n"
    "v_mfma_f32_16x16x32_bf16 v[168:171], a[124:127], a[44:47], v[168:171]\n"
    "ds_write_b128 v220, v[0:3] offset:0\n"
    "v_exp_f32 v161, v161\n"
    "v_mfma_f32_16x16x32_bf16 v[172:175], a[140:143], a[44:47], -4.0\n"
    "v_cvt_pk_bf16_f32 v184, v160, v161\n"
    "v_cvt_pk_bf16_f32 v9, v10, v11\n"
    "v_mfma_f32_16x16x32_bf16 v[172:175], a[136:139], a[40:43], v[172:175]\n"
    "v_exp_f32 v162, v162\n"
    "v_mfma_f32_16x16x32_bf16 v[172:175], a[132:135], a[36:39], v[172:175]\n"
    "v_cvt_pk_bf16_f32 v10, v12, v13\n"
    "v_cvt_pk_bf16_f32 v11, v14, v15\n"
    "v_mfma_f32_16x16x32_bf16 v[172:175], a[128:131], a[32:35], v[172:175]\n"
    "ds_write_b128 v221, v[8:11] offset:0\n"
    "v_exp_f32 v163, v163\n"
    "v_smfmac_f32_16x16x64_bf16 v[196:199], a[240:243], v[176:183], v237\n"
    "v_cvt_pk_bf16_f32 v185, v162, v163\n"
    "v_mfma_f32_16x16x32_bf16 v[32:35], a[208:211], v[180:183], v[32:35]\n"
    "v_exp_f32 v164, v164\n"
    "v_mfma_f32_16x16x32_bf16 v[32:35], a[176:179], v[176:179], v[32:35]\n"
    "v_cvt_pk_bf16_f32 v16, v16, v17\n"
    "v_cvt_pk_bf16_f32 v17, v18, v19\n"
    "v_mfma_f32_16x16x32_bf16 v[36:39], a[180:183], v[176:179], v[36:39]\n"
    "v_exp_f32 v165, v165\n"
    "v_mfma_f32_16x16x32_bf16 v[36:39], a[212:215], v[180:183], v[36:39]\n"
    "v_cvt_pk_bf16_f32 v186, v164, v165\n"
    "v_cvt_pk_bf16_f32 v18, v20, v21\n"
    "v_mfma_f32_16x16x32_bf16 v[40:43], a[216:219], v[180:183], v[40:43]\n"
    "v_exp_f32 v166, v166\n"
    "v_mfma_f32_16x16x32_bf16 v[40:43], a[184:187], v[176:179], v[40:43]\n"
    "v_add_f32 v233, v196, v197\n"
    "v_cvt_pk_bf16_f32 v19, v22, v23\n"
    "v_mfma_f32_16x16x32_bf16 v[44:47], a[188:191], v[176:179], v[44:47]\n"
    "ds_write_b128 v222, v[16:19] offset:0\n"
    "v_exp_f32 v167, v167\n"
    "v_mfma_f32_16x16x32_bf16 v[44:47], a[220:223], v[180:183], v[44:47]\n"
    "v_cvt_pk_bf16_f32 v187, v166, v167\n"
    "v_cvt_pk_bf16_f32 v24, v24, v25\n"
    "v_mfma_f32_16x16x32_bf16 v[48:51], a[224:227], v[180:183], v[48:51]\n"
    "v_exp_f32 v168, v168\n"
    "v_mfma_f32_16x16x32_bf16 v[48:51], a[192:195], v[176:179], v[48:51]\n"
    "v_rcp_f32 v232, v233\n"
    "v_mfma_f32_16x16x32_bf16 v[52:55], a[196:199], v[176:179], v[52:55]\n"
    "v_exp_f32 v169, v169\n"
    "v_mfma_f32_16x16x32_bf16 v[52:55], a[228:231], v[180:183], v[52:55]\n"
    "v_cvt_pk_bf16_f32 v188, v168, v169\n"
    "v_subrev_u32 v234, s75, v233\n"
    "v_mfma_f32_16x16x32_bf16 v[56:59], a[232:235], v[180:183], v[56:59]\n"
    "v_exp_f32 v170, v170\n"
    "v_mfma_f32_16x16x32_bf16 v[56:59], a[200:203], v[176:179], v[56:59]\n"
    "v_cmp_lt_u32 vcc, s77, v234\n"
    "v_cvt_pk_bf16_f32 v25, v26, v27\n"
    "v_mfma_f32_16x16x32_bf16 v[60:63], a[204:207], v[176:179], v[60:63]\n"
    "v_exp_f32 v171, v171\n"
    "v_mfma_f32_16x16x32_bf16 v[60:63], a[236:239], v[180:183], v[60:63]\n"
    "v_cvt_pk_bf16_f32 v189, v170, v171\n"
    "v_cvt_pk_bf16_f32 v26, v28, v29\n"
    "v_mfma_f32_16x16x32_bf16 v[160:163], a[80:83], a[48:51], -4.0\n"
    "s_cmp_lg_u64 vcc, 0\n"
    "s_addc_u32 s78, s78, s78\n"
    "v_exp_f32 v172, v172\n"
    "v_mfma_f32_16x16x32_bf16 v[160:163], a[84:87], a[52:55], v[160:163]\n"
    "v_cvt_pk_bf16_f32 v27, v30, v31\n"
    "v_mfma_f32_16x16x32_bf16 v[160:163], a[88:91], a[56:59], v[160:163]\n"
    "ds_write_b128 v223, v[24:27] offset:0\n"
    "v_exp_f32 v173, v173\n"
    "v_mfma_f32_16x16x32_bf16 v[160:163], a[92:95], a[60:63], v[160:163]\n"
    "ds_read_b128 a[0:3], v224 offset:0\n"
    "v_cvt_pk_bf16_f32 v190, v172, v173\n"
    "v_mfma_f32_16x16x32_bf16 v[164:167], a[108:111], a[60:63], -4.0\n"
    "v_exp_f32 v174, v174\n"
    "v_mfma_f32_16x16x32_bf16 v[164:167], a[104:107], a[56:59], v[164:167]\n"
    "v_pk_mul_f32 v[32:33], v[32:33], v[232:233] op_sel_hi:[1,0]\n"
    "v_pk_mul_f32 v[34:35], v[34:35], v[232:233] op_sel_hi:[1,0]\n"
    "v_pk_mul_f32 v[36:37], v[36:37], v[232:233] op_sel_hi:[1,0]\n"
    "v_pk_mul_f32 v[38:39], v[38:39], v[232:233] op_sel_hi:[1,0]\n"
    "v_pk_mul_f32 v[40:41], v[40:41], v[232:233] op_sel_hi:[1,0]\n"
    "v_pk_mul_f32 v[42:43], v[42:43], v[232:233] op_sel_hi:[1,0]\n"
    "v_pk_mul_f32 v[44:45], v[44:45], v[232:233] op_sel_hi:[1,0]\n"
    "v_pk_mul_f32 v[46:47], v[46:47], v[232:233] op_sel_hi:[1,0]\n"
    "v_pk_mul_f32 v[48:49], v[48:49], v[232:233] op_sel_hi:[1,0]\n"
    "v_pk_mul_f32 v[50:51], v[50:51], v[232:233] op_sel_hi:[1,0]\n"
    "v_pk_mul_f32 v[52:53], v[52:53], v[232:233] op_sel_hi:[1,0]\n"
    "v_pk_mul_f32 v[54:55], v[54:55], v[232:233] op_sel_hi:[1,0]\n"
    "v_pk_mul_f32 v[56:57], v[56:57], v[232:233] op_sel_hi:[1,0]\n"
    "v_pk_mul_f32 v[58:59], v[58:59], v[232:233] op_sel_hi:[1,0]\n"
    "v_pk_mul_f32 v[60:61], v[60:61], v[232:233] op_sel_hi:[1,0]\n"
    "v_pk_mul_f32 v[62:63], v[62:63], v[232:233] op_sel_hi:[1,0]\n"
    "v_mfma_f32_16x16x32_bf16 v[164:167], a[100:103], a[52:55], v[164:167]\n"
    "v_exp_f32 v175, v175\n"
    "v_mfma_f32_16x16x32_bf16 v[164:167], a[96:99], a[48:51], v[164:167]\n"
    "ds_read_b128 a[4:7], v225 offset:1024\n"
    "v_cvt_pk_bf16_f32 v191, v174, v175\n"
    "v_mfma_f32_16x16x32_bf16 v[168:171], a[112:115], a[48:51], -4.0\n"
    "ds_read_b128 a[8:11], v226 offset:2048\n"
    "ds_read_b128 a[12:15], v227 offset:3072\n"
    "v_mfma_f32_16x16x32_bf16 v[168:171], a[116:119], a[52:55], v[168:171]\n"
    "v_exp_f32 v160, v160\n"
    "v_mfma_f32_16x16x32_bf16 v[168:171], a[120:123], a[56:59], v[168:171]\n"
    "v_cvt_pk_bf16_f32 v32, v32, v33\n"
    "v_cvt_pk_bf16_f32 v33, v34, v35\n"
    "v_mfma_f32_16x16x32_bf16 v[168:171], a[124:127], a[60:63], v[168:171]\n"
    "s_add_u32 s56, s64, 0\n"
    "v_exp_f32 v161, v161\n"
    "v_mfma_f32_16x16x32_bf16 v[172:175], a[140:143], a[60:63], -4.0\n"
    "s_waitcnt lgkmcnt(3)\n"
    "buffer_store_dwordx4 a[0:3], v228, s[48:51], s56 offen nt\n"
    "v_cvt_pk_bf16_f32 v176, v160, v161\n"
    "v_cvt_pk_bf16_f32 v34, v36, v37\n"
    "v_mfma_f32_16x16x32_bf16 v[172:175], a[136:139], a[56:59], v[172:175]\n"
    "v_exp_f32 v162, v162\n"
    "v_mfma_f32_16x16x32_bf16 v[172:175], a[132:135], a[52:55], v[172:175]\n"
    "v_cvt_pk_bf16_f32 v35, v38, v39\n"
    "v_cvt_pk_bf16_f32 v40, v40, v41\n"
    "v_mfma_f32_16x16x32_bf16 v[172:175], a[128:131], a[48:51], v[172:175]\n"
    "ds_write_b128 v220, v[32:35] offset:4096\n"
    "v_exp_f32 v163, v163\n"
    "v_smfmac_f32_16x16x64_bf16 v[200:203], a[240:243], v[184:191], v237\n"
    "s_waitcnt lgkmcnt(1)\n"
    "buffer_store_dwordx4 a[4:7], v228, s[48:51], s56 offen offset:1024 nt\n"
    "v_cvt_pk_bf16_f32 v177, v162, v163\n"
    "v_mfma_f32_16x16x32_bf16 v[64:67], a[208:211], v[188:191], v[64:67]\n"
    "v_exp_f32 v164, v164\n"
    "v_mfma_f32_16x16x32_bf16 v[64:67], a[176:179], v[184:187], v[64:67]\n"
    "v_cvt_pk_bf16_f32 v41, v42, v43\n"
    "v_cvt_pk_bf16_f32 v42, v44, v45\n"
    "v_mfma_f32_16x16x32_bf16 v[68:71], a[180:183], v[184:187], v[68:71]\n"
    "v_exp_f32 v165, v165\n"
    "v_mfma_f32_16x16x32_bf16 v[68:71], a[212:215], v[188:191], v[68:71]\n"
    "buffer_store_dwordx4 a[8:11], v228, s[48:51], s56 offen offset:2048 nt\n"
    "v_cvt_pk_bf16_f32 v178, v164, v165\n"
    "v_cvt_pk_bf16_f32 v43, v46, v47\n"
    "v_mfma_f32_16x16x32_bf16 v[72:75], a[216:219], v[188:191], v[72:75]\n"
    "ds_write_b128 v221, v[40:43] offset:4096\n"
    "v_exp_f32 v166, v166\n"
    "v_mfma_f32_16x16x32_bf16 v[72:75], a[184:187], v[184:187], v[72:75]\n"
    "v_add_f32 v231, v200, v201\n"
    "v_cvt_pk_bf16_f32 v48, v48, v49\n"
    "v_mfma_f32_16x16x32_bf16 v[76:79], a[188:191], v[184:187], v[76:79]\n"
    "v_exp_f32 v167, v167\n"
    "v_mfma_f32_16x16x32_bf16 v[76:79], a[220:223], v[188:191], v[76:79]\n"
    "buffer_store_dwordx4 a[12:15], v228, s[48:51], s56 offen offset:3072 nt\n"
    "v_cvt_pk_bf16_f32 v179, v166, v167\n"
    "v_cvt_pk_bf16_f32 v49, v50, v51\n"
    "v_mfma_f32_16x16x32_bf16 v[80:83], a[224:227], v[188:191], v[80:83]\n"
    "v_exp_f32 v168, v168\n"
    "v_mfma_f32_16x16x32_bf16 v[80:83], a[192:195], v[184:187], v[80:83]\n"
    "v_rcp_f32 v230, v231\n"
    "v_mfma_f32_16x16x32_bf16 v[84:87], a[196:199], v[184:187], v[84:87]\n"
    "v_exp_f32 v169, v169\n"
    "v_mfma_f32_16x16x32_bf16 v[84:87], a[228:231], v[188:191], v[84:87]\n"
    "v_cvt_pk_bf16_f32 v180, v168, v169\n"
    "v_subrev_u32 v234, s75, v231\n"
    "v_mfma_f32_16x16x32_bf16 v[88:91], a[232:235], v[188:191], v[88:91]\n"
    "v_exp_f32 v170, v170\n"
    "v_mfma_f32_16x16x32_bf16 v[88:91], a[200:203], v[184:187], v[88:91]\n"
    "v_cmp_lt_u32 vcc, s77, v234\n"
    "v_cvt_pk_bf16_f32 v50, v52, v53\n"
    "v_mfma_f32_16x16x32_bf16 v[92:95], a[204:207], v[184:187], v[92:95]\n"
    "v_exp_f32 v171, v171\n"
    "v_mfma_f32_16x16x32_bf16 v[92:95], a[236:239], v[188:191], v[92:95]\n"
    "v_cvt_pk_bf16_f32 v181, v170, v171\n"
    "v_cvt_pk_bf16_f32 v51, v54, v55\n"
    "v_mfma_f32_16x16x32_bf16 v[160:163], a[80:83], a[64:67], -4.0\n"
    "s_cmp_lg_u64 vcc, 0\n"
    "s_addc_u32 s78, s78, s78\n"
    "ds_write_b128 v222, v[48:51] offset:4096\n"
    "v_exp_f32 v172, v172\n"
    "v_mfma_f32_16x16x32_bf16 v[160:163], a[84:87], a[68:71], v[160:163]\n"
    "v_cvt_pk_bf16_f32 v56, v56, v57\n"
    "v_cvt_pk_bf16_f32 v57, v58, v59\n"
    "v_mfma_f32_16x16x32_bf16 v[160:163], a[88:91], a[72:75], v[160:163]\n"
    "v_exp_f32 v173, v173\n"
    "v_mfma_f32_16x16x32_bf16 v[160:163], a[92:95], a[76:79], v[160:163]\n"
    "v_cvt_pk_bf16_f32 v182, v172, v173\n"
    "v_cvt_pk_bf16_f32 v58, v60, v61\n"
    "v_mfma_f32_16x16x32_bf16 v[164:167], a[108:111], a[76:79], -4.0\n"
    "v_exp_f32 v174, v174\n"
    "v_mfma_f32_16x16x32_bf16 v[164:167], a[104:107], a[72:75], v[164:167]\n"
    "v_cvt_pk_bf16_f32 v59, v62, v63\n"
    "v_pk_mul_f32 v[64:65], v[64:65], v[230:231] op_sel_hi:[1,0]\n"
    "v_pk_mul_f32 v[66:67], v[66:67], v[230:231] op_sel_hi:[1,0]\n"
    "v_pk_mul_f32 v[68:69], v[68:69], v[230:231] op_sel_hi:[1,0]\n"
    "v_pk_mul_f32 v[70:71], v[70:71], v[230:231] op_sel_hi:[1,0]\n"
    "v_pk_mul_f32 v[72:73], v[72:73], v[230:231] op_sel_hi:[1,0]\n"
    "v_pk_mul_f32 v[74:75], v[74:75], v[230:231] op_sel_hi:[1,0]\n"
    "v_pk_mul_f32 v[76:77], v[76:77], v[230:231] op_sel_hi:[1,0]\n"
    "v_pk_mul_f32 v[78:79], v[78:79], v[230:231] op_sel_hi:[1,0]\n"
    "v_pk_mul_f32 v[80:81], v[80:81], v[230:231] op_sel_hi:[1,0]\n"
    "v_pk_mul_f32 v[82:83], v[82:83], v[230:231] op_sel_hi:[1,0]\n"
    "v_pk_mul_f32 v[84:85], v[84:85], v[230:231] op_sel_hi:[1,0]\n"
    "v_pk_mul_f32 v[86:87], v[86:87], v[230:231] op_sel_hi:[1,0]\n"
    "v_pk_mul_f32 v[88:89], v[88:89], v[230:231] op_sel_hi:[1,0]\n"
    "v_pk_mul_f32 v[90:91], v[90:91], v[230:231] op_sel_hi:[1,0]\n"
    "v_pk_mul_f32 v[92:93], v[92:93], v[230:231] op_sel_hi:[1,0]\n"
    "v_pk_mul_f32 v[94:95], v[94:95], v[230:231] op_sel_hi:[1,0]\n"
    "v_mfma_f32_16x16x32_bf16 v[164:167], a[100:103], a[68:71], v[164:167]\n"
    "ds_write_b128 v223, v[56:59] offset:4096\n"
    "v_exp_f32 v175, v175\n"
    "v_mfma_f32_16x16x32_bf16 v[164:167], a[96:99], a[64:67], v[164:167]\n"
    "ds_read_b128 a[16:19], v224 offset:4096\n"
    "v_cvt_pk_bf16_f32 v183, v174, v175\n"
    "v_mfma_f32_16x16x32_bf16 v[168:171], a[112:115], a[64:67], -4.0\n"
    "ds_read_b128 a[20:23], v225 offset:5120\n"
    "ds_read_b128 a[24:27], v226 offset:6144\n"
    "v_mfma_f32_16x16x32_bf16 v[168:171], a[116:119], a[68:71], v[168:171]\n"
    "v_exp_f32 v160, v160\n"
    "v_mfma_f32_16x16x32_bf16 v[168:171], a[120:123], a[72:75], v[168:171]\n"
    "ds_read_b128 a[28:31], v227 offset:7168\n"
    "v_cvt_pk_bf16_f32 v64, v64, v65\n"
    "v_mfma_f32_16x16x32_bf16 v[168:171], a[124:127], a[76:79], v[168:171]\n"
    "v_exp_f32 v161, v161\n"
    "v_mfma_f32_16x16x32_bf16 v[172:175], a[140:143], a[76:79], -4.0\n"
    "v_cvt_pk_bf16_f32 v184, v160, v161\n"
    "v_cvt_pk_bf16_f32 v65, v66, v67\n"
    "v_mfma_f32_16x16x32_bf16 v[172:175], a[136:139], a[72:75], v[172:175]\n"
    "v_exp_f32 v162, v162\n"
    "v_mfma_f32_16x16x32_bf16 v[172:175], a[132:135], a[68:71], v[172:175]\n"
    "v_cvt_pk_bf16_f32 v66, v68, v69\n"
    "v_cvt_pk_bf16_f32 v67, v70, v71\n"
    "v_mfma_f32_16x16x32_bf16 v[172:175], a[128:131], a[64:67], v[172:175]\n"
    "s_add_u32 s56, s64, 4096\n"
    "ds_write_b128 v220, v[64:67] offset:8192\n"
    "v_exp_f32 v163, v163\n"
    "v_smfmac_f32_16x16x64_bf16 v[204:207], a[240:243], v[176:183], v237\n"
    "s_waitcnt lgkmcnt(2)\n"
    "buffer_store_dwordx4 a[16:19], v228, s[48:51], s56 offen nt\n"
    "v_cvt_pk_bf16_f32 v185, v162, v163\n"
    "v_mfma_f32_16x16x32_bf16 v[96:99], a[208:211], v[180:183], v[96:99]\n"
    "v_exp_f32 v164, v164\n"
    "v_mfma_f32_16x16x32_bf16 v[96:99], a[176:179], v[176:179], v[96:99]\n"
    "v_cvt_pk_bf16_f32 v72, v72, v73\n"
    "v_cvt_pk_bf16_f32 v73, v74, v75\n"
    "v_mfma_f32_16x16x32_bf16 v[100:103], a[180:183], v[176:179], v[100:103]\n"
    "v_exp_f32 v165, v165\n"
    "v_mfma_f32_16x16x32_bf16 v[100:103], a[212:215], v[180:183], v[100:103]\n"
    "buffer_store_dwordx4 a[20:23], v228, s[48:51], s56 offen offset:1024 nt\n"
    "v_cvt_pk_bf16_f32 v186, v164, v165\n"
    "v_cvt_pk_bf16_f32 v74, v76, v77\n"
    "v_mfma_f32_16x16x32_bf16 v[104:107], a[216:219], v[180:183], v[104:107]\n"
    "v_exp_f32 v166, v166\n"
    "v_mfma_f32_16x16x32_bf16 v[104:107], a[184:187], v[176:179], v[104:107]\n"
    "v_add_f32 v233, v204, v205\n"
    "v_cvt_pk_bf16_f32 v75, v78, v79\n"
    "v_mfma_f32_16x16x32_bf16 v[108:111], a[188:191], v[176:179], v[108:111]\n"
    "ds_write_b128 v221, v[72:75] offset:8192\n"
    "v_exp_f32 v167, v167\n"
    "v_mfma_f32_16x16x32_bf16 v[108:111], a[220:223], v[180:183], v[108:111]\n"
    "buffer_store_dwordx4 a[24:27], v228, s[48:51], s56 offen offset:2048 nt\n"
    "v_cvt_pk_bf16_f32 v187, v166, v167\n"
    "v_cvt_pk_bf16_f32 v80, v80, v81\n"
    "v_mfma_f32_16x16x32_bf16 v[112:115], a[224:227], v[180:183], v[112:115]\n"
    "v_exp_f32 v168, v168\n"
    "v_mfma_f32_16x16x32_bf16 v[112:115], a[192:195], v[176:179], v[112:115]\n"
    "v_rcp_f32 v232, v233\n"
    "v_mfma_f32_16x16x32_bf16 v[116:119], a[196:199], v[176:179], v[116:119]\n"
    "v_exp_f32 v169, v169\n"
    "v_mfma_f32_16x16x32_bf16 v[116:119], a[228:231], v[180:183], v[116:119]\n"
    "s_waitcnt lgkmcnt(1)\n"
    "buffer_store_dwordx4 a[28:31], v228, s[48:51], s56 offen offset:3072 nt\n"
    "v_cvt_pk_bf16_f32 v188, v168, v169\n"
    "v_subrev_u32 v234, s75, v233\n"
    "v_mfma_f32_16x16x32_bf16 v[120:123], a[232:235], v[180:183], v[120:123]\n"
    "v_exp_f32 v170, v170\n"
    "v_mfma_f32_16x16x32_bf16 v[120:123], a[200:203], v[176:179], v[120:123]\n"
    "v_cmp_lt_u32 vcc, s77, v234\n"
    "v_cvt_pk_bf16_f32 v81, v82, v83\n"
    "v_mfma_f32_16x16x32_bf16 v[124:127], a[204:207], v[176:179], v[124:127]\n"
    "v_exp_f32 v171, v171\n"
    "v_mfma_f32_16x16x32_bf16 v[124:127], a[236:239], v[180:183], v[124:127]\n"
    "v_cvt_pk_bf16_f32 v189, v170, v171\n"
    "v_cvt_pk_bf16_f32 v82, v84, v85\n"
    "s_cmp_lg_u64 vcc, 0\n"
    "s_addc_u32 s78, s78, s78\n"
    "v_exp_f32 v172, v172\n"
    "v_cvt_pk_bf16_f32 v83, v86, v87\n"
    "v_cvt_pk_bf16_f32 v88, v88, v89\n"
    "ds_write_b128 v222, v[80:83] offset:8192\n"
    "v_exp_f32 v173, v173\n"
    "s_nop 0\n"
    "v_cvt_pk_bf16_f32 v190, v172, v173\n"
    "v_cvt_pk_bf16_f32 v89, v90, v91\n"
    "v_exp_f32 v174, v174\n"
    "v_cvt_pk_bf16_f32 v90, v92, v93\n"
    "v_cvt_pk_bf16_f32 v91, v94, v95\n"
    "v_pk_mul_f32 v[96:97], v[96:97], v[232:233] op_sel_hi:[1,0]\n"
    "v_pk_mul_f32 v[98:99], v[98:99], v[232:233] op_sel_hi:[1,0]\n"
    "v_pk_mul_f32 v[100:101], v[100:101], v[232:233] op_sel_hi:[1,0]\n"
    "v_pk_mul_f32 v[102:103], v[102:103], v[232:233] op_sel_hi:[1,0]\n"
    "v_pk_mul_f32 v[104:105], v[104:105], v[232:233] op_sel_hi:[1,0]\n"
    "v_pk_mul_f32 v[106:107], v[106:107], v[232:233] op_sel_hi:[1,0]\n"
    "v_pk_mul_f32 v[108:109], v[108:109], v[232:233] op_sel_hi:[1,0]\n"
    "v_pk_mul_f32 v[110:111], v[110:111], v[232:233] op_sel_hi:[1,0]\n"
    "v_pk_mul_f32 v[112:113], v[112:113], v[232:233] op_sel_hi:[1,0]\n"
    "v_pk_mul_f32 v[114:115], v[114:115], v[232:233] op_sel_hi:[1,0]\n"
    "v_pk_mul_f32 v[116:117], v[116:117], v[232:233] op_sel_hi:[1,0]\n"
    "v_pk_mul_f32 v[118:119], v[118:119], v[232:233] op_sel_hi:[1,0]\n"
    "v_pk_mul_f32 v[120:121], v[120:121], v[232:233] op_sel_hi:[1,0]\n"
    "v_pk_mul_f32 v[122:123], v[122:123], v[232:233] op_sel_hi:[1,0]\n"
    "v_pk_mul_f32 v[124:125], v[124:125], v[232:233] op_sel_hi:[1,0]\n"
    "v_pk_mul_f32 v[126:127], v[126:127], v[232:233] op_sel_hi:[1,0]\n"
    "ds_write_b128 v223, v[88:91] offset:8192\n"
    "v_exp_f32 v175, v175\n"
    "ds_read_b128 a[32:35], v224 offset:8192\n"
    "v_cvt_pk_bf16_f32 v191, v174, v175\n"
    "ds_read_b128 a[36:39], v225 offset:9216\n"
    "ds_read_b128 a[40:43], v226 offset:10240\n"
    "ds_read_b128 a[44:47], v227 offset:11264\n"
    "v_cvt_pk_bf16_f32 v96, v96, v97\n"
    "v_cvt_pk_bf16_f32 v97, v98, v99\n"
    "v_cvt_pk_bf16_f32 v98, v100, v101\n"
    "v_cvt_pk_bf16_f32 v99, v102, v103\n"
    "v_cvt_pk_bf16_f32 v104, v104, v105\n"
    "ds_write_b128 v220, v[96:99] offset:12288\n"
    "v_cvt_pk_bf16_f32 v105, v106, v107\n"
    "v_cvt_pk_bf16_f32 v106, v108, v109\n"
    "v_cvt_pk_bf16_f32 v107, v110, v111\n"
    "v_cvt_pk_bf16_f32 v112, v112, v113\n"
    "ds_write_b128 v221, v[104:107] offset:12288\n"
    "v_cvt_pk_bf16_f32 v113, v114, v115\n"
    "v_cvt_pk_bf16_f32 v114, v116, v117\n"
    "v_cvt_pk_bf16_f32 v115, v118, v119\n"
    "v_cvt_pk_bf16_f32 v120, v120, v121\n"
    "v_smfmac_f32_16x16x64_bf16 v[208:211], a[240:243], v[184:191], v237\n"
    "s_add_u32 s56, s64, 8192\n"
    "ds_write_b128 v222, v[112:115] offset:12288\n"
    "v_cvt_pk_bf16_f32 v121, v122, v123\n"
    "v_mfma_f32_16x16x32_bf16 v[128:131], a[208:211], v[188:191], v[128:131]\n"
    "s_waitcnt lgkmcnt(6)\n"
    "buffer_store_dwordx4 a[32:35], v228, s[48:51], s56 offen nt\n"
    "v_cvt_pk_bf16_f32 v122, v124, v125\n"
    "v_cvt_pk_bf16_f32 v123, v126, v127\n"
    "v_mfma_f32_16x16x32_bf16 v[128:131], a[176:179], v[184:187], v[128:131]\n"
    "ds_write_b128 v223, v[120:123] offset:12288\n"
    "v_mfma_f32_16x16x32_bf16 v[132:135], a[180:183], v[184:187], v[132:135]\n"
    "ds_read_b128 a[48:51], v224 offset:12288\n"
    "ds_read_b128 a[52:55], v225 offset:13312\n"
    "v_mfma_f32_16x16x32_bf16 v[132:135], a[212:215], v[188:191], v[132:135]\n"
    "ds_read_b128 a[56:59], v226 offset:14336\n"
    "ds_read_b128 a[60:63], v227 offset:15360\n"
    "v_mfma_f32_16x16x32_bf16 v[136:139], a[216:219], v[188:191], v[136:139]\n"
    "s_waitcnt lgkmcnt(10)\n"
    "buffer_store_dwordx4 a[36:39], v228, s[48:51], s56 offen offset:1024 nt\n"
    "v_mfma_f32_16x16x32_bf16 v[136:139], a[184:187], v[184:187], v[136:139]\n"
    "v_add_f32 v231, v208, v209\n"
    "v_mfma_f32_16x16x32_bf16 v[140:143], a[188:191], v[184:187], v[140:143]\n"
    "v_rcp_f32 v230, v231\n"
    "v_mfma_f32_16x16x32_bf16 v[140:143], a[220:223], v[188:191], v[140:143]\n"
    "v_subrev_u32 v234, s75, v231\n"
    "v_cmp_lt_u32 vcc, s77, v234\n"
    "v_mfma_f32_16x16x32_bf16 v[144:147], a[224:227], v[188:191], v[144:147]\n"
    "s_waitcnt lgkmcnt(5)\n"
    "buffer_store_dwordx4 a[40:43], v228, s[48:51], s56 offen offset:2048 nt\n"
    "v_mfma_f32_16x16x32_bf16 v[144:147], a[192:195], v[184:187], v[144:147]\n"
    "v_pk_mul_f32 v[128:129], v[128:129], v[230:231] op_sel_hi:[1,0]\n"
    "v_pk_mul_f32 v[130:131], v[130:131], v[230:231] op_sel_hi:[1,0]\n"
    "v_pk_mul_f32 v[132:133], v[132:133], v[230:231] op_sel_hi:[1,0]\n"
    "v_pk_mul_f32 v[134:135], v[134:135], v[230:231] op_sel_hi:[1,0]\n"
    "v_mfma_f32_16x16x32_bf16 v[148:151], a[196:199], v[184:187], v[148:151]\n"
    "s_cmp_lg_u64 vcc, 0\n"
    "s_addc_u32 s78, s78, s78\n"
    "v_cvt_pk_bf16_f32 v128, v128, v129\n"
    "v_cvt_pk_bf16_f32 v129, v130, v131\n"
    "v_mfma_f32_16x16x32_bf16 v[148:151], a[228:231], v[188:191], v[148:151]\n"
    "v_cvt_pk_bf16_f32 v130, v132, v133\n"
    "v_cvt_pk_bf16_f32 v131, v134, v135\n"
    "v_mfma_f32_16x16x32_bf16 v[152:155], a[232:235], v[188:191], v[152:155]\n"
    "ds_write_b128 v220, v[128:131] offset:16384\n"
    "v_mfma_f32_16x16x32_bf16 v[152:155], a[200:203], v[184:187], v[152:155]\n"
    "v_pk_mul_f32 v[136:137], v[136:137], v[230:231] op_sel_hi:[1,0]\n"
    "v_pk_mul_f32 v[138:139], v[138:139], v[230:231] op_sel_hi:[1,0]\n"
    "v_pk_mul_f32 v[140:141], v[140:141], v[230:231] op_sel_hi:[1,0]\n"
    "v_pk_mul_f32 v[142:143], v[142:143], v[230:231] op_sel_hi:[1,0]\n"
    "v_mfma_f32_16x16x32_bf16 v[156:159], a[204:207], v[184:187], v[156:159]\n"
    "buffer_store_dwordx4 a[44:47], v228, s[48:51], s56 offen offset:3072 nt\n"
    "v_cvt_pk_bf16_f32 v136, v136, v137\n"
    "v_cvt_pk_bf16_f32 v137, v138, v139\n"
    "v_mfma_f32_16x16x32_bf16 v[156:159], a[236:239], v[188:191], v[156:159]\n"
    "v_cvt_pk_bf16_f32 v138, v140, v141\n"
    "v_cvt_pk_bf16_f32 v139, v142, v143\n"
    "ds_write_b128 v221, v[136:139] offset:16384\n"
    "s_add_u32 s56, s64, 12288\n"
    "v_pk_mul_f32 v[144:145], v[144:145], v[230:231] op_sel_hi:[1,0]\n"
    "v_pk_mul_f32 v[146:147], v[146:147], v[230:231] op_sel_hi:[1,0]\n"
    "v_pk_mul_f32 v[148:149], v[148:149], v[230:231] op_sel_hi:[1,0]\n"
    "v_pk_mul_f32 v[150:151], v[150:151], v[230:231] op_sel_hi:[1,0]\n"
    "s_waitcnt lgkmcnt(2)\n"
    "buffer_store_dwordx4 a[48:51], v228, s[48:51], s56 offen nt\n"
    "v_cvt_pk_bf16_f32 v144, v144, v145\n"
    "v_cvt_pk_bf16_f32 v145, v146, v147\n"
    "v_cvt_pk_bf16_f32 v146, v148, v149\n"
    "v_cvt_pk_bf16_f32 v147, v150, v151\n"
    "ds_write_b128 v222, v[144:147] offset:16384\n"
    "v_pk_mul_f32 v[152:153], v[152:153], v[230:231] op_sel_hi:[1,0]\n"
    "v_pk_mul_f32 v[154:155], v[154:155], v[230:231] op_sel_hi:[1,0]\n"
    "v_pk_mul_f32 v[156:157], v[156:157], v[230:231] op_sel_hi:[1,0]\n"
    "v_pk_mul_f32 v[158:159], v[158:159], v[230:231] op_sel_hi:[1,0]\n"
    "buffer_store_dwordx4 a[52:55], v228, s[48:51], s56 offen offset:1024 nt\n"
    "v_cvt_pk_bf16_f32 v152, v152, v153\n"
    "v_cvt_pk_bf16_f32 v153, v154, v155\n"
    "v_cvt_pk_bf16_f32 v154, v156, v157\n"
    "v_cvt_pk_bf16_f32 v155, v158, v159\n"
    "ds_write_b128 v223, v[152:155] offset:16384\n"
    "ds_read_b128 a[64:67], v224 offset:16384\n"
    "ds_read_b128 a[68:71], v225 offset:17408\n"
    "ds_read_b128 a[72:75], v226 offset:18432\n"
    "ds_read_b128 a[76:79], v227 offset:19456\n"
    "buffer_store_dwordx4 a[56:59], v228, s[48:51], s56 offen offset:2048 nt\n"
    "buffer_store_dwordx4 a[60:63], v228, s[48:51], s56 offen offset:3072 nt\n"
    "s_add_u32 s56, s64, 16384\n"
    "s_waitcnt lgkmcnt(3)\n"
    "buffer_store_dwordx4 a[64:67], v228, s[48:51], s56 offen nt\n"
    "s_waitcnt lgkmcnt(2)\n"
    "buffer_store_dwordx4 a[68:71], v228, s[48:51], s56 offen offset:1024 nt\n"
    "s_waitcnt lgkmcnt(1)\n"
    "buffer_store_dwordx4 a[72:75], v228, s[48:51], s56 offen offset:2048 nt\n"
    "s_waitcnt lgkmcnt(0)\n"
    "buffer_store_dwordx4 a[76:79], v228, s[48:51], s56 offen offset:3072 nt\n"
    "s_cmp_lg_u32 s78, 0\n"
    "s_cbranch_scc1 LEND%=\n"
    "s_endpgm\n"
    "LEND%=:\n"
    "s_mov_b32 %[flg], s78\n"
    : [flg] "=s"(flags)
    : [ka] "s"(__builtin_amdgcn_kernarg_segment_ptr()), [w] "s"(w), [lds] "s"(ldsb),
      [wg] "s"(wg)
    : "v0", "v1", "v2", "v3", "v4", "v5", "v6", "v7", "v8", "v9", "v10", "v11", "v12", "v13", "v14", "v15", "v16", "v17", "v18", "v19", "v20", "v21", "v22", "v23", "v24", "v25", "v26", "v27", "v28", "v29", "v30", "v31", "v32", "v33", "v34", "v35", "v36", "v37", "v38", "v39", "v40", "v41", "v42", "v43", "v44", "v45", "v46", "v47", "v48", "v49", "v50", "v51", "v52", "v53", "v54", "v55", "v56", "v57", "v58", "v59", "v60", "v61", "v62", "v63", "v64", "v65", "v66", "v67", "v68", "v69", "v70", "v71", "v72", "v73", "v74", "v75", "v76", "v77", "v78", "v79", "v80", "v81", "v82", "v83", "v84", "v85", "v86", "v87", "v88", "v89", "v90", "v91", "v92", "v93", "v94", "v95", "v96", "v97", "v98", "v99", "v100", "v101", "v102", "v103", "v104", "v105", "v106", "v107", "v108", "v109", "v110", "v111", "v112", "v113", "v114", "v115", "v116", "v117", "v118", "v119", "v120", "v121", "v122", "v123", "v124", "v125", "v126", "v127", "v128", "v129", "v130", "v131", "v132", "v133", "v134", "v135", "v136", "v137", "v138", "v139", "v140", "v141", "v142", "v143", "v144", "v145", "v146", "v147", "v148", "v149", "v150", "v151", "v152", "v153", "v154", "v155", "v156", "v157", "v158", "v159", "v160", "v161", "v162", "v163", "v164", "v165", "v166", "v167", "v168", "v169", "v170", "v171", "v172", "v173", "v174", "v175", "v176", "v177", "v178", "v179", "v180", "v181", "v182", "v183", "v184", "v185", "v186", "v187", "v188", "v189", "v190", "v191", "v192", "v193", "v194", "v195", "v196", "v197", "v198", "v199", "v200", "v201", "v202", "v203", "v204", "v205", "v206", "v207", "v208", "v209", "v210", "v211", "v212", "v213", "v214", "v215", "v216", "v217", "v218", "v219", "v220", "v221", "v222", "v223", "v224", "v225", "v226", "v227", "v228", "v229", "v230", "v231", "v232", "v233", "v234", "v235", "v236", "v237", "v238", "v239", "v240", "v241", "v242", "v243", "v244", "v245", "v246", "v247", "v248", "v249", "v250", "v251", "v252", "v253", "v254", "v255", "a0", "a1", "a2", "a3", "a4", "a5", "a6", "a7", "a8", "a9", "a10", "a11", "a12", "a13", "a14", "a15", "a16", "a17", "a18", "a19", "a20", "a21", "a22", "a23", "a24", "a25", "a26", "a27", "a28", "a29", "a30", "a31", "a32", "a33", "a34", "a35", "a36", "a37", "a38", "a39", "a40", "a41", "a42", "a43", "a44", "a45", "a46", "a47", "a48", "a49", "a50", "a51", "a52", "a53", "a54", "a55", "a56", "a57", "a58", "a59", "a60", "a61", "a62", "a63", "a64", "a65", "a66", "a67", "a68", "a69", "a70", "a71", "a72", "a73", "a74", "a75", "a76", "a77", "a78", "a79", "a80", "a81", "a82", "a83", "a84", "a85", "a86", "a87", "a88", "a89", "a90", "a91", "a92", "a93", "a94", "a95", "a96", "a97", "a98", "a99", "a100", "a101", "a102", "a103", "a104", "a105", "a106", "a107", "a108", "a109", "a110", "a111", "a112", "a113", "a114", "a115", "a116", "a117", "a118", "a119", "a120", "a121", "a122", "a123", "a124", "a125", "a126", "a127", "a128", "a129", "a130", "a131", "a132", "a133", "a134", "a135", "a136", "a137", "a138", "a139", "a140", "a141", "a142", "a143", "a144", "a145", "a146", "a147", "a148", "a149", "a150", "a151", "a152", "a153", "a154", "a155", "a156", "a157", "a158", "a159", "a160", "a161", "a162", "a163", "a164", "a165", "a166", "a167", "a168", "a169", "a170", "a171", "a172", "a173", "a174", "a175", "a176", "a177", "a178", "a179", "a180", "a181", "a182", "a183", "a184", "a185", "a186", "a187", "a188", "a189", "a190", "a191", "a192", "a193", "a194", "a195", "a196", "a197", "a198", "a199", "a200", "a201", "a202", "a203", "a204", "a205", "a206", "a207", "a208", "a209", "a210", "a211", "a212", "a213", "a214", "a215", "a216", "a217", "a218", "a219", "a220", "a221", "a222", "a223", "a224", "a225", "a226", "a227", "a228", "a229", "a230", "a231", "a232", "a233", "a234", "a235", "a236", "a237", "a238", "a239", "a240", "a241", "a242", "a243", "a244", "a245", "a246", "a247", "a248", "a249", "a250", "a251", "a252", "a253", "a254", "a255", "s12", "s13", "s14", "s15", "s16", "s17", "s18", "s19", "s20", "s21", "s22", "s23", "s24", "s25", "s26", "s27", "s28", "s29", "s36", "s37", "s38", "s39", "s40", "s41", "s42", "s43", "s44", "s45", "s46", "s47", "s48", "s49", "s50", "s51", "s52", "s53", "s54", "s55", "s56", "s57", "s58", "s59", "s60", "s61", "s62", "s63", "s64", "s65", "s66", "s67", "s68", "s69", "s70", "s71", "s72", "s73", "s74", "s75", "s76", "s77", "s78", "s79", "s80", "s81", "s82", "s83", "s84", "s85", "s86", "s87", "s88", "s89", "s90", "s91", "s92", "s93", "s94", "s95", "s96", "s97", "vcc", "scc", "memory");
  unsigned lane;
  asm volatile("v_mbcnt_lo_u32_b32 %0, -1, 0\n\tv_mbcnt_hi_u32_b32 %0, -1, %0" : "=v"(lane));
  if (flags) asm volatile("s_waitcnt vmcnt(0)" ::: "memory");
  while (flags) {
    const unsigned b = __builtin_ctz(flags);
    flags &= flags - 1;
    const unsigned idx = 24u - b, r = idx / 5u, qb = idx % 5u;
    const unsigned qo = unit_qoff(wg, r, w) + qb * 4096u, ko = unit_head(wg, r) * KHEAD;
    exact_rows(Q + qo, K + ko, V + ko, O + qo, lane);
  }
}

extern "C" void kernel_launch(void* const* d_in, const int* in_sizes, int n_in, void* d_out, int out_size, void* d_ws, size_t ws_size, hipStream_t stream) {
  (void)in_sizes; (void)n_in; (void)out_size; (void)d_ws; (void)ws_size;
  (void)hipFuncSetAttribute((const void*)attn_fwd, hipFuncAttributeMaxDynamicSharedMemorySize, 160512);
  hipLaunchKernelGGL(attn_fwd, dim3(256), dim3(256), 160512, stream, (const char*)d_in[0], (const char*)d_in[1], (const char*)d_in[2], (char*)d_out);
}
```
